```python
import torch

def _softmax(x, axis=-1):
    return torch.softmax(x, dim=axis)
def _gelu(x):
    return torch.nn.functional.gelu(x, approximate="tanh")
def _silu(x):
    return torch.nn.functional.silu(x)
def _sigmoid(x):
    return torch.sigmoid(x)
def _layernorm(x, axis=-1, eps=1e-5):
    m = x.mean(dim=axis, keepdim=True)
    v = x.var(dim=axis, keepdim=True, unbiased=False)
    return (x - m) / torch.sqrt(v + eps)
def _scatter_add(tgt, idx, src, axis=0):
    return tgt.clone().index_add_(axis, idx, src)
def _segment_sum(src, seg, n):
    out = torch.zeros((n,) + src.shape[1:], dtype=src.dtype)
    return out.index_add_(0, seg, src)
def _pad(x, pw):
    flat = tuple(v for pair in reversed(pw) for v in pair)
    return torch.nn.functional.pad(x, flat)

kernel_name = 'gated_mix__mha_block_cached__compressed_kv_cla__decode'

def setup_inputs():

    _w = (lambda a: (lambda t: t.to(torch.bfloat16) if t.is_floating_point() else t)(torch.as_tensor(a)))
    def _A_setup():
        B, Sq, D = 2, 1, 512
        H, Sk, Dh = 32, 8192, 128
        return {
            "x": _w((lambda *s: torch.randn(*s, dtype=torch.bfloat16) * 0.05)(B, Sq, D)),
            "k_cache": _w((lambda *s: torch.randn(*s, dtype=torch.bfloat16) * 6.4)(B, H, Sk, Dh)),
            "v_cache": _w((lambda *s: torch.randn(*s, dtype=torch.bfloat16) * 3.2)(B, H, Sk, Dh)),
            "wq": _w((lambda *s: torch.randn(*s, dtype=torch.bfloat16) * 6.4)(D, H * Dh) * (D ** -0.5)),
            "wo": _w((lambda *s: torch.randn(*s, dtype=torch.bfloat16) * 3.2)(H * Dh, D) * ((H * Dh) ** -0.5)),
        }
    _A_in = _A_setup()

    def _B_setup():
        B, H, Sq, Sk, D, M = 2, 32, 1, 8192, 128, 512
        Dc = 4 * D
        return {
            "x": _w((lambda *s: torch.randn(*s, dtype=torch.bfloat16) * 0.05)(B, Sq, M)),
            "c_kv": _w((lambda *s: torch.randn(*s, dtype=torch.bfloat16) * 1.6)(B, Sk, Dc)),
            "w_q": _w((lambda *s: torch.randn(*s, dtype=torch.bfloat16) * 6.4)(M, H, D) * (M ** -0.5)),
            "w_uk": _w((lambda *s: torch.randn(*s, dtype=torch.bfloat16) * 6.4)(Dc, H, D) * (Dc ** -0.5)),
            "w_uv": _w((lambda *s: torch.randn(*s, dtype=torch.bfloat16) * 1.6)(Dc, H, D) * (Dc ** -0.5)),
            "w_o": _w((lambda *s: torch.randn(*s, dtype=torch.bfloat16) * 1.6)(H, D, M) * ((H * D) ** -0.5)),
        }
    _B_in = _B_setup()

    return {
        "A_x": _A_in["x"],
        "A_k_cache": _A_in["k_cache"],
        "A_v_cache": _A_in["v_cache"],
        "A_wq": _A_in["wq"],
        "A_wo": _A_in["wo"],
        "B_x": _B_in["x"],
        "B_c_kv": _B_in["c_kv"],
        "B_w_q": _B_in["w_q"],
        "B_w_uk": _B_in["w_uk"],
        "B_w_uv": _B_in["w_uv"],
        "B_w_o": _B_in["w_o"],
        "gate_w": _w((lambda *s: torch.randn(*s, dtype=torch.bfloat16) * 0.1)(512) * 16.0),
    }

def reference(A_x, A_k_cache, A_v_cache, A_wq, A_wo, B_x, B_c_kv, B_w_q, B_w_uk, B_w_uv, B_w_o, gate_w):
    def _A_ref(x, k_cache, v_cache, wq, wo):
        B, Sq, D = x.shape
        H, Dh = 32, 128
        q = (lambda x, *d: x.permute(*d))((x @ wq).reshape(B, Sq, H, Dh), 0, 2, 1, 3)
        scores = torch.einsum("bhqd,bhkd->bhqk", q, k_cache) * (Dh ** -0.5)
        p = _softmax(scores, axis=-1)
        o = torch.einsum("bhqk,bhkd->bhqd", p, v_cache)
        return (lambda x, *d: x.permute(*d))(o, 0, 2, 1, 3).reshape(B, Sq, H * Dh) @ wo

    def _B_ref(x, c_kv, w_q, w_uk, w_uv, w_o):
        B, Sq, M = x.shape[0], x.shape[1], x.shape[2]
        Sk = c_kv.shape[1]
        H, D = w_q.shape[1], w_q.shape[2]
        q = torch.einsum("bqm,mhd->bhqd", x, w_q)
        k = torch.einsum("bkc,chd->bhkd", c_kv, w_uk)
        v = torch.einsum("bkc,chd->bhkd", c_kv, w_uv)
        scores = torch.einsum("bhqd,bhkd->bhqk", q, k) * (D ** -0.5)
        off = Sk - Sq
        qi = torch.arange(Sq)[:, None]
        kj = torch.arange(Sk)[None, :]
        cmask = kj <= (qi + off)
        scores = torch.where(cmask[None, None], scores, -1e9)
        p = _softmax(scores, axis=-1)
        ctx = torch.einsum("bhqk,bhkd->bhqd", p, v)
        return torch.einsum("bhqd,hdm->bqm", ctx, w_o)

    x = A_x
    g = _sigmoid((x * gate_w).sum(-1, keepdims=True))
    a = _A_ref(x, A_k_cache, A_v_cache, A_wq, A_wo)
    b = _B_ref(x, B_c_kv, B_w_q, B_w_uk, B_w_uv, B_w_o)
    return g * a + (1 - g) * b
```

```cpp
#include <hip/hip_runtime.h>
#include <hip/hip_bf16.h>
#include <stdint.h>

typedef unsigned short u16;
typedef unsigned u32x4 __attribute__((ext_vector_type(4)));
typedef __bf16 bf16x2 __attribute__((ext_vector_type(2)));

constexpr int NB = 2, H = 32, SK = 8192, DH = 128, DM = 512, DC = 512;
constexpr int NSA = 8;
constexpr int NSB = 64;
constexpr float SCALE = 0.08838834764831845f;
constexpr float LOG2E = 1.4426950408889634f;

__device__ __forceinline__ float bf2f(u16 x) { return __uint_as_float(((unsigned)x) << 16); }
__device__ __forceinline__ u16 f2bf(float f) { __hip_bfloat16 b = __float2bfloat16(f); return *(u16*)&b; }
__device__ __forceinline__ float dot2(unsigned k, unsigned q, float acc) {
  return __builtin_amdgcn_fdot2_f32_bf16(__builtin_bit_cast(bf16x2, k), __builtin_bit_cast(bf16x2, q), acc, false);
}

constexpr size_t WS_QA = 0;
constexpr size_t WS_QABS = WS_QA + NB * H * DH;
constexpr size_t WS_PA = WS_QABS + NB * H * DC;
constexpr size_t WS_PB = WS_PA + (size_t)NB * H * NSA * (DH + 2);
constexpr size_t WS_OA = WS_PB + (size_t)NB * H * NSB * (DC + 2);
constexpr size_t WS_OB = WS_OA + NB * H * DH;

__global__ void __launch_bounds__(256) k_proj(const u16* x, const u16* wq, const u16* w_q, const u16* w_uk, float* ws) {
  __shared__ float xs[NB][DM];
  __shared__ float qs[NB][DH];
  int h = blockIdx.x & 31, type = blockIdx.x >> 5;
  int t = threadIdx.x;
  for (int i = t; i < NB * DM; i += 256) xs[i / DM][i % DM] = bf2f(x[i]);
  __syncthreads();
  const u16* w = type ? w_q : wq;
  int b = t >> 7, d = t & 127;
  float acc = 0.f;
  for (int m = 0; m < DM; ++m) acc += xs[b][m] * bf2f(w[(size_t)m * 4096 + h * 128 + d]);
  float qr = bf2f(f2bf(acc));
  if (type == 0) { ws[WS_QA + (b * H + h) * DH + d] = qr; return; }
  qs[b][d] = qr;
  __syncthreads();
  for (int i = t; i < NB * DC; i += 256) {
    int bb = i / DC, c = i % DC;
    float a = 0.f;
    for (int dd = 0; dd < DH; ++dd) a += qs[bb][dd] * bf2f(w_uk[(size_t)c * 4096 + h * 128 + dd]);
    ws[WS_QABS + (bb * H + h) * DC + c] = a;
  }
}

__global__ void __launch_bounds__(256) k_attnA(const u16* K, const u16* V, float* ws) {
  __shared__ float sc[SK / NSA];
  __shared__ float red[4][DH];
  __shared__ float rm[4];
  int s = blockIdx.x, bh = blockIdx.y;
  int t = threadIdx.x, lane = t & 63, w = t >> 6;
  constexpr int NK = SK / NSA;
  const u16* kb = K + ((size_t)bh * SK + s * NK) * DH;
  const u16* vb = V + ((size_t)bh * SK + s * NK) * DH;
  int c = lane & 15, g = lane >> 4;
  unsigned q[4];
  for (int j = 0; j < 4; ++j) {
    float a = ws[WS_QA + bh * DH + c * 8 + 2 * j], b = ws[WS_QA + bh * DH + c * 8 + 2 * j + 1];
    q[j] = (__float_as_uint(a) >> 16) | (__float_as_uint(b) & 0xffff0000u);
  }
  float mx = -INFINITY;
  for (int k = w * 4 + g; k < NK; k += 16) {
    u32x4 kv = *(const u32x4*)(kb + (size_t)k * DH + c * 8);
    float d = 0.f;
    for (int j = 0; j < 4; ++j) d = dot2(kv[j], q[j], d);
    for (int o = 1; o < 16; o <<= 1) d += __shfl_xor(d, o);
    d *= SCALE * LOG2E;
    if (c == 0) sc[k] = d;
    mx = fmaxf(mx, d);
  }
  for (int o = 16; o < 64; o <<= 1) mx = fmaxf(mx, __shfl_xor(mx, o));
  if (lane == 0) rm[w] = mx;
  __syncthreads();
  mx = fmaxf(fmaxf(rm[0], rm[1]), fmaxf(rm[2], rm[3]));
  float acc[8] = {0, 0, 0, 0, 0, 0, 0, 0};
  float l = 0.f;
  for (int k = w * 4 + g; k < NK; k += 16) {
    u32x4 vv = *(const u32x4*)(vb + (size_t)k * DH + c * 8);
    float p = exp2f(sc[k] - mx);
    l += p;
    for (int j = 0; j < 4; ++j) {
      acc[2 * j] += p * __uint_as_float(vv[j] << 16);
      acc[2 * j + 1] += p * __uint_as_float(vv[j] & 0xffff0000u);
    }
  }
  for (int o = 16; o < 64; o <<= 1) {
    l += __shfl_xor(l, o);
    for (int j = 0; j < 8; ++j) acc[j] += __shfl_xor(acc[j], o);
  }
  __syncthreads();
  if (g == 0) for (int j = 0; j < 8; ++j) red[w][c * 8 + j] = acc[j];
  if (lane == 0) rm[w] = l;
  __syncthreads();
  float* po = ws + WS_PA + ((size_t)bh * NSA + s) * (DH + 2);
  if (t < DH) po[t] = red[0][t] + red[1][t] + red[2][t] + red[3][t];
  if (t == 0) { po[DH] = mx; po[DH + 1] = rm[0] + rm[1] + rm[2] + rm[3]; }
}

__global__ void __launch_bounds__(256) k_attnB(const u16* C, float* ws) {
  constexpr int NK = SK / NSB;
  __shared__ float sc[H][NK];
  __shared__ float mh[H], lh[H];
  int s = blockIdx.x, b = blockIdx.y, t = threadIdx.x;
  const u16* cb = C + ((size_t)b * SK + s * NK) * DC;
  const float* qa = ws + WS_QABS + (size_t)b * H * DC;
  for (int i = t; i < H * NK; i += 256) {
    int h = i / NK, k = i % NK;
    float a = 0.f;
    for (int c = 0; c < DC; c += 8) {
      u32x4 cv = *(const u32x4*)(cb + (size_t)k * DC + c);
      for (int j = 0; j < 4; ++j) {
        a += qa[h * DC + c + 2 * j] * __uint_as_float(cv[j] << 16);
        a += qa[h * DC + c + 2 * j + 1] * __uint_as_float(cv[j] & 0xffff0000u);
      }
    }
    sc[h][k] = a * SCALE * LOG2E;
  }
  __syncthreads();
  if (t < H) {
    float m = -INFINITY;
    for (int k = 0; k < NK; ++k) m = fmaxf(m, sc[t][k]);
    float l = 0.f;
    for (int k = 0; k < NK; ++k) { float p = exp2f(sc[t][k] - m); sc[t][k] = p; l += p; }
    mh[t] = m; lh[t] = l;
  }
  __syncthreads();
  float acc[H][2];
  for (int h = 0; h < H; ++h) acc[h][0] = acc[h][1] = 0.f;
  for (int k = 0; k < NK; ++k) {
    unsigned cv = *(const unsigned*)(cb + (size_t)k * DC + 2 * t);
    float c0 = __uint_as_float(cv << 16), c1 = __uint_as_float(cv & 0xffff0000u);
#pragma unroll
    for (int h = 0; h < H; ++h) { float p = sc[h][k]; acc[h][0] += p * c0; acc[h][1] += p * c1; }
  }
#pragma unroll
  for (int h = 0; h < H; ++h) {
    float* po = ws + WS_PB + ((size_t)(b * H + h) * NSB + s) * (DC + 2);
    po[2 * t] = acc[h][0]; po[2 * t + 1] = acc[h][1];
    if (t == 0) { po[DC] = mh[h]; po[DC + 1] = lh[h]; }
  }
}

__global__ void __launch_bounds__(256) k_comb(const u16* w_uv, float* ws) {
  __shared__ float cc[DC];
  __shared__ float r2[2][DH];
  int bh = blockIdx.x, b = bh >> 5, h = bh & 31, t = threadIdx.x;
  if (t < DH) {
    const float* pa = ws + WS_PA + (size_t)bh * NSA * (DH + 2);
    float M = -INFINITY;
    for (int s = 0; s < NSA; ++s) M = fmaxf(M, pa[s * (DH + 2) + DH]);
    float L = 0.f, o = 0.f;
    for (int s = 0; s < NSA; ++s) { float w = exp2f(pa[s * (DH + 2) + DH] - M); L += w * pa[s * (DH + 2) + DH + 1]; o += w * pa[s * (DH + 2) + t]; }
    ws[WS_OA + b * 4096 + h * 128 + t] = o / L;
  }
  {
    const float* pb = ws + WS_PB + (size_t)bh * NSB * (DC + 2);
    float M = -INFINITY;
    for (int s = 0; s < NSB; ++s) M = fmaxf(M, pb[s * (DC + 2) + DC]);
    float L = 0.f, o0 = 0.f, o1 = 0.f;
    for (int s = 0; s < NSB; ++s) { float w = exp2f(pb[s * (DC + 2) + DC] - M); L += w * pb[s * (DC + 2) + DC + 1]; o0 += w * pb[s * (DC + 2) + t]; o1 += w * pb[s * (DC + 2) + 256 + t]; }
    cc[t] = o0 / L; cc[t + 256] = o1 / L;
  }
  __syncthreads();
  int d = t & 127, half = t >> 7;
  float a = 0.f;
  for (int c = half * 256; c < half * 256 + 256; ++c) a += cc[c] * bf2f(w_uv[(size_t)c * 4096 + h * 128 + d]);
  r2[half][d] = a;
  __syncthreads();
  if (t < DH) ws[WS_OB + b * 4096 + h * 128 + t] = r2[0][t] + r2[1][t];
}

__global__ void __launch_bounds__(256) k_out(const u16* x, const u16* gw, const u16* wo, const u16* w_o, const float* ws, u16* out) {
  __shared__ float red[256][17];
  __shared__ float gs[2];
  int t = threadIdx.x, n0 = blockIdx.x * 8;
  if (t < 128) {
    int b = t >> 6, lane = t & 63;
    float s = 0.f;
    for (int i = lane; i < DM; i += 64) s += bf2f(f2bf(bf2f(x[b * DM + i]) * bf2f(gw[i])));
    for (int o = 1; o < 64; o <<= 1) s += __shfl_xor(s, o);
    if (lane == 0) { float z = bf2f(f2bf(s)); gs[b] = bf2f(f2bf(1.f / (1.f + __expf(-z)))); }
  }
  __syncthreads();
  float acc[2][8] = {};
  float g0 = gs[0], g1 = gs[1];
  for (int k = t; k < 4096; k += 256) {
    u32x4 a = *(const u32x4*)(wo + (size_t)k * DM + n0);
    u32x4 bb = *(const u32x4*)(w_o + (size_t)k * DM + n0);
    float oa0 = ws[WS_OA + k] * g0, oa1 = ws[WS_OA + 4096 + k] * g1;
    float ob0 = ws[WS_OB + k] * (1.f - g0), ob1 = ws[WS_OB + 4096 + k] * (1.f - g1);
    for (int j = 0; j < 4; ++j) {
      float a0 = __uint_as_float(a[j] << 16), a1 = __uint_as_float(a[j] & 0xffff0000u);
      float b0 = __uint_as_float(bb[j] << 16), b1 = __uint_as_float(bb[j] & 0xffff0000u);
      acc[0][2 * j] += oa0 * a0 + ob0 * b0; acc[0][2 * j + 1] += oa0 * a1 + ob0 * b1;
      acc[1][2 * j] += oa1 * a0 + ob1 * b0; acc[1][2 * j + 1] += oa1 * a1 + ob1 * b1;
    }
  }
  for (int j = 0; j < 16; ++j) red[t][j] = acc[j >> 3][j & 7];
  __syncthreads();
  if (t < 16) {
    float s = 0.f;
    for (int i = 0; i < 256; ++i) s += red[i][t];
    out[(t >> 3) * DM + n0 + (t & 7)] = f2bf(s);
  }
}

extern "C" void kernel_launch(void* const* d_in, const int* in_sizes, int n_in, void* d_out, int out_size, void* d_ws, size_t ws_size, hipStream_t stream) {
  const u16* x = (const u16*)d_in[0];
  const u16* K = (const u16*)d_in[1];
  const u16* V = (const u16*)d_in[2];
  const u16* wq = (const u16*)d_in[3];
  const u16* wo = (const u16*)d_in[4];
  const u16* C = (const u16*)d_in[6];
  const u16* w_q = (const u16*)d_in[7];
  const u16* w_uk = (const u16*)d_in[8];
  const u16* w_uv = (const u16*)d_in[9];
  const u16* w_o = (const u16*)d_in[10];
  const u16* gw = (const u16*)d_in[11];
  float* ws = (float*)d_ws;
  k_proj<<<64, 256, 0, stream>>>(x, wq, w_q, w_uk, ws);
  k_attnA<<<dim3(NSA, NB * H), 256, 0, stream>>>(K, V, ws);
  k_attnB<<<dim3(NSB, NB), 256, 0, stream>>>(C, ws);
  k_comb<<<NB * H, 256, 0, stream>>>(w_uv, ws);
  k_out<<<64, 256, 0, stream>>>(x, gw, wo, w_o, ws, (u16*)d_out);
}
```

```cpp
#include <hip/hip_runtime.h>
#include <stdint.h>

typedef unsigned short u16;
typedef unsigned long long u64;
typedef unsigned u32x4 __attribute__((ext_vector_type(4)));
typedef unsigned u32x2 __attribute__((ext_vector_type(2)));
typedef float f32x2 __attribute__((ext_vector_type(2)));
typedef float f32x4 __attribute__((ext_vector_type(4)));
typedef float f32x16 __attribute__((ext_vector_type(16)));
typedef __bf16 bf16x8 __attribute__((ext_vector_type(8)));
typedef __bf16 bf16x2 __attribute__((ext_vector_type(2)));
typedef short s16x4 __attribute__((ext_vector_type(4)));
typedef __amdgpu_buffer_rsrc_t rsrc_t;
#define GAS __attribute__((address_space(1)))
#define LAS __attribute__((address_space(3)))
#define DI __device__ __forceinline__
#define RLX_AGENT __ATOMIC_RELAXED, __HIP_MEMORY_SCOPE_AGENT

#define NL 4
#define W_AUX 2
#define DYN 1
#define ORD 1
#define NO_WQ 0
#define NO_WO 0
#define NO_COMP 0
#define NO_AUX 0
#define AUXOFF 0
#define STAMPS 0
constexpr int AUX_NT = 2, AUX_SC1 = 16;
constexpr float SCALE = 0.08838834764831845f;
constexpr float LOG2E = 1.4426950408889634f;

constexpr int WS_EPOCH = 0;
constexpr int WS_F1 = 1024;
constexpr int WS_F2 = 2048;
constexpr int WS_F3 = 4096;
constexpr int WS_QF = 8192;
constexpr int WS_ML = WS_QF + 65536;
constexpr int WS_PB = WS_ML + 16384;
constexpr int WS_Y = WS_PB + 2 * 32 * 32 * 512 * 2;
constexpr int WS_YML = WS_Y + 2 * 128 * 512 * 4;
constexpr int WS_END = WS_YML + 2 * 128 * 8;
constexpr int WS_STAMP = 4 << 20;

constexpr int L_RING = 0;
constexpr int L_RED = 131072;
constexpr int L_P = L_RED + 8192;
constexpr int L_MX = L_P + 32 * 144;
constexpr int L_ML = L_MX + 512;
constexpr int L_Y = L_ML + 1024;
constexpr int L_CNT = 8192;
constexpr int L_END = L_Y + 4096;

DI rsrc_t mk_rsrc(const void* p, unsigned bytes) { return __builtin_amdgcn_make_buffer_rsrc((void*)p, 0, bytes, 0x00020000); }
template <int AUX> DI u32x4 ld128(rsrc_t r, int voff, int soff) { return __builtin_amdgcn_raw_buffer_load_b128(r, voff, soff, AUX); }
DI float u2f(unsigned u) { return __builtin_bit_cast(float, u); }
DI unsigned f2u(float f) { return __builtin_bit_cast(unsigned, f); }
DI unsigned pk_bf16(float lo, float hi) { bf16x2 r; r[0] = (__bf16)lo; r[1] = (__bf16)hi; return __builtin_bit_cast(unsigned, r); }
DI float bfr(float x) { return u2f(pk_bf16(x, x) << 16); }
DI unsigned hilo(float x) { const float h = bfr(x); return pk_bf16(h, x - h); }
DI float dot2(unsigned a, unsigned b, float c) { return __builtin_amdgcn_fdot2_f32_bf16(__builtin_bit_cast(bf16x2, a), __builtin_bit_cast(bf16x2, b), c, false); }
template <int CTRL> DI float dppf(float x) { return u2f(__builtin_amdgcn_mov_dpp(f2u(x), CTRL, 0xf, 0xf, true)); }
template <int T> DI unsigned bcast16(unsigned v) { return __builtin_amdgcn_mov_dpp(v, 0x150 + T, 0xf, 0xf, true); }
constexpr int XOR1 = 0xB1, XOR2 = 0x4E, XOR7 = 0x141, ROR8 = 0x128, ROR4 = 0x124;
DI float xrow_max(float x) {
  auto s = __builtin_amdgcn_permlane16_swap(f2u(x), f2u(x), false, false); x = fmaxf(u2f(s[0]), u2f(s[1]));
  auto t = __builtin_amdgcn_permlane32_swap(f2u(x), f2u(x), false, false); return fmaxf(u2f(t[0]), u2f(t[1]));
}
DI float xrow_sum(float x) {
  auto s = __builtin_amdgcn_permlane16_swap(f2u(x), f2u(x), false, false); x = u2f(s[0]) + u2f(s[1]);
  auto t = __builtin_amdgcn_permlane32_swap(f2u(x), f2u(x), false, false); return u2f(t[0]) + u2f(t[1]);
}
DI float row_max(float x) { x = fmaxf(x, dppf<ROR8>(x)); x = fmaxf(x, dppf<ROR4>(x)); x = fmaxf(x, dppf<XOR2>(x)); return fmaxf(x, dppf<XOR1>(x)); }
DI float row_sum(float x) { x += dppf<ROR8>(x); x += dppf<ROR4>(x); x += dppf<XOR2>(x); return x + dppf<XOR1>(x); }
DI f32x4 mfma16(u32x4 a, u32x4 b, f32x4 c) { return __builtin_amdgcn_mfma_f32_16x16x32_bf16(__builtin_bit_cast(bf16x8, a), __builtin_bit_cast(bf16x8, b), c, 0, 0, 0); }
DI f32x16 mfma32(u32x4 a, u32x4 b, f32x16 c) { return __builtin_amdgcn_mfma_f32_32x32x16_bf16(__builtin_bit_cast(bf16x8, a), __builtin_bit_cast(bf16x8, b), c, 0, 0, 0); }
DI u64 uniform64(u64 v) { return ((u64)(unsigned)__builtin_amdgcn_readfirstlane((unsigned)(v >> 32)) << 32) | (unsigned)__builtin_amdgcn_readfirstlane((unsigned)v); }
DI void bar() { __syncthreads(); }
#define DRAIN() asm volatile("s_waitcnt vmcnt(0)" ::: "memory")
#define PIN() asm volatile("" ::: "memory")
#define SB() __builtin_amdgcn_sched_barrier(0)

template <int N, int STEP, int T = 0> DI void rowsum(const u32x4 (&W)[N], unsigned pair, const u32x4& sel, f32x4& D) {
  if constexpr (T < N) {
    const unsigned s = bcast16<T * STEP>(pair);
    const u32x4 B = {__builtin_amdgcn_perm(s, s, sel[0]), __builtin_amdgcn_perm(s, s, sel[1]), __builtin_amdgcn_perm(s, s, sel[2]), __builtin_amdgcn_perm(s, s, sel[3])};
    D = mfma16(W[T], B, D);
    rowsum<N, STEP, T + 1>(W, pair, sel, D);
  }
}
DI u32x4 make_sel(int c) {
  const int j = c & 7; const unsigned b = (c & 8) ? 0x0302u : 0x0100u;
  const unsigned v = (j & 1) ? ((b << 16) | 0x0c0cu) : (0x0c0c0000u | b);
  u32x4 s;
#pragma unroll
  for (int w = 0; w < 4; ++w) s[w] = (w == (j >> 1)) ? v : 0x0c0c0c0cu;
  return s;
}
template <int N> DI float treduce(float (&x)[N], int c) {
#define TR_STEP(CNT, CTRL, BIT) _Pragma("unroll") for (int i = 0; i < (CNT); ++i) { const float a = x[i] + dppf<CTRL>(x[i]), b = x[i + (CNT)] + dppf<CTRL>(x[i + (CNT)]); x[i] = (c & (BIT)) ? b : a; }
  TR_STEP(N / 2, ROR8, 8)
  TR_STEP(N / 4, XOR7, 4)
  if constexpr (N >= 8) { TR_STEP(N / 8, XOR2, 2) } else { x[0] += dppf<XOR2>(x[0]); }
  if constexpr (N >= 16) { TR_STEP(N / 16, XOR1, 1) } else { x[0] += dppf<XOR1>(x[0]); }
#undef TR_STEP
  return x[0];
}
template <bool TWO> DI void wait_flags(const GAS u64* f, int n, u64 tag0, u64 tag1, int lane) {
  for (unsigned spins = 0; spins < (1u << 22); ++spins) {
    bool ok = true;
    if (lane < n) ok = __hip_atomic_load(f + lane, RLX_AGENT) == tag0;
    if (TWO) ok = ok && (__hip_atomic_load(f + 64 + lane, RLX_AGENT) == tag1);
    if (__all(ok)) break;
    __builtin_amdgcn_s_sleep(2);
  }
  __builtin_amdgcn_fence(__ATOMIC_ACQUIRE, "wavefront");
}
#define STAMP(k) do {} while (0)

DI void out_proj(const u32x4 (&W)[16], float val, const u32x4& sel, char* lds, rsrc_t rws, int yoff, int tid) {
  const int lane = tid & 63, w = tid >> 6, g = lane >> 4, c = lane & 15;
  f32x4 D = {0.f, 0.f, 0.f, 0.f};
  rowsum<16, 1>(W, hilo(val), sel, D);
  float* y = (float*)(lds + L_Y) + (w >> 2) * 512 + (w & 3) * 128 + 32 * g + c;
#pragma unroll
  for (int r = 0; r < 4; ++r) { const float s = D[r] + dppf<ROR8>(D[r]); if (c < 8) y[8 * r] = s; }
  bar();
  if (tid < 128) {
    const f32x4 a = *(const f32x4*)(lds + L_Y + tid * 16), b = *(const f32x4*)(lds + L_Y + 2048 + tid * 16);
    __builtin_amdgcn_raw_buffer_store_b128(__builtin_bit_cast(u32x4, a + b), rws, yoff + tid * 16, 0, AUX_SC1);
  }
}

__global__ void __launch_bounds__(512, 2)
fused_decode(const u16* __restrict__ x, const u16* __restrict__ Kc, const u16* __restrict__ Vc, const u16* __restrict__ wq, const u16* __restrict__ wo,
             const u16* __restrict__ Cc, const u16* __restrict__ w_q, const u16* __restrict__ w_uk, const u16* __restrict__ w_uv, const u16* __restrict__ w_o,
             const u16* __restrict__ gw, u16* __restrict__ out, char* ws) {
  __shared__ __attribute__((aligned(16))) char lds[L_END];
  const int bid = blockIdx.x, tid = threadIdx.x, lane = tid & 63, w = __builtin_amdgcn_readfirstlane(tid >> 6), g = lane >> 4, c = lane & 15;
  const int x8 = bid & 7, idx = bid >> 3;
  const bool is_stream = idx < 24;
  const int h = __builtin_amdgcn_readfirstlane(is_stream ? 4 * x8 + idx / 6 : 4 * x8 + ((idx - 24) >> 1));
  const int b = __builtin_amdgcn_readfirstlane(is_stream ? (idx % 6) / 3 : (idx & 1));
  const int sp = __builtin_amdgcn_readfirstlane(idx % 3);
  STAMP(0);
  const rsrc_t rws = mk_rsrc(ws, WS_END);
#define LD_SMALL() u64 e0 = __hip_atomic_load((GAS u64*)(ws + WS_EPOCH), RLX_AGENT), e1 = __hip_atomic_load((GAS u64*)(ws + WS_EPOCH + 128), RLX_AGENT); \
  unsigned x0 = x[tid], x1 = x[512 + tid];
#define USE_SMALL() asm volatile("" : "+v"(e0), "+v"(e1), "+v"(x0), "+v"(x1)); \
  const u64 tag0 = uniform64(e0) + 1ull, tag1 = uniform64(e1) + 1ull; const unsigned xpair = x0 | (x1 << 16);
  const u32x4 sel = make_sel(c);
  float* red = (float*)(lds + L_RED);

#define LD_ROWS(W, ptr, OFF) { const rsrc_t rq = mk_rsrc(ptr, (OFF) ? 0 : 512 * 4096 * 2); \
    _Pragma("unroll") for (int t = 0; t < 16; ++t) W[t] = ld128<W_AUX>(rq, g * (16 * 8192) + c * 16, (64 * w + t) * 8192 + h * 256); }
#define LD_OUTW(W, ptr, OFF) { const rsrc_t ro = mk_rsrc(ptr, (OFF) ? 0 : 4096 * 512 * 2); \
    _Pragma("unroll") for (int i = 0; i < 16; ++i) W[i] = ld128<W_AUX>(ro, g * (16 * 1024) + c * 16, (h * 128 + 64 * (w >> 2) + i) * 1024 + (w & 3) * 256); }

  if (is_stream) {
    constexpr int NT = 2048 / NL;
    u32x4 W[16];
    LD_SMALL();
    LD_ROWS(W, wq, NO_WQ);
    SB(); __builtin_amdgcn_s_barrier(); SB();
    const rsrc_t rk = mk_rsrc(Kc, 128u << 20), rv = mk_rsrc(Vc, 128u << 20);
    const int t1 = (b * 32 + h) * NT + NT * (sp + 1) / 3;
    int t = (b * 32 + h) * NT + NT * sp / 3 + w;
    u32x4 kb[NL], vb[NL];
#define LDT(r, tt, i) ld128<AUX_NT>(r, lane * 16 + ((i) & 3) * 1024, (tt) * (NL * 1024) + ((i) >> 2) * 4096)
#pragma unroll
    for (int i = 0; i < NL; ++i) kb[i] = LDT(rk, t, i);
#pragma unroll
    for (int i = 0; i < NL; ++i) vb[i] = LDT(rv, t, i);
    SB();
    USE_SMALL();
    {
      f32x4 D = {0.f, 0.f, 0.f, 0.f};
      rowsum<16, 1>(W, xpair, sel, D);
#pragma unroll
      for (int r = 0; r < 4; ++r) red[(w * 2 + (c >> 3)) * 128 + 32 * g + 8 * r + (c & 7)] = D[r];
    }
    if (tid == 0) *(int*)(lds + L_CNT) = t + 16;
    bar();
    u32x4 q;
    {
      f32x4 a0 = {0.f, 0.f, 0.f, 0.f}, a1 = a0;
#pragma unroll
      for (int ww = 0; ww < 8; ++ww) { const f32x4* p = (const f32x4*)(red + (ww * 2 + b) * 128 + 8 * c); a0 += p[0]; a1 += p[1]; }
      q = u32x4{pk_bf16(a0[0], a0[1]), pk_bf16(a0[2], a0[3]), pk_bf16(a1[0], a1[1]), pk_bf16(a1[2], a1[3])};
    }
    STAMP(1);
    float m = -INFINITY, l = 0.f;
    f32x4 O = {0.f, 0.f, 0.f, 0.f};
#define QK_PART() float xs[NL]; _Pragma("unroll") for (int i = 0; i < NL; ++i) xs[i] = dot2(kb[i][3], q[3], dot2(kb[i][2], q[2], dot2(kb[i][1], q[1], dot2(kb[i][0], q[0], 0.f)))); \
    _Pragma("unroll") for (int i = 0; i < NL; ++i) asm volatile("" : "+v"(xs[i]));
#define PV_PART() { const float s = bfr(bfr(treduce<NL>(xs, c)) * SCALE); \
      const float mn = fmaxf(m, xrow_max(row_max(s))), ms = (mn == -INFINITY) ? 0.f : mn; \
      const float al = __builtin_amdgcn_exp2f((m - ms) * LOG2E), p = __builtin_amdgcn_exp2f((s - ms) * LOG2E); \
      m = mn; l = l * al + p; O *= al; rowsum<NL, 16 / NL>(vb, hilo(p), sel, O); asm volatile("" : "+v"(O), "+v"(l)); }
#define NEXT_TILE() { int tn = 0; if (lane == 0) tn = __hip_atomic_fetch_add((LAS int*)(lds + L_CNT), 1, __ATOMIC_RELAXED, __HIP_MEMORY_SCOPE_WORKGROUP); t = __builtin_amdgcn_readfirstlane(tn); }
#define TILE(NEXT) QK_PART(); SB(); NEXT; more = t < t1; \
      const rsrc_t rk2 = mk_rsrc(Kc, more ? (128u << 20) : 0u), rv2 = mk_rsrc(Vc, more ? (128u << 20) : 0u); t = more ? t : 0; \
      _Pragma("unroll") for (int i = 0; i < NL; ++i) kb[i] = LDT(rk2, t, i); \
      SB(); PV_PART(); SB(); \
      _Pragma("unroll") for (int i = 0; i < NL; ++i) vb[i] = LDT(rv2, t, i); \
      SB();
    bool more;
    { TILE(t += 8); }
    LD_OUTW(W, wo, NO_WO);
    PIN(); SB();
    do { TILE(NEXT_TILE()); } while (more);
    STAMP(2);
    l = xrow_sum(row_sum(l)) * (NL / 16.f);
    float* ml = (float*)(lds + L_ML);
    float* oo = (float*)(lds + L_RING);
#pragma unroll
    for (int r = 0; r < 4; ++r) { const float s = O[r] + dppf<ROR8>(O[r]); if (c < 8) oo[w * 128 + 32 * g + 8 * r + c] = s; }
    if (lane == 0) { ml[w] = m; ml[8 + w] = l; }
    bar();
    float M = -INFINITY;
#pragma unroll
    for (int ww = 0; ww < 8; ++ww) M = fmaxf(M, ml[ww]);
    const float Ms = (M == -INFINITY) ? 0.f : M;
    float L = 0.f, val = 0.f;
#pragma unroll
    for (int ww = 0; ww < 8; ++ww) { const float f = __builtin_amdgcn_exp2f((ml[ww] - Ms) * LOG2E); L += f * ml[8 + ww]; val += f * oo[ww * 128 + 64 * (w >> 2) + lane]; }
    const int src = b * 128 + h * 4 + sp;
    out_proj(W, val, sel, lds, rws, WS_Y + src * 2048, tid);
    if (tid == 0) __hip_atomic_store((GAS u64*)(ws + WS_YML) + src, ((u64)f2u(L) << 32) | f2u(M), RLX_AGENT);
    if (tid < 128) DRAIN();
    bar();
    if (tid == 0) __hip_atomic_store((GAS u64*)(ws + WS_F3) + src, b ? tag1 : tag0, RLX_AGENT);
    STAMP(3);
    return;
  }

  const rsrc_t rc = mk_rsrc(Cc, (AUXOFF & 4) ? 0 : (16u << 20));
  const rsrc_t rpb = mk_rsrc(ws, (AUXOFF & 32) ? 0 : WS_END);
  u32x4 W[16], U[8], S0[8], S1[8];
  LD_SMALL();
  LD_ROWS(W, w_q, AUXOFF & 1);
  SB(); __builtin_amdgcn_s_barrier(); SB();
  {
    const rsrc_t ru = mk_rsrc(w_uk, (AUXOFF & 2) ? 0 : 512 * 4096 * 2);
#pragma unroll
    for (int t = 0; t < 8; ++t) U[t] = ld128<AUX_NT>(ru, g * (8 * 8192) + c * 16, (256 * b + 32 * w + t) * 8192 + h * 256);
  }
  SB(); __builtin_amdgcn_s_barrier(); SB();
#define LDC(st, i) ld128<AUX_NT>(rc, lane * 16 + ((i) & 3) * 1024, (b * 8192 + h * 256 + (st) * 64 + 8 * w + ((i) & 4)) * 1024)
#pragma unroll
  for (int i = 0; i < 8; ++i) S0[i] = LDC(0, i);
#pragma unroll
  for (int i = 0; i < 8; ++i) S1[i] = LDC(1, i);
  SB();
  USE_SMALL();
  {
    f32x4 D = {0.f, 0.f, 0.f, 0.f};
    rowsum<16, 1>(W, xpair, sel, D);
#pragma unroll
    for (int r = 0; r < 4; ++r) red[(w * 2 + (c >> 3)) * 128 + 32 * g + 8 * r + (c & 7)] = D[r];
  }
  bar();
  {
    float xs[16];
#pragma unroll
    for (int bb = 0; bb < 2; ++bb) {
      f32x4 a0 = {0.f, 0.f, 0.f, 0.f}, a1 = a0;
#pragma unroll
      for (int ww = 0; ww < 8; ++ww) { const f32x4* p = (const f32x4*)(red + (ww * 2 + bb) * 128 + 8 * c); a0 += p[0]; a1 += p[1]; }
      const u32x4 q = {pk_bf16(a0[0], a0[1]), pk_bf16(a0[2], a0[3]), pk_bf16(a1[0], a1[1]), pk_bf16(a1[2], a1[3])};
#pragma unroll
      for (int t = 0; t < 8; ++t) xs[bb * 8 + t] = dot2(U[t][3], q[3], dot2(U[t][2], q[2], dot2(U[t][1], q[1], dot2(U[t][0], q[0], 0.f))));
    }
    const float qa = treduce<16>(xs, c);
    const int off = WS_QF + (((((c >> 3) * 2 + (h >> 4)) * 16 + 8 * b + w) * 64 + g * 16 + (h & 15)) * 8 + (c & 7)) * 2;
    __builtin_amdgcn_raw_buffer_store_b16((u16)(pk_bf16(qa, qa) & 0xffffu), rws, off, 0, AUX_SC1);
  }
  const int wr_off = g * 16384 + 8 * w * 256;
#define STC(slot, S) _Pragma("unroll") for (int i = 0; i < 8; ++i) *(u32x4*)(lds + L_RING + (slot) * 65536 + wr_off + i * 256 + 16 * (c ^ (((i & 3) << 2) | (((8 * w + i) >> 2) & 3)))) = S[i];
  STC(0, S0);
  STC(1, S1);
  DRAIN();
  bar();
  if (tid == 0) __hip_atomic_store((GAS u64*)(ws + WS_F1) + h * 2 + b, tag0, RLX_AGENT);
  STAMP(1);
#pragma unroll
  for (int i = 0; i < 8; ++i) S0[i] = LDC(2, i);
#pragma unroll
  for (int i = 0; i < 8; ++i) S1[i] = LDC(3, i);
  PIN();
  if (w == 0) wait_flags<false>((const GAS u64*)(ws + WS_F1), 64, tag0, tag0, lane);
  bar();
  STAMP(2);
#pragma unroll
  for (int s = 0; s < 16; ++s) W[s] = ld128<AUX_SC1>(rws, lane * 16, WS_QF + ((b * 2 + (w >> 2)) * 16 + s) * 1024);
  {
    const int kt = w & 3, hh = w >> 2;
    float mq = -INFINITY, lq = 0.f, mp = -INFINITY;
    f32x16 A0, A1;
#pragma unroll
    for (int i = 0; i < 16; ++i) { A0[i] = 0.f; A1[i] = 0.f; }
    float* mx = (float*)(lds + L_MX);
    const int row = 16 * kt + c, swz = ((row & 3) << 2) | ((row >> 2) & 3);
    const int h5 = lane >> 5, blk = (lane >> 4) & 1, q4 = (lane & 15) >> 2, p4 = lane & 3;
#pragma unroll
    for (int st = 0; st < 4; ++st) {
      const char* tile = lds + L_RING + (st & 1) * 65536;
      f32x4 X = {0.f, 0.f, 0.f, 0.f};
#pragma unroll
      for (int s = 0; s < 16; ++s) X = mfma16(*(const u32x4*)(tile + (s >> 2) * 16384 + row * 256 + 16 * ((4 * (s & 3) + g) ^ swz)), W[s], X);
      float sc[4];
#pragma unroll
      for (int r = 0; r < 4; ++r) sc[r] = bfr(bfr(X[r]) * SCALE);
      const float tm = xrow_max(fmaxf(fmaxf(sc[0], sc[1]), fmaxf(sc[2], sc[3])));
      if (g == 0) mx[kt * 32 + 16 * hh + c] = tm;
      bar();
      {
        const int hq = 16 * hh + c;
        const float mn = fmaxf(fmaxf(mq, fmaxf(mx[hq], mx[32 + hq])), fmaxf(mx[64 + hq], mx[96 + hq])), ms = (mn == -INFINITY) ? 0.f : mn;
        float p[4];
#pragma unroll
        for (int r = 0; r < 4; ++r) p[r] = __builtin_amdgcn_exp2f((sc[r] - ms) * LOG2E);
        lq = lq * __builtin_amdgcn_exp2f((mq - ms) * LOG2E) + ((p[0] + p[1]) + (p[2] + p[3]));
        mq = mn;
        *(u32x2*)(lds + L_P + hq * 144 + (16 * kt + 4 * g) * 2) = u32x2{pk_bf16(p[0], p[1]), pk_bf16(p[2], p[3])};
      }
      {
        const int hp = lane & 31;
        const float mn = fmaxf(fmaxf(mp, fmaxf(mx[hp], mx[32 + hp])), fmaxf(mx[64 + hp], mx[96 + hp])), ms = (mn == -INFINITY) ? 0.f : mn;
        const float al = __builtin_amdgcn_exp2f((mp - ms) * LOG2E);
        mp = mn;
        A0 *= al; A1 *= al;
      }
      bar();
#pragma unroll
      for (int ks = 0; ks < 4; ++ks) {
        const u32x4 P = *(const u32x4*)(lds + L_P + (lane & 31) * 144 + (16 * ks + 8 * h5) * 2);
        s16x4 f[2][2];
#pragma unroll
        for (int ct = 0; ct < 2; ++ct)
#pragma unroll
          for (int tt = 0; tt < 2; ++tt) {
            const int r2 = 16 * ks + 8 * h5 + 4 * tt + q4, ch = 4 * (2 * (w & 1) + ct) + 2 * blk + (p4 >> 1);
            f[ct][tt] = __builtin_amdgcn_ds_read_tr16_b64_v4i16((LAS s16x4*)(tile + (w >> 1) * 16384 + r2 * 256 + 16 * (ch ^ (((r2 & 3) << 2) | ((r2 >> 2) & 3))) + 8 * (p4 & 1)));
          }
        A0 = mfma32(__builtin_bit_cast(u32x4, __builtin_shufflevector(f[0][0], f[0][1], 0, 1, 2, 3, 4, 5, 6, 7)), P, A0);
        A1 = mfma32(__builtin_bit_cast(u32x4, __builtin_shufflevector(f[1][0], f[1][1], 0, 1, 2, 3, 4, 5, 6, 7)), P, A1);
      }
      bar();
      if (st == 0) { STC(0, S0); }
      if (st == 1) { STC(1, S1); }
    }
    float* lx = (float*)(lds + L_ML);
    lq = xrow_sum(lq);
    if (g == 0) lx[kt * 32 + 16 * hh + c] = lq;
    bar();
    const int hp = lane & 31;
    const float lt = (lx[hp] + lx[32 + hp]) + (lx[64 + hp] + lx[96 + hp]);
    const float inv = 1.f / lt;
    char* img = lds + L_RING + hp * 1040 + (64 * w + 4 * h5) * 2;
#pragma unroll
    for (int k = 0; k < 4; ++k) {
      *(u32x2*)(img + 16 * k) = u32x2{pk_bf16(A0[4 * k] * inv, A0[4 * k + 1] * inv), pk_bf16(A0[4 * k + 2] * inv, A0[4 * k + 3] * inv)};
      *(u32x2*)(img + 64 + 16 * k) = u32x2{pk_bf16(A1[4 * k] * inv, A1[4 * k + 1] * inv), pk_bf16(A1[4 * k + 2] * inv, A1[4 * k + 3] * inv)};
    }
    if (tid < 32) __hip_atomic_store((GAS u64*)(ws + WS_ML) + (b * 32 + hp) * 32 + h, ((u64)f2u(lt) << 32) | f2u(mp), RLX_AGENT);
    bar();
#pragma unroll
    for (int k = 0; k < 4; ++k) {
      const int hd = 4 * w + k;
      __builtin_amdgcn_raw_buffer_store_b128(*(const u32x4*)(lds + L_RING + hd * 1040 + lane * 16), rpb, lane * 16, WS_PB + ((b * 32 + hd) * 32 + h) * 1024, AUX_SC1);
    }
    DRAIN();
    bar();
    if (tid == 0) __hip_atomic_store((GAS u64*)(ws + WS_F2) + b * 32 + h, tag0, RLX_AGENT);
  }
  STAMP(3);
  u32x4 W2[16];
  LD_ROWS(W, w_uv, AUXOFF & 8);
  LD_OUTW(W2, w_o, AUXOFF & 16);
  float gate;
  {
    const u32x4 xv = *(const u32x4*)(x + b * 512 + lane * 8), gv = *(const u32x4*)(gw + lane * 8);
    float s = 0.f;
#pragma unroll
    for (int j = 0; j < 4; ++j) s += bfr(u2f(xv[j] << 16) * u2f(gv[j] << 16)) + bfr(u2f(xv[j] & 0xffff0000u) * u2f(gv[j] & 0xffff0000u));
    s = bfr(xrow_sum(row_sum(s)));
    gate = bfr(1.f / (1.f + __expf(-s)));
  }
  PIN();
  if (w == 0) wait_flags<false>((const GAS u64*)(ws + WS_F2) + b * 32, 32, tag0, tag0, lane);
  bar();
  STAMP(4);
  {
    u32x4 T[4];
#pragma unroll
    for (int k = 0; k < 4; ++k) T[k] = ld128<AUX_SC1>(rpb, lane * 16, WS_PB + ((b * 32 + h) * 32 + 4 * w + k) * 1024);
    const u64 mlw = __hip_atomic_load((GAS u64*)(ws + WS_ML) + (b * 32 + h) * 32 + (lane & 31), RLX_AGENT);
    const float ms = u2f((unsigned)mlw), ls = u2f((unsigned)(mlw >> 32));
    const float M0 = xrow_max(row_max(ms)), M = (M0 == -INFINITY) ? 0.f : M0;
    const float wt = __builtin_amdgcn_exp2f((ms - M) * LOG2E) * ls;
    const float cf = wt / (0.5f * xrow_sum(row_sum(wt)));
    float a[8];
#pragma unroll
    for (int j = 0; j < 8; ++j) a[j] = 0.f;
#pragma unroll
    for (int k = 0; k < 4; ++k) {
      const float f = u2f(__builtin_amdgcn_readlane(f2u(cf), 4 * w + k));
#pragma unroll
      for (int j = 0; j < 4; ++j) { a[2 * j] += f * u2f(T[k][j] << 16); a[2 * j + 1] += f * u2f(T[k][j] & 0xffff0000u); }
    }
    float* cr = (float*)(lds + L_RING);
    *(f32x4*)(cr + w * 512 + lane * 8) = f32x4{a[0], a[1], a[2], a[3]};
    *(f32x4*)(cr + w * 512 + lane * 8 + 4) = f32x4{a[4], a[5], a[6], a[7]};
    bar();
    float cv = 0.f;
#pragma unroll
    for (int ww = 0; ww < 8; ++ww) cv += cr[ww * 512 + tid];
    f32x4 D = {0.f, 0.f, 0.f, 0.f};
    rowsum<16, 1>(W, hilo(cv), sel, D);
#pragma unroll
    for (int r = 0; r < 4; ++r) { const float s = D[r] + dppf<ROR8>(D[r]); if (c < 8) red[w * 128 + 32 * g + 8 * r + c] = s; }
    bar();
    float val = 0.f;
#pragma unroll
    for (int ww = 0; ww < 8; ++ww) val += red[ww * 128 + 64 * (w >> 2) + lane];
    const int src = b * 128 + h * 4 + 3;
    out_proj(W2, bfr(val), sel, lds, rws, WS_Y + src * 2048, tid);
    if (tid < 128) DRAIN();
    bar();
    if (tid == 0) __hip_atomic_store((GAS u64*)(ws + WS_F3) + src, b ? tag1 : tag0, RLX_AGENT);
  }
  STAMP(5);
  if (w == 0) wait_flags<true>((const GAS u64*)(ws + WS_F3) + b * 128, 64, b ? tag1 : tag0, b ? tag1 : tag0, lane);
  bar();
  STAMP(6);
  {
    const int src = tid >> 2;
    const bool isb = (src & 3) == 3;
    const f32x4 y = __builtin_bit_cast(f32x4, ld128<AUX_SC1>(rws, (b * 128 + src) * 2048 + (tid & 3) * 16, WS_Y + h * 64));
    const u64 mlw = __hip_atomic_load((GAS u64*)(ws + WS_YML) + b * 128 + (isb ? src - 1 : src), RLX_AGENT);
    const float ms = u2f((unsigned)mlw), ls = isb ? 0.f : u2f((unsigned)(mlw >> 32));
    float M = fmaxf(ms, dppf<ROR8>(ms)); M = fmaxf(M, dppf<ROR4>(M)); M = (M == -INFINITY) ? 0.f : M;
    const float e = __builtin_amdgcn_exp2f((ms - M) * LOG2E);
    float L = e * ls; L += dppf<ROR8>(L); L += dppf<ROR4>(L);
    const float cf = e / L;
    float* fr = (float*)(lds + L_RING);
#pragma unroll
    for (int j = 0; j < 4; ++j) {
      float a = isb ? 0.f : cf * y[j], bb = isb ? y[j] : 0.f;
      a += dppf<ROR8>(a); a += dppf<ROR4>(a); a = xrow_sum(a);
      bb += dppf<ROR8>(bb); bb += dppf<ROR4>(bb); bb = xrow_sum(bb);
      if (lane < 4) { fr[w * 32 + lane * 4 + j] = a; fr[w * 32 + 16 + lane * 4 + j] = bb; }
    }
    bar();
    if (tid < 16) {
      float a = 0.f, bb = 0.f;
#pragma unroll
      for (int ww = 0; ww < 8; ++ww) { a += fr[ww * 32 + tid]; bb += fr[ww * 32 + 16 + tid]; }
      const float o = bfr(gate * bfr(a)) + bfr(bfr(1.f - gate) * bfr(bb));
      out[b * 512 + h * 16 + tid] = (u16)(pk_bf16(o, o) & 0xffffu);
    }
    if (h == 0 && tid == 0) __hip_atomic_store((GAS u64*)(ws + WS_EPOCH + b * 128), b ? tag1 : tag0, RLX_AGENT);
  }
  STAMP(7);
}

extern "C" void kernel_launch(void* const* d_in, const int* in_sizes, int n_in, void* d_out, int out_size, void* d_ws, size_t ws_size, hipStream_t stream) {
  fused_decode<<<256, 512, 0, stream>>>((const u16*)d_in[0], (const u16*)d_in[1], (const u16*)d_in[2], (const u16*)d_in[3], (const u16*)d_in[4],
                                        (const u16*)d_in[6], (const u16*)d_in[7], (const u16*)d_in[8], (const u16*)d_in[9], (const u16*)d_in[10],
                                        (const u16*)d_in[11], (u16*)d_out, (char*)d_ws);
}
```

```cpp
#include <hip/hip_runtime.h>
#include <stdint.h>

typedef unsigned short u16;
typedef unsigned long long u64;
typedef unsigned u32x4 __attribute__((ext_vector_type(4)));
typedef unsigned u32x2 __attribute__((ext_vector_type(2)));
typedef float f32x4 __attribute__((ext_vector_type(4)));
typedef float f32x16 __attribute__((ext_vector_type(16)));
typedef __bf16 bf16x8 __attribute__((ext_vector_type(8)));
typedef __bf16 bf16x2 __attribute__((ext_vector_type(2)));
typedef short s16x4 __attribute__((ext_vector_type(4)));
typedef __amdgpu_buffer_rsrc_t rsrc_t;
#define GAS __attribute__((address_space(1)))
#define LAS __attribute__((address_space(3)))
#define DI __device__ __forceinline__
#define RLX_AGENT __ATOMIC_RELAXED, __HIP_MEMORY_SCOPE_AGENT

#define NL 4
#define ALT 1
#define OFFM 0
#define TL 0
#define STAG 1
#define NDMA 8
#define SSLEEP 24
#define PB_ST 16
#define PB_LD 16
#define FSLEEP 4
#define STAMPS 0
constexpr int AUX_NT = 2, AUX_SC1 = 16;
constexpr float SCALE = 0.08838834764831845f;
constexpr float LOG2E = 1.4426950408889634f;

constexpr int WS_EPOCH = 0;
constexpr int WS_F1 = 1024;
constexpr int WS_F2 = 2048;
constexpr int WS_GO = 4096;
constexpr int WS_QA = 8192;
constexpr int WS_ML = WS_QA + 65536;
constexpr int WS_PB = WS_ML + 8192;
constexpr int WS_Y = WS_PB + 2 * 32 * 16 * 1024;
constexpr int WS_END = WS_Y + 2 * 128 * 2048;
constexpr int WS_STAMP = 4 << 20;

constexpr int L_RING = 0;
constexpr int L_RED = 131072;
constexpr int L_P = L_RED + 8192;
constexpr int L_MX = L_P + 32 * 144;
constexpr int L_ML = L_MX + 512;
constexpr int L_FIN = L_ML + 512;
constexpr int L_QA = 131072;
constexpr int L_END = 163840;

DI rsrc_t mk_rsrc(const void* p, unsigned bytes) { return __builtin_amdgcn_make_buffer_rsrc((void*)p, 0, bytes, 0x00020000); }
template <int AUX> DI u32x4 ld128(rsrc_t r, int voff, int soff) { return __builtin_amdgcn_raw_buffer_load_b128(r, voff, soff, AUX); }
template <int AUX> DI u32x2 ld64(rsrc_t r, int voff, int soff) { return __builtin_amdgcn_raw_buffer_load_b64(r, voff, soff, AUX); }
template <int AUX> DI unsigned ld32(rsrc_t r, int voff, int soff) { return __builtin_amdgcn_raw_buffer_load_b32(r, voff, soff, AUX); }
DI float u2f(unsigned u) { return __builtin_bit_cast(float, u); }
DI unsigned f2u(float f) { return __builtin_bit_cast(unsigned, f); }
DI unsigned pk_bf16(float lo, float hi) { bf16x2 r; r[0] = (__bf16)lo; r[1] = (__bf16)hi; return __builtin_bit_cast(unsigned, r); }
DI float bfr(float x) { return u2f(pk_bf16(x, x) << 16); }
DI unsigned hilo(float x) { const float h = bfr(x); return pk_bf16(h, x - h); }
DI float ex2(float x) { return __builtin_amdgcn_exp2f(x); }
DI float dot2(unsigned a, unsigned b, float c) { return __builtin_amdgcn_fdot2_f32_bf16(__builtin_bit_cast(bf16x2, a), __builtin_bit_cast(bf16x2, b), c, false); }
DI float dot8(const u32x4& a, const u32x4& b) { return dot2(a[3], b[3], dot2(a[2], b[2], dot2(a[1], b[1], dot2(a[0], b[0], 0.f)))); }
template <int CTRL> DI float dppf(float x) { return u2f(__builtin_amdgcn_mov_dpp(f2u(x), CTRL, 0xf, 0xf, true)); }
template <int T> DI unsigned bcast16(unsigned v) { return __builtin_amdgcn_mov_dpp(v, 0x150 + T, 0xf, 0xf, true); }
constexpr int XOR1 = 0xB1, XOR2 = 0x4E, XOR7 = 0x141, ROR8 = 0x128, ROR4 = 0x124;
DI float xrow_max(float x) {
  auto s = __builtin_amdgcn_permlane16_swap(f2u(x), f2u(x), false, false); x = fmaxf(u2f(s[0]), u2f(s[1]));
  auto t = __builtin_amdgcn_permlane32_swap(f2u(x), f2u(x), false, false); return fmaxf(u2f(t[0]), u2f(t[1]));
}
DI float xrow_sum(float x) {
  auto s = __builtin_amdgcn_permlane16_swap(f2u(x), f2u(x), false, false); x = u2f(s[0]) + u2f(s[1]);
  auto t = __builtin_amdgcn_permlane32_swap(f2u(x), f2u(x), false, false); return u2f(t[0]) + u2f(t[1]);
}
DI float row_max(float x) { x = fmaxf(x, dppf<ROR8>(x)); x = fmaxf(x, dppf<ROR4>(x)); x = fmaxf(x, dppf<XOR2>(x)); return fmaxf(x, dppf<XOR1>(x)); }
DI float row_sum(float x) { x += dppf<ROR8>(x); x += dppf<ROR4>(x); x += dppf<XOR2>(x); return x + dppf<XOR1>(x); }
DI f32x4 mfma16(u32x4 a, u32x4 b, f32x4 c) { return __builtin_amdgcn_mfma_f32_16x16x32_bf16(__builtin_bit_cast(bf16x8, a), __builtin_bit_cast(bf16x8, b), c, 0, 0, 0); }
DI f32x16 mfma32(u32x4 a, u32x4 b, f32x16 c) { return __builtin_amdgcn_mfma_f32_32x32x16_bf16(__builtin_bit_cast(bf16x8, a), __builtin_bit_cast(bf16x8, b), c, 0, 0, 0); }
DI u64 uniform64(u64 v) { return ((u64)(unsigned)__builtin_amdgcn_readfirstlane((unsigned)(v >> 32)) << 32) | (unsigned)__builtin_amdgcn_readfirstlane((unsigned)v); }
DI void bar() { __syncthreads(); }
#define DRAIN() asm volatile("s_waitcnt vmcnt(0)" ::: "memory")
#define PIN() asm volatile("" ::: "memory")
#define SB() __builtin_amdgcn_sched_barrier(0)
constexpr unsigned SPIN_CAP = 1u << 22;

template <int N, int STEP, int T = 0> DI void rowsum(const u32x4 (&W)[N], unsigned pair, const u32x4& sel, f32x4& D) {
  if constexpr (T < N) {
    const unsigned s = bcast16<T * STEP>(pair);
    const u32x4 B = {__builtin_amdgcn_perm(s, s, sel[0]), __builtin_amdgcn_perm(s, s, sel[1]), __builtin_amdgcn_perm(s, s, sel[2]), __builtin_amdgcn_perm(s, s, sel[3])};
    D = mfma16(W[T], B, D);
    rowsum<N, STEP, T + 1>(W, pair, sel, D);
  }
}
template <int T> DI void qproj_refill(const u32x4 (&W)[16], u32x4 (&U)[16], unsigned pair, const u32x4& sel, f32x4& D, rsrc_t ru, int voff, int soff) {
  if constexpr (T < 16) {
    const unsigned s = bcast16<T>(pair);
    const u32x4 B = {__builtin_amdgcn_perm(s, s, sel[0]), __builtin_amdgcn_perm(s, s, sel[1]), __builtin_amdgcn_perm(s, s, sel[2]), __builtin_amdgcn_perm(s, s, sel[3])};
    D = mfma16(W[T], B, D);
    asm volatile("" : "+v"(D));
    __builtin_amdgcn_sched_barrier(0);
    U[T] = ld128<AUX_NT>(ru, voff, soff + T * 8192);
    __builtin_amdgcn_sched_barrier(0);
    qproj_refill<T + 1>(W, U, pair, sel, D, ru, voff, soff);
  }
}
DI int swz16(int row) { const int u = (row >> 2) & 3; return ((row & 3) << 2) | (((u ^ (u >> 1)) & 1) << 1) | (u >> 1); }
DI u32x4 make_sel(int c) {
  const int j = c & 7; const unsigned b = (c & 8) ? 0x0302u : 0x0100u;
  const unsigned v = (j & 1) ? ((b << 16) | 0x0c0cu) : (0x0c0c0000u | b);
  u32x4 s;
#pragma unroll
  for (int w = 0; w < 4; ++w) s[w] = (w == (j >> 1)) ? v : 0x0c0c0c0cu;
  return s;
}
template <int N> DI float treduce(float (&x)[N], int c) {
#define TR_STEP(CNT, CTRL, BIT) _Pragma("unroll") for (int i = 0; i < (CNT); ++i) { const float a = x[i] + dppf<CTRL>(x[i]), b = x[i + (CNT)] + dppf<CTRL>(x[i + (CNT)]); x[i] = (c & (BIT)) ? b : a; }
  TR_STEP(N / 2, ROR8, 8)
  TR_STEP(N / 4, XOR7, 4)
  if constexpr (N >= 8) { TR_STEP(N / 8, XOR2, 2) } else { x[0] += dppf<XOR2>(x[0]); }
  if constexpr (N >= 16) { TR_STEP(N / 16, XOR1, 1) } else { x[0] += dppf<XOR1>(x[0]); }
#undef TR_STEP
  return x[0];
}
DI void wait_flags32(const GAS u64* f, u64 tag, int lane) {
#pragma unroll 1
  for (unsigned spins = 0; spins < SPIN_CAP; ++spins) {
    if (__all(__hip_atomic_load(f + (lane & 31), RLX_AGENT) == tag)) break;
    __builtin_amdgcn_s_sleep(6);
  }
  __builtin_amdgcn_fence(__ATOMIC_ACQUIRE, "wavefront");
}
DI void transpose_w(const u32x2 (&R)[32], u32x4 (&F)[16]) {
#pragma unroll
  for (int s = 0; s < 4; ++s)
#pragma unroll
    for (int t = 0; t < 4; ++t)
#pragma unroll
      for (int jj = 0; jj < 4; ++jj) F[4 * s + t][jj] = __builtin_amdgcn_perm(R[8 * s + 2 * jj + 1][t >> 1], R[8 * s + 2 * jj][t >> 1], (t & 1) ? 0x07060302u : 0x05040100u);
}
DI void project(const u32x4 (&F)[16], const char* lds, int g, int c, f32x4 (&D)[4]) {
  u32x4 A[4];
#pragma unroll
  for (int s = 0; s < 4; ++s) A[s] = *(const u32x4*)(lds + L_P + c * 272 + 64 * s + 16 * g);
#pragma unroll
  for (int t = 0; t < 4; ++t) D[t] = f32x4{0.f, 0.f, 0.f, 0.f};
#pragma unroll
  for (int s = 0; s < 4; ++s)
#pragma unroll
    for (int t = 0; t < 4; ++t) D[t] = mfma16(A[s], F[4 * s + t], D[t]);
}
DI unsigned lsb4(const u32x4& v) { return (v[0] & 1u) | ((v[1] & 1u) << 1) | ((v[2] & 1u) << 2) | ((v[3] & 1u) << 3); }
DI unsigned mark(float v, unsigned old, int k) { return (f2u(v) & ~1u) | (((old >> k) & 1u) ^ 1u); }

#define STAMP(k) do {} while (0)

__global__ void __launch_bounds__(512, 2)
fused_decode(const u16* __restrict__ x, const u16* __restrict__ Kc, const u16* __restrict__ Vc, const u16* __restrict__ wq, const u16* __restrict__ wo,
             const u16* __restrict__ Cc, const u16* __restrict__ w_q, const u16* __restrict__ w_uk, const u16* __restrict__ w_uv, const u16* __restrict__ w_o,
             const u16* __restrict__ gw, u16* __restrict__ out, char* ws) {
  __shared__ __attribute__((aligned(16))) char lds[L_END];
  const int bid = blockIdx.x, tid = threadIdx.x, lane = tid & 63, w = __builtin_amdgcn_readfirstlane(tid >> 6), g = lane >> 4, c = lane & 15;
  const int x8 = bid & 7, idx = bid >> 3;
  STAMP(0);
  const u64 t_start = (OFFM & (1024 | 2048)) ? __builtin_amdgcn_s_memrealtime() : 0;
  const rsrc_t rws = mk_rsrc(ws, WS_END);
  float* red = (float*)(lds + L_RED);
#define LD_EPOCH() u64 e0 = __hip_atomic_load((GAS u64*)(ws + WS_EPOCH), RLX_AGENT), e1 = __hip_atomic_load((GAS u64*)(ws + WS_EPOCH + 128), RLX_AGENT);
#define USE_EPOCH() asm volatile("" : "+v"(e0), "+v"(e1)); const u64 tag0 = uniform64(e0) + 1ull, tag1 = uniform64(e1) + 1ull;
#define LD_ROWS(W, ptr, OFF) { const rsrc_t rq = mk_rsrc(ptr, (OFF) ? 0 : 512 * 4096 * 2); \
    _Pragma("unroll") for (int t = 0; t < 16; ++t) W[t] = ld128<AUX_NT>(rq, g * (16 * 8192) + c * 16, (64 * w + t) * 8192 + h * 256); }
#define LD_OUTW(R, ptr, OFF) { const rsrc_t ro = mk_rsrc(ptr, (OFF) ? 0 : 4096 * 512 * 2); \
    _Pragma("unroll") for (int i = 0; i < 32; ++i) R[i] = ld64<AUX_NT>(ro, g * 8192 + c * 8 + (i & 3) * 1024, (h * 128 + 32 * (i >> 3) + (i & 4)) * 1024 + w * 128); }
#define QPROJ() { asm volatile("" : "+v"(x0), "+v"(x1)); f32x4 D = {0.f, 0.f, 0.f, 0.f}; rowsum<16, 1>(W, x0 | (x1 << 16), sel, D); \
    _Pragma("unroll") for (int r = 0; r < 4; ++r) red[(w * 2 + (c >> 3)) * 128 + 32 * g + 8 * r + (c & 7)] = D[r]; }
#define QREAD(q, bb) { f32x4 a0 = {0.f, 0.f, 0.f, 0.f}, a1 = a0; \
    _Pragma("unroll") for (int ww = 0; ww < 8; ++ww) { const f32x4* p = (const f32x4*)(red + (ww * 2 + (bb)) * 128 + 8 * c); a0 += p[0]; a1 += p[1]; } \
    q = u32x4{pk_bf16(a0[0], a0[1]), pk_bf16(a0[2], a0[3]), pk_bf16(a1[0], a1[1]), pk_bf16(a1[2], a1[3])}; }

  if (idx < 24) {
    const int h = __builtin_amdgcn_readfirstlane(4 * x8 + idx / 6), b = __builtin_amdgcn_readfirstlane((idx % 6) / 3), sp = __builtin_amdgcn_readfirstlane(idx % 3);
    const int src = b * 128 + h * 4 + sp;
    constexpr int NT = 2048 / NL;
    const u32x4 sel = make_sel(c);
    u32x4 W[16], kb[NL], vb[NL];
    unsigned x0 = x[tid], x1 = x[512 + tid];
    LD_EPOCH();
    unsigned old = ld32<AUX_SC1>(rws, lane * 4, WS_Y + src * 2048 + w * 256);
    const rsrc_t ro = mk_rsrc(wo, (OFFM & 128) ? 0 : 4096 * 512 * 2);
    unsigned touch = ld32<AUX_NT>(ro, 0, h * 131072);
    const rsrc_t rk = mk_rsrc(Kc, 128u << 20), rv = mk_rsrc(Vc, 128u << 20);
    const int t1 = (b * 32 + h) * NT + NT * (sp + 1) / 3;
    int t = (b * 32 + h) * NT + NT * sp / 3 + w;
#define LDT(r, tt, i) ld128<AUX_NT>(r, lane * 16 + ((i) & 3) * 1024, (tt) * (NL * 1024) + ((i) >> 2) * 4096)
#define LDKV(a, r, tt) _Pragma("unroll") for (int i = 0; i < NL; ++i) a[i] = LDT(r, tt, i);
    const bool late_q = STAG && (h & 1);
    u32x4 kb2[NL], vb2[NL];
    u64 tag;
#define USE_SMALL() { USE_EPOCH(); tag = b ? tag1 : tag0; asm volatile("" : "+v"(old) : "v"(touch)); }
    if (late_q) {
      LDKV(kb2, rk, t); LDKV(vb2, rv, t); LDKV(kb, rk, t + 8);
      if (!ALT) { LDKV(vb, rv, t + 8); }
      LD_ROWS(W, wq, OFFM & 64);
      SB();
      USE_SMALL();
      QPROJ();
    } else {
      LD_ROWS(W, wq, OFFM & 64);
      SB(); __builtin_amdgcn_s_barrier(); SB();
      LDKV(kb, rk, t);
      if (!ALT) { LDKV(vb, rv, t); }
      SB();
      USE_SMALL();
      QPROJ();
    }
    t = __builtin_amdgcn_readfirstlane(t + (late_q ? 8 : 0));
    tag = uniform64(tag);
    bar();
    u32x4 q;
    QREAD(q, b);
    STAMP(1);
    float m = -INFINITY, l = 0.f;
    f32x4 O = {0.f, 0.f, 0.f, 0.f};
#define TILE(NEXT) float xs[NL]; \
      _Pragma("unroll") for (int i = 0; i < NL; ++i) asm volatile("" : "+v"(kb[i])); \
      SB(); \
      _Pragma("unroll") for (int i = 0; i < NL; ++i) vb[i] = LDT(rv, t, i); \
      SB(); \
      _Pragma("unroll") for (int i = 0; i < NL; ++i) xs[i] = dot8(kb[i], q); \
      const float s = bfr(bfr(treduce<NL>(xs, c)) * SCALE); \
      const float mn = fmaxf(m, xrow_max(row_max(s))), ms = (mn == -INFINITY) ? 0.f : mn; \
      const float al = ex2((m - ms) * LOG2E), p = ex2((s - ms) * LOG2E); \
      m = mn; l = l * al + p; O *= al; unsigned hl = hilo(p); \
      asm volatile("" : "+v"(hl), "+v"(O), "+v"(l)); \
      NEXT; more = t < t1; \
      const rsrc_t rk2 = mk_rsrc(Kc, more ? (128u << 20) : 0u); t = more ? t : 0; \
      SB(); \
      _Pragma("unroll") for (int i = 0; i < NL; ++i) asm volatile("" : "+v"(vb[i])); \
      SB(); \
      _Pragma("unroll") for (int i = 0; i < NL; ++i) kb[i] = LDT(rk2, t, i); \
      SB(); \
      rowsum<NL, 16 / NL>(vb, hl, sel, O); asm volatile("" : "+v"(O)); \
      SB();
#define TLREC()
    bool more;
    { TILE(t += 8); TLREC(); }
    if (late_q) {
      float xs[NL];
#pragma unroll
      for (int i = 0; i < NL; ++i) xs[i] = dot8(kb2[i], q);
      const float s = bfr(bfr(treduce<NL>(xs, c)) * SCALE);
      const float mn = fmaxf(m, xrow_max(row_max(s))), ms = (mn == -INFINITY) ? 0.f : mn;
      const float al = ex2((m - ms) * LOG2E), p = ex2((s - ms) * LOG2E);
      m = mn; l = l * al + p; O *= al; rowsum<NL, 16 / NL>(vb2, hilo(p), sel, O); asm volatile("" : "+v"(O), "+v"(l));
      TLREC();
    }
    const unsigned img = (unsigned)(uintptr_t)(LAS char*)(lds + L_RING) + w * 16384;
    int dvo = (lane >> 3) * 1024 + 16 * ((lane & 7) ^ ((lane >> 4 & 1) << 1));
#pragma unroll 1
    for (int k = 0; k < 16; k += 16 / NDMA) {
      TILE(t += 8); TLREC();
#pragma unroll
      for (int j = 0; j < 16 / NDMA; ++j) {
        asm volatile("s_mov_b32 m0, %0\n\ts_nop 0\n\tbuffer_load_dwordx4 %1, %2, %3 offen nt lds" :: "s"(img + (k + j) * 1024), "v"(dvo), "s"(ro), "s"((h * 128 + 8 * (k + j)) * 1024 + w * 128) : "memory");
        dvo ^= 64;
      }
      SB();
    }
    { TILE(t += 8); TLREC(); }
    asm volatile("s_waitcnt vmcnt(%0)" :: "i"(ALT ? NL : 2 * NL) : "memory");
    {
      const int q4 = c >> 2, p4 = lane & 3, phi = (q4 >> 1) | ((g & 1) << 1);
#pragma unroll
      for (int s = 0; s < 4; ++s)
#pragma unroll
        for (int ct = 0; ct < 4; ++ct) {
          const char* a = lds + L_RING + w * 16384 + (32 * s + 8 * g + q4) * 128 + 16 * ((p4 >> 1) | ((ct ^ phi) << 1)) + 8 * (p4 & 1);
          const s16x4 lo = __builtin_amdgcn_ds_read_tr16_b64_v4i16((LAS s16x4*)a), hi = __builtin_amdgcn_ds_read_tr16_b64_v4i16((LAS s16x4*)(a + 512));
          W[4 * s + ct] = __builtin_bit_cast(u32x4, __builtin_shufflevector(lo, hi, 0, 1, 2, 3, 4, 5, 6, 7));
        }
    }
#pragma unroll
    for (int i = 0; i < 16; ++i) asm volatile("" : "+v"(W[i]));
    const GAS u64* go = (const GAS u64*)(ws + WS_GO) + h * 16 + b;
#pragma unroll 1
    while (t + 16 < t1) { TILE(t += 8); TLREC(); }
    u64 gov = __hip_atomic_load(go, RLX_AGENT);
    PIN(); SB();
    do { TILE(t += 8); TLREC(); } while (more);
    STAMP(2);
    l = xrow_sum(row_sum(l)) * (NL / 16.f);
    float* ml = (float*)(lds + L_ML);
#pragma unroll
    for (int r = 0; r < 4; ++r) {
      const unsigned hl = hilo(O[r] + dppf<ROR8>(O[r]));
      *(u16*)(lds + L_P + (2 * w + (c >> 3)) * 272 + (32 * g + 8 * r + (c & 7)) * 2) = (u16)((c & 8) ? (hl >> 16) : hl);
    }
    if (lane == 0) { ml[w] = m; ml[8 + w] = l; }
    bar();
    STAMP(4);
    f32x4 D[4];
    project(W, lds, g, c, D);
    const f32x4 m0 = *(const f32x4*)ml, m1 = *(const f32x4*)(ml + 4), l0 = *(const f32x4*)(ml + 8), l1 = *(const f32x4*)(ml + 12);
    const float ma = ml[2 * g], mb = ml[2 * g + 1];
    const float M = fmaxf(fmaxf(fmaxf(m0[0], m0[1]), fmaxf(m0[2], m0[3])), fmaxf(fmaxf(m1[0], m1[1]), fmaxf(m1[2], m1[3]))), Ms = (M == -INFINITY) ? 0.f : M;
    float L = 0.f;
#pragma unroll
    for (int k = 0; k < 4; ++k) L += ex2((m0[k] - Ms) * LOG2E) * l0[k] + ex2((m1[k] - Ms) * LOG2E) * l1[k];
    const float inv = 1.f / L, fa = ex2((ma - Ms) * LOG2E) * inv, fb = ex2((mb - Ms) * LOG2E) * inv;
    float y4[4];
#pragma unroll
    for (int k = 0; k < 4; ++k) y4[k] = fa * (D[k][0] + D[k][1]) + fb * (D[k][2] + D[k][3]);
    const auto s0 = __builtin_amdgcn_permlane32_swap(f2u(y4[0]), f2u(y4[2]), false, false), s1 = __builtin_amdgcn_permlane32_swap(f2u(y4[1]), f2u(y4[3]), false, false);
    const auto s2 = __builtin_amdgcn_permlane16_swap(f2u(u2f(s0[0]) + u2f(s0[1])), f2u(u2f(s1[0]) + u2f(s1[1])), false, false);
    const unsigned code = f2u(Ms * LOG2E + __builtin_amdgcn_logf(L)) >> 4;
    const unsigned yw = (f2u(u2f(s2[0]) + u2f(s2[1])) & ~0xffu) | (((code >> (7 * (lane & 3))) & 0x7fu) << 1) | ((old & 1u) ^ 1u);
    const u32x4 yv = {yw, f2u(dppf<0x55>(u2f(yw))), f2u(dppf<0xAA>(u2f(yw))), f2u(dppf<0xFF>(u2f(yw)))};
#pragma unroll 1
    for (unsigned spins = 0; !(OFFM & 256) && uniform64(gov) != tag && spins < SPIN_CAP; ++spins) gov = __hip_atomic_load(go, RLX_AGENT);
    if ((lane & 3) == 0) __builtin_amdgcn_raw_buffer_store_b128(yv, rws, lane * 4, WS_Y + src * 2048 + w * 256, AUX_SC1);
    STAMP(5); STAMP(6); STAMP(3);
    return;
  }

  if (OFFM & 256) { STAMP(1); STAMP(2); STAMP(3); STAMP(4); STAMP(5); STAMP(6); STAMP(7); return; }
  const int rb = idx >= 28, rs = __builtin_amdgcn_readfirstlane(4 * x8 + (idx & 3));
  const int fy_off = (rb * 128 + (tid >> 2)) * 2048 + rs * 64 + (tid & 3) * 16;
  unsigned fold; u64 tagr;
  float gate;
#define LD_GATE() const u32x4 xv = *(const u32x4*)(x + rb * 512 + lane * 8), gv = *(const u32x4*)(gw + lane * 8);
#define USE_GATE() { float s = 0.f; \
    _Pragma("unroll") for (int j = 0; j < 4; ++j) s += bfr(u2f(xv[j] << 16) * u2f(gv[j] << 16)) + bfr(u2f(xv[j] & 0xffff0000u) * u2f(gv[j] & 0xffff0000u)); \
    s = bfr(xrow_sum(row_sum(s))); gate = bfr(1.f / (1.f + __expf(-s))); }

  if (idx < 28) {
    const int h = rs;
    const u32x4 sel = make_sel(c);
    u32x4 W[16], U[16];
    unsigned x0 = x[tid], x1 = x[512 + tid];
    LD_ROWS(W, w_q, OFFM & 1);
    SB();
    {
      asm volatile("" : "+v"(x0), "+v"(x1));
      const unsigned xp = x0 | (x1 << 16);
      const rsrc_t ru = mk_rsrc(w_uk, (OFFM & 2) ? 0 : 512 * 4096 * 2);
      f32x4 D = {0.f, 0.f, 0.f, 0.f};
      qproj_refill<0>(W, U, xp, sel, D, ru, g * (16 * 8192) + c * 16, 64 * w * 8192 + h * 256);
#pragma unroll
      for (int r = 0; r < 4; ++r) red[(w * 2 + (c >> 3)) * 128 + 32 * g + 8 * r + (c & 7)] = D[r];
    }
    LD_EPOCH();
    const u32x4 fy = ld128<AUX_SC1>(rws, fy_off, WS_Y);
    const u32x4 oy0 = ld128<AUX_SC1>(rws, c * 16, WS_Y + (h * 4 + 3) * 2048 + w * 256), oy1 = ld128<AUX_SC1>(rws, c * 16, WS_Y + (128 + h * 4 + 3) * 2048 + w * 256);
    LD_GATE();
    SB();
    bar();
#pragma unroll
    for (int bb = 0; bb < 2; ++bb) {
      u32x4 q; QREAD(q, bb);
      float xs[16];
#pragma unroll
      for (int t = 0; t < 16; ++t) xs[t] = dot8(U[t], q);
      const float qa = treduce<16>(xs, c);
      *(u16*)(lds + L_RING + bb * 1024 + tid * 2) = (u16)pk_bf16(qa, qa);
    }
    SB();
    USE_EPOCH(); USE_GATE();
    fold = lsb4(fy);
    unsigned old = lsb4(oy0) | (lsb4(oy1) << 4);
    asm volatile("" : "+v"(fold), "+v"(gate), "+v"(old));
    tagr = tag0;
    DRAIN();
    bar();
    if (w == 0) {
      __builtin_amdgcn_raw_buffer_store_b128(*(const u32x4*)(lds + L_RING + lane * 16), rws, lane * 16, WS_QA + h * 1024, AUX_SC1);
      __builtin_amdgcn_raw_buffer_store_b128(*(const u32x4*)(lds + L_RING + 1024 + lane * 16), rws, lane * 16, WS_QA + (32 + h) * 1024, AUX_SC1);
      DRAIN();
      if (lane == 0) __hip_atomic_store((GAS u64*)(ws + WS_F1) + h, tag0, RLX_AGENT);
    }
    STAMP(1);
    {
      u32x2 R[32];
      LD_OUTW(R, w_o, OFFM & 16);
      LD_ROWS(W, w_uv, OFFM & 8);
      SB();
      transpose_w(R, U);
    }
#pragma unroll
    for (int i = 0; i < 16; ++i) asm volatile("" : "+v"(W[i]), "+v"(U[i]));
    STAMP(2);
    if (w == 0) {
      if (OFFM & 2048) { while (__builtin_amdgcn_s_memrealtime() - t_start < 3900) __builtin_amdgcn_s_sleep(8); }
      wait_flags32((const GAS u64*)(ws + WS_F2), tag0, lane);
      if (lane < 2) __hip_atomic_store((GAS u64*)(ws + WS_GO) + h * 16 + lane, lane ? tag1 : tag0, RLX_AGENT);
    }
    bar();
    STAMP(3);
    u32x4 T[4];
#pragma unroll
    for (int k = 0; k < 4; ++k) T[k] = ld128<PB_LD>(mk_rsrc(ws, (OFFM & 32) ? 0 : WS_END), lane * 16, WS_PB + (((k >> 1) * 32 + h) * 16 + 2 * w + (k & 1)) * 1024);
    const u32x2 mlw = ld64<AUX_SC1>(rws, (g & 1) * 4096 + c * 8, WS_ML + h * 128);
    const float ms = u2f(mlw[0]), M0 = row_max(ms), M = (M0 == -INFINITY) ? 0.f : M0;
    const float wt = ex2((ms - M) * LOG2E) * u2f(mlw[1]), cf = wt / row_sum(wt);
    float* cr = (float*)(lds + L_RING);
#pragma unroll
    for (int bb = 0; bb < 2; ++bb) {
      const float f0 = u2f(__builtin_amdgcn_readlane(f2u(cf), 16 * bb + 2 * w)), f1 = u2f(__builtin_amdgcn_readlane(f2u(cf), 16 * bb + 2 * w + 1));
      float a[8];
#pragma unroll
      for (int j = 0; j < 4; ++j) {
        a[2 * j] = f0 * u2f(T[2 * bb][j] << 16) + f1 * u2f(T[2 * bb + 1][j] << 16);
        a[2 * j + 1] = f0 * u2f(T[2 * bb][j] & 0xffff0000u) + f1 * u2f(T[2 * bb + 1][j] & 0xffff0000u);
      }
      *(f32x4*)(cr + (w * 2 + bb) * 512 + lane * 8) = f32x4{a[0], a[1], a[2], a[3]};
      *(f32x4*)(cr + (w * 2 + bb) * 512 + lane * 8 + 4) = f32x4{a[4], a[5], a[6], a[7]};
    }
    bar();
    {
      float c0 = 0.f, c1 = 0.f;
#pragma unroll
      for (int ww = 0; ww < 8; ++ww) { c0 += cr[ww * 1024 + tid]; c1 += cr[ww * 1024 + 512 + tid]; }
      const unsigned h0 = hilo(c0), h1 = hilo(c1);
      f32x4 D = {0.f, 0.f, 0.f, 0.f};
      rowsum<16, 1>(W, __builtin_amdgcn_perm(h1, h0, 0x05040100u), sel, D);
      rowsum<16, 1>(W, __builtin_amdgcn_perm(h1, h0, 0x07060302u), sel, D);
#pragma unroll
      for (int r = 0; r < 4; ++r) red[(w * 2 + (c >> 3)) * 128 + 32 * g + 8 * r + (c & 7)] = D[r];
    }
    bar();
    if (tid < 256) {
      float v = 0.f;
#pragma unroll
      for (int ww = 0; ww < 8; ++ww) v += red[ww * 256 + tid];
      *(u16*)(lds + L_P + (tid >> 7) * 272 + (tid & 127) * 2) = (u16)pk_bf16(v, v);
    }
    bar();
    f32x4 D[4];
    project(U, lds, g, c & 1, D);
    if (g == 0) {
      __builtin_amdgcn_raw_buffer_store_b128(u32x4{mark(D[0][0], old, 0), mark(D[1][0], old, 1), mark(D[2][0], old, 2), mark(D[3][0], old, 3)}, rws, c * 16, WS_Y + (h * 4 + 3) * 2048 + w * 256, AUX_SC1);
      __builtin_amdgcn_raw_buffer_store_b128(u32x4{mark(D[0][1], old, 4), mark(D[1][1], old, 5), mark(D[2][1], old, 6), mark(D[3][1], old, 7)}, rws, c * 16, WS_Y + (128 + h * 4 + 3) * 2048 + w * 256, AUX_SC1);
    }
    STAMP(4);
  } else {
    const int b = rs & 1, sp = rs >> 1;
    u32x4 W[16], S0[8], S1[8];
#define LDC(st, i) ld128<AUX_NT>(mk_rsrc(Cc, ((st) < 8 && !(OFFM & 4)) ? (16u << 20) : 0u), lane * 16 + ((i) & 3) * 1024, (b * 8192 + sp * 512 + ((st) & 7) * 64 + 8 * w + ((i) & 4)) * 1024)
#define LDS_(S, st) _Pragma("unroll") for (int i = 0; i < 8; ++i) S[i] = LDC(st, i);
#define STC(slot, S) { int ln = lane; asm volatile("" : "+v"(ln)); char* base = lds + L_RING + (slot) * 65536 + (ln >> 4) * 16384 + 8 * w * 256; \
      _Pragma("unroll") for (int i = 0; i < 8; ++i) *(u32x4*)(base + i * 256 + 16 * ((ln & 15) ^ swz16(8 * w + i))) = S[i]; }
    LD_EPOCH();
    const u32x4 fy = ld128<AUX_SC1>(rws, fy_off, WS_Y);
    LD_GATE();
    LDS_(S0, 0);
    LDS_(S1, 1);
    SB();
    USE_EPOCH(); USE_GATE();
    fold = lsb4(fy);
    asm volatile("" : "+v"(fold), "+v"(gate));
    tagr = tag1;
    STC(0, S0);
    SB();
    LDS_(S0, 2);
    SB();
    STC(1, S1);
    SB();
    LDS_(S1, 3);
    PIN();
    STAMP(1);
    if (w == 0) wait_flags32((const GAS u64*)(ws + WS_F1), tag0, lane);
    bar();
#pragma unroll
    for (int k = 0; k < 4; ++k) *(u32x4*)(lds + L_QA + (4 * w + k) * 1024 + 16 * (lane ^ swz16(4 * w + k))) = ld128<AUX_SC1>(rws, lane * 16, WS_QA + (b * 32 + 4 * w + k) * 1024);
    bar();
    const int kt = w & 3, hh = w >> 2;
#pragma unroll
    for (int s = 0; s < 16; ++s) W[s] = *(const u32x4*)(lds + L_QA + (16 * hh + c) * 1024 + 16 * ((4 * s + g) ^ swz16(c)));
#pragma unroll
    for (int s = 0; s < 16; ++s) asm volatile("" : "+v"(W[s]));
    bar();
    STAMP(2);
    float mq = -INFINITY, lq = 0.f, mp = -INFINITY;
    f32x16 A0, A1;
#pragma unroll
    for (int i = 0; i < 16; ++i) { A0[i] = 0.f; A1[i] = 0.f; }
    float* mx = (float*)(lds + L_MX);
#define STAGE(slot) { const char* tile = lds + L_RING + (slot) * 65536; \
      int ln = lane; asm volatile("" : "+v"(ln)); \
      const int g = ln >> 4, c = ln & 15, row = 16 * kt + c, swz = swz16(row); \
      const int h5 = ln >> 5, blk = (ln >> 4) & 1, q4 = (ln & 15) >> 2, p4 = ln & 3, hq = 16 * hh + c, hp = ln & 31; \
      f32x4 X = {0.f, 0.f, 0.f, 0.f}; \
      _Pragma("unroll") for (int s = 0; s < 16; ++s) X = mfma16(*(const u32x4*)(tile + (s >> 2) * 16384 + row * 256 + 16 * ((4 * (s & 3) + g) ^ swz)), W[s], X); \
      float sc[4]; \
      _Pragma("unroll") for (int r = 0; r < 4; ++r) sc[r] = bfr(bfr(X[r]) * SCALE); \
      const float tm = xrow_max(fmaxf(fmaxf(sc[0], sc[1]), fmaxf(sc[2], sc[3]))); \
      if (g == 0) mx[kt * 32 + hq] = tm; \
      bar(); \
      { const float mn = fmaxf(fmaxf(mq, fmaxf(mx[hq], mx[32 + hq])), fmaxf(mx[64 + hq], mx[96 + hq])), ms = (mn == -INFINITY) ? 0.f : mn; \
        float p[4]; \
        _Pragma("unroll") for (int r = 0; r < 4; ++r) p[r] = ex2((sc[r] - ms) * LOG2E); \
        lq = lq * ex2((mq - ms) * LOG2E) + ((p[0] + p[1]) + (p[2] + p[3])); \
        mq = mn; \
        *(u32x2*)(lds + L_P + hq * 144 + (16 * kt + 4 * g) * 2) = u32x2{pk_bf16(p[0], p[1]), pk_bf16(p[2], p[3])}; } \
      { const float mn = fmaxf(fmaxf(mp, fmaxf(mx[hp], mx[32 + hp])), fmaxf(mx[64 + hp], mx[96 + hp])), ms = (mn == -INFINITY) ? 0.f : mn; \
        const float al = ex2((mp - ms) * LOG2E); mp = mn; A0 *= al; A1 *= al; } \
      bar(); \
      _Pragma("unroll") for (int ks = 0; ks < 4; ++ks) { \
        const u32x4 P = *(const u32x4*)(lds + L_P + hp * 144 + (16 * ks + 8 * h5) * 2); \
        s16x4 f[2][2]; \
        _Pragma("unroll") for (int ct = 0; ct < 2; ++ct) _Pragma("unroll") for (int tt = 0; tt < 2; ++tt) { \
          const int r2 = 16 * ks + 8 * h5 + 4 * tt + q4, ch = 4 * (2 * (w & 1) + ct) + 2 * blk + (p4 >> 1); \
          f[ct][tt] = __builtin_amdgcn_ds_read_tr16_b64_v4i16((LAS s16x4*)(tile + (w >> 1) * 16384 + r2 * 256 + 16 * (ch ^ swz16(r2)) + 8 * (p4 & 1))); } \
        A0 = mfma32(__builtin_bit_cast(u32x4, __builtin_shufflevector(f[0][0], f[0][1], 0, 1, 2, 3, 4, 5, 6, 7)), P, A0); \
        A1 = mfma32(__builtin_bit_cast(u32x4, __builtin_shufflevector(f[1][0], f[1][1], 0, 1, 2, 3, 4, 5, 6, 7)), P, A1); } \
      bar(); }
#pragma unroll 1
    for (int it = 0; it < 4; ++it) {
      STAGE(0);
      if (it == 0) STAMP(8);
      STC(0, S0); SB(); LDS_(S0, 2 * it + 4); SB();
      STAGE(1);
      if (it == 0) STAMP(9);
      STC(1, S1); SB(); LDS_(S1, 2 * it + 5); SB();
      STAMP(10 + it);
    }
    const int h5 = lane >> 5, hq = 16 * hh + c, hp = lane & 31;
    float* lx = (float*)(lds + L_ML);
    lq = xrow_sum(lq);
    if (g == 0) lx[kt * 32 + hq] = lq;
    bar();
    const float lt = (lx[hp] + lx[32 + hp]) + (lx[64 + hp] + lx[96 + hp]);
    const float inv = 1.f / lt;
    char* img = lds + L_RING + hp * 1040 + (64 * w + 4 * h5) * 2;
#pragma unroll
    for (int k = 0; k < 4; ++k) {
      *(u32x2*)(img + 16 * k) = u32x2{pk_bf16(A0[4 * k] * inv, A0[4 * k + 1] * inv), pk_bf16(A0[4 * k + 2] * inv, A0[4 * k + 3] * inv)};
      *(u32x2*)(img + 64 + 16 * k) = u32x2{pk_bf16(A1[4 * k] * inv, A1[4 * k + 1] * inv), pk_bf16(A1[4 * k + 2] * inv, A1[4 * k + 3] * inv)};
    }
    if (tid < 32) __builtin_amdgcn_raw_buffer_store_b64(u32x2{f2u(mp), f2u(lt)}, mk_rsrc(ws, (OFFM & 512) ? 0 : WS_END), hp * 128, WS_ML + b * 4096 + sp * 8, AUX_SC1);
    bar();
#pragma unroll
    for (int k = 0; k < 4; ++k)
      __builtin_amdgcn_raw_buffer_store_b128(*(const u32x4*)(lds + L_RING + (4 * w + k) * 1040 + lane * 16), mk_rsrc(ws, (OFFM & 32) ? 0 : WS_END), lane * 16, WS_PB + ((b * 32 + 4 * w + k) * 16 + sp) * 1024, PB_ST);
    DRAIN();
    bar();
    if (tid == 0) __hip_atomic_store((GAS u64*)(ws + WS_F2) + rs, tag0, RLX_AGENT);
    STAMP(3); STAMP(4);
  }
  STAMP(5);
  if (lane == 0) *(unsigned*)(lds + L_FIN + w * 4) = fold;
  bar();
  {
    const bool isb = (lane & 12) == 12;
    if (OFFM & 1024) { while (__builtin_amdgcn_s_memrealtime() - t_start < 4300) __builtin_amdgcn_s_sleep(8); }
    if (w == 0) {
      const int po = (rb * 128 + 16 * (lane & 7)) * 2048 + rs * 64;
      const unsigned po_old = *(const unsigned*)(lds + L_FIN + (lane & 7) * 4);
#pragma unroll 1
      for (unsigned spins = 0; spins < SPIN_CAP; ++spins) {
        if (__any((ld32<AUX_SC1>(rws, po, WS_Y) ^ po_old) & 1u)) break;
        __builtin_amdgcn_s_sleep(SSLEEP);
      }
    }
    bar();
    u32x4 y;
#pragma unroll 1
    for (unsigned spins = 0; spins < SPIN_CAP; ++spins) {
      y = ld128<AUX_SC1>(rws, fy_off, WS_Y);
      if ((lsb4(y) ^ fold) == 15u) break;
      __builtin_amdgcn_s_sleep(FSLEEP);
    }
    STAMP(6);
    const float lse = isb ? -INFINITY : u2f((((y[0] >> 1) & 0x7fu) | (((y[1] >> 1) & 0x7fu) << 7) | (((y[2] >> 1) & 0x7fu) << 14) | (((y[3] >> 1) & 0x7fu) << 21)) << 4);
    float M = fmaxf(lse, dppf<ROR8>(lse)); M = fmaxf(M, dppf<ROR4>(M)); M = (M == -INFINITY) ? 0.f : M;
    const float e = ex2(lse - M);
    float L = e + dppf<ROR8>(e); L += dppf<ROR4>(L);
    const float cf = e / L;
    float* fr = (float*)(lds + L_RING);
#pragma unroll
    for (int j = 0; j < 4; ++j) {
      const float v = u2f(y[j] & ~0xffu);
      float a = isb ? 0.f : cf * v;
      a += dppf<ROR8>(a); a += dppf<ROR4>(a);
      a = xrow_sum(isb ? v : a);
      if (lane < 4) fr[w * 32 + lane * 4 + j] = a;
      if (lane >= 12 && lane < 16) fr[w * 32 + 16 + (lane - 12) * 4 + j] = a;
    }
    bar();
    if (tid < 16) {
      float a = 0.f, bb = 0.f;
#pragma unroll
      for (int ww = 0; ww < 8; ++ww) { a += fr[ww * 32 + tid]; bb += fr[ww * 32 + 16 + tid]; }
      const float o = bfr(gate * bfr(a)) + bfr(bfr(1.f - gate) * bfr(bb));
      out[rb * 512 + rs * 16 + tid] = (u16)pk_bf16(o, o);
    }
    if (rs == 0 && tid == 0) __hip_atomic_store((GAS u64*)(ws + WS_EPOCH + rb * 128), tagr, RLX_AGENT);
  }
  STAMP(7);
}

extern "C" void kernel_launch(void* const* d_in, const int* in_sizes, int n_in, void* d_out, int out_size, void* d_ws, size_t ws_size, hipStream_t stream) {
  fused_decode<<<256, 512, 0, stream>>>((const u16*)d_in[0], (const u16*)d_in[1], (const u16*)d_in[2], (const u16*)d_in[3], (const u16*)d_in[4],
                                        (const u16*)d_in[6], (const u16*)d_in[7], (const u16*)d_in[8], (const u16*)d_in[9], (const u16*)d_in[10],
                                        (const u16*)d_in[11], (u16*)d_out, (char*)d_ws);
}
```

```cpp
#include <hip/hip_runtime.h>
#include <stdint.h>
#include <type_traits>

typedef unsigned short u16;
typedef unsigned long long u64;
typedef unsigned u32x4 __attribute__((ext_vector_type(4)));
typedef unsigned u32x2 __attribute__((ext_vector_type(2)));
typedef float f32x4 __attribute__((ext_vector_type(4)));
typedef float f32x16 __attribute__((ext_vector_type(16)));
typedef __bf16 bf16x8 __attribute__((ext_vector_type(8)));
typedef __bf16 bf16x2 __attribute__((ext_vector_type(2)));
typedef short s16x4 __attribute__((ext_vector_type(4)));
typedef __amdgpu_buffer_rsrc_t rsrc_t;
#define GAS __attribute__((address_space(1)))
#define LAS __attribute__((address_space(3)))
#define DI __device__ __forceinline__
#define RLX_AGENT __ATOMIC_RELAXED, __HIP_MEMORY_SCOPE_AGENT

#define NL 4
#define OFFM 0
#define TL 0
#define STAG 0
#define NDMA 8
#define SSLEEP 24
#define PB_ST 16
#define PB_LD 16
#define BOOST 1
#define EBAR 0
#define HROT 0
#define HMAP 0
#define DEEP1 0
#define DEEP0 0
#define WROT 1
#define TROT 0
#define ILV 0
#define COOP 0
#define FSLEEP 1
#define STAMPS 0
constexpr int AUX_NT = 2, AUX_SC1 = 16;
constexpr float SCALE = 0.08838834764831845f;
constexpr float LOG2E = 1.4426950408889634f;

constexpr int WS_EPOCH = 0;
constexpr int WS_F1 = 1024;
constexpr int WS_F2 = 2048;
constexpr int WS_GO = 4096;
constexpr int WS_QA = 8192;
constexpr int WS_ML = WS_QA + 65536;
constexpr int WS_PB = WS_ML + 8192;
constexpr int WS_Y = WS_PB + 2 * 32 * 16 * 1024;
constexpr int WS_END = WS_Y + 2 * 128 * 2048;
constexpr int WS_STAMP = 4 << 20;

constexpr int L_RING = 0;
constexpr int L_RED = 131072;
constexpr int L_P = L_RED + 8192;
constexpr int L_MX = L_P + 32 * 144;
constexpr int L_ML = L_MX + 512;
constexpr int L_FIN = L_ML + 512;
constexpr int L_QA = 131072;
constexpr int L_END = 163840;

DI rsrc_t mk_rsrc(const void* p, unsigned bytes) { return __builtin_amdgcn_make_buffer_rsrc((void*)p, 0, bytes, 0x00020000); }
template <int AUX> DI u32x4 ld128(rsrc_t r, int voff, int soff) { return __builtin_amdgcn_raw_buffer_load_b128(r, voff, soff, AUX); }
template <int AUX> DI u32x2 ld64(rsrc_t r, int voff, int soff) { return __builtin_amdgcn_raw_buffer_load_b64(r, voff, soff, AUX); }
template <int AUX> DI unsigned ld32(rsrc_t r, int voff, int soff) { return __builtin_amdgcn_raw_buffer_load_b32(r, voff, soff, AUX); }
DI float u2f(unsigned u) { return __builtin_bit_cast(float, u); }
DI unsigned f2u(float f) { return __builtin_bit_cast(unsigned, f); }
DI unsigned pk_bf16(float lo, float hi) { bf16x2 r; r[0] = (__bf16)lo; r[1] = (__bf16)hi; return __builtin_bit_cast(unsigned, r); }
DI float bfr(float x) { return u2f(pk_bf16(x, x) << 16); }
DI unsigned hilo(float x) { const float h = bfr(x); return pk_bf16(h, x - h); }
DI float ex2(float x) { return __builtin_amdgcn_exp2f(x); }
DI float dot2(unsigned a, unsigned b, float c) { return __builtin_amdgcn_fdot2_f32_bf16(__builtin_bit_cast(bf16x2, a), __builtin_bit_cast(bf16x2, b), c, false); }
DI float dot8(const u32x4& a, const u32x4& b) { return dot2(a[3], b[3], dot2(a[2], b[2], dot2(a[1], b[1], dot2(a[0], b[0], 0.f)))); }
template <int CTRL> DI float dppf(float x) { return u2f(__builtin_amdgcn_mov_dpp(f2u(x), CTRL, 0xf, 0xf, true)); }
template <int T> DI unsigned bcast16(unsigned v) { return __builtin_amdgcn_mov_dpp(v, 0x150 + T, 0xf, 0xf, true); }
constexpr int XOR1 = 0xB1, XOR2 = 0x4E, XOR7 = 0x141, ROR8 = 0x128, ROR4 = 0x124;
DI float xrow_max(float x) {
  auto s = __builtin_amdgcn_permlane16_swap(f2u(x), f2u(x), false, false); x = fmaxf(u2f(s[0]), u2f(s[1]));
  auto t = __builtin_amdgcn_permlane32_swap(f2u(x), f2u(x), false, false); return fmaxf(u2f(t[0]), u2f(t[1]));
}
DI float xrow_sum(float x) {
  auto s = __builtin_amdgcn_permlane16_swap(f2u(x), f2u(x), false, false); x = u2f(s[0]) + u2f(s[1]);
  auto t = __builtin_amdgcn_permlane32_swap(f2u(x), f2u(x), false, false); return u2f(t[0]) + u2f(t[1]);
}
DI float row_max(float x) { x = fmaxf(x, dppf<ROR8>(x)); x = fmaxf(x, dppf<ROR4>(x)); x = fmaxf(x, dppf<XOR2>(x)); return fmaxf(x, dppf<XOR1>(x)); }
DI float row_sum(float x) { x += dppf<ROR8>(x); x += dppf<ROR4>(x); x += dppf<XOR2>(x); return x + dppf<XOR1>(x); }
DI f32x4 mfma16(u32x4 a, u32x4 b, f32x4 c) { return __builtin_amdgcn_mfma_f32_16x16x32_bf16(__builtin_bit_cast(bf16x8, a), __builtin_bit_cast(bf16x8, b), c, 0, 0, 0); }
DI f32x16 mfma32(u32x4 a, u32x4 b, f32x16 c) { return __builtin_amdgcn_mfma_f32_32x32x16_bf16(__builtin_bit_cast(bf16x8, a), __builtin_bit_cast(bf16x8, b), c, 0, 0, 0); }
DI u64 uniform64(u64 v) { return ((u64)(unsigned)__builtin_amdgcn_readfirstlane((unsigned)(v >> 32)) << 32) | (unsigned)__builtin_amdgcn_readfirstlane((unsigned)v); }
DI void bar() { __syncthreads(); }
#define DRAIN() asm volatile("s_waitcnt vmcnt(0)" ::: "memory")
#define PIN() asm volatile("" ::: "memory")
#define SB() __builtin_amdgcn_sched_barrier(0)
constexpr unsigned SPIN_CAP = 1u << 22;

template <int N, int STEP, int T = 0> DI void rowsum(const u32x4 (&W)[N], unsigned pair, const u32x4& sel, f32x4& D) {
  if constexpr (T < N) {
    const unsigned s = bcast16<T * STEP>(pair);
    const u32x4 B = {__builtin_amdgcn_perm(s, s, sel[0]), __builtin_amdgcn_perm(s, s, sel[1]), __builtin_amdgcn_perm(s, s, sel[2]), __builtin_amdgcn_perm(s, s, sel[3])};
    D = mfma16(W[T], B, D);
    rowsum<N, STEP, T + 1>(W, pair, sel, D);
  }
}
template <int N, int I = 0, class F> DI void static_for(F&& f) { if constexpr (I < N) { f(std::integral_constant<int, I>{}); static_for<N, I + 1>(f); } }
template <int T, class F> DI void rowsum_refill(const u32x4 (&W)[16], unsigned pair, const u32x4& sel, f32x4& D, F&& issue) {
  if constexpr (T < 16) {
    const unsigned s = bcast16<T>(pair);
    const u32x4 B = {__builtin_amdgcn_perm(s, s, sel[0]), __builtin_amdgcn_perm(s, s, sel[1]), __builtin_amdgcn_perm(s, s, sel[2]), __builtin_amdgcn_perm(s, s, sel[3])};
    D = mfma16(W[T], B, D);
    asm volatile("" : "+v"(D));
    __builtin_amdgcn_sched_barrier(0);
    issue(std::integral_constant<int, T>{});
    __builtin_amdgcn_sched_barrier(0);
    rowsum_refill<T + 1>(W, pair, sel, D, issue);
  }
}
DI int swz16(int row) { const int u = (row >> 2) & 3; return ((row & 3) << 2) | (((u ^ (u >> 1)) & 1) << 1) | (u >> 1); }
DI u32x4 make_sel(int c) {
  const int j = c & 7; const unsigned b = (c & 8) ? 0x0302u : 0x0100u;
  const unsigned v = (j & 1) ? ((b << 16) | 0x0c0cu) : (0x0c0c0000u | b);
  u32x4 s;
#pragma unroll
  for (int w = 0; w < 4; ++w) s[w] = (w == (j >> 1)) ? v : 0x0c0c0c0cu;
  return s;
}
template <int N> DI float treduce(float (&x)[N], int c) {
#define TR_STEP(CNT, CTRL, BIT) _Pragma("unroll") for (int i = 0; i < (CNT); ++i) { const float a = x[i] + dppf<CTRL>(x[i]), b = x[i + (CNT)] + dppf<CTRL>(x[i + (CNT)]); x[i] = (c & (BIT)) ? b : a; }
  TR_STEP(N / 2, ROR8, 8)
  TR_STEP(N / 4, XOR7, 4)
  if constexpr (N >= 8) { TR_STEP(N / 8, XOR2, 2) } else { x[0] += dppf<XOR2>(x[0]); }
  if constexpr (N >= 16) { TR_STEP(N / 16, XOR1, 1) } else { x[0] += dppf<XOR1>(x[0]); }
#undef TR_STEP
  return x[0];
}
DI void wait_flags32(const GAS u64* f, u64 tag, int lane) {
#pragma unroll 1
  for (unsigned spins = 0; spins < SPIN_CAP; ++spins) {
    if (__all(__hip_atomic_load(f + (lane & 31), RLX_AGENT) == tag)) break;
    __builtin_amdgcn_s_sleep(6);
  }
  __builtin_amdgcn_fence(__ATOMIC_ACQUIRE, "wavefront");
}
DI void transpose_w(const u32x2 (&R)[32], u32x4 (&F)[16]) {
#pragma unroll
  for (int s = 0; s < 4; ++s)
#pragma unroll
    for (int t = 0; t < 4; ++t)
#pragma unroll
      for (int jj = 0; jj < 4; ++jj) F[4 * s + t][jj] = __builtin_amdgcn_perm(R[8 * s + 2 * jj + 1][t >> 1], R[8 * s + 2 * jj][t >> 1], (t & 1) ? 0x07060302u : 0x05040100u);
}
DI void project(const u32x4 (&F)[16], const char* lds, int g, int c, f32x4 (&D)[4]) {
  u32x4 A[4];
#pragma unroll
  for (int s = 0; s < 4; ++s) A[s] = *(const u32x4*)(lds + L_P + c * 272 + 64 * s + 16 * g);
#pragma unroll
  for (int t = 0; t < 4; ++t) D[t] = f32x4{0.f, 0.f, 0.f, 0.f};
#pragma unroll
  for (int s = 0; s < 4; ++s)
#pragma unroll
    for (int t = 0; t < 4; ++t) D[t] = mfma16(A[s], F[4 * s + t], D[t]);
}
DI unsigned lsb4(const u32x4& v) { return (v[0] & 1u) | ((v[1] & 1u) << 1) | ((v[2] & 1u) << 2) | ((v[3] & 1u) << 3); }
DI unsigned mark(float v, unsigned old, int k) { return (f2u(v) & ~1u) | (((old >> k) & 1u) ^ 1u); }

#define STAMP(k) do {} while (0)

__global__ void __launch_bounds__(512, 2)
fused_decode(const u16* __restrict__ x, const u16* __restrict__ Kc, const u16* __restrict__ Vc, const u16* __restrict__ wq, const u16* __restrict__ wo,
             const u16* __restrict__ Cc, const u16* __restrict__ w_q, const u16* __restrict__ w_uk, const u16* __restrict__ w_uv, const u16* __restrict__ w_o,
             const u16* __restrict__ gw, u16* __restrict__ out, char* ws) {
  __shared__ __attribute__((aligned(16))) char lds[L_END];
  const int bid = blockIdx.x, tid = threadIdx.x, lane = tid & 63, w = __builtin_amdgcn_readfirstlane(tid >> 6), g = lane >> 4, c = lane & 15;
  const int x8 = bid & 7, idx = bid >> 3;
  STAMP(0);
  const u64 t_start = (OFFM & (1024 | 2048)) ? __builtin_amdgcn_s_memrealtime() : 0;
  const rsrc_t rws = mk_rsrc(ws, WS_END);
  float* red = (float*)(lds + L_RED);
#define LD_EPOCH() u64 e0 = __hip_atomic_load((GAS u64*)(ws + WS_EPOCH), RLX_AGENT), e1 = __hip_atomic_load((GAS u64*)(ws + WS_EPOCH + 128), RLX_AGENT);
#define USE_EPOCH() asm volatile("" : "+v"(e0), "+v"(e1)); const u64 tag0 = uniform64(e0) + 1ull, tag1 = uniform64(e1) + 1ull;
#define LD_ROWS(W, ptr, OFF) { const rsrc_t rq = mk_rsrc(ptr, (OFF) ? 0 : 512 * 4096 * 2); \
    _Pragma("unroll") for (int t = 0; t < 16; ++t) W[t] = ld128<AUX_NT>(rq, g * (16 * 8192) + c * 16, (64 * w + t) * 8192 + h * 256); }
#define LD_OUTW(R, ptr, OFF) { const rsrc_t ro = mk_rsrc(ptr, (OFF) ? 0 : 4096 * 512 * 2); \
    _Pragma("unroll") for (int i = 0; i < 32; ++i) R[i] = ld64<AUX_NT>(ro, g * 8192 + c * 8 + (i & 3) * 1024, (h * 128 + 32 * (i >> 3) + (i & 4)) * 1024 + w * 128); }
#define QPROJ() { asm volatile("" : "+v"(x0), "+v"(x1)); f32x4 D = {0.f, 0.f, 0.f, 0.f}; rowsum<16, 1>(W, x0 | (x1 << 16), sel, D); \
    _Pragma("unroll") for (int r = 0; r < 4; ++r) red[(w * 2 + (c >> 3)) * 128 + 32 * g + 8 * r + (c & 7)] = D[r]; }
#define QREAD(q, bb) { f32x4 a0 = {0.f, 0.f, 0.f, 0.f}, a1 = a0; \
    _Pragma("unroll") for (int ww = 0; ww < 8; ++ww) { const f32x4* p = (const f32x4*)(red + (ww * 2 + (bb)) * 128 + 8 * c); a0 += p[0]; a1 += p[1]; } \
    q = u32x4{pk_bf16(a0[0], a0[1]), pk_bf16(a0[2], a0[3]), pk_bf16(a1[0], a1[1]), pk_bf16(a1[2], a1[3])}; }

  if (idx < 24) {
    const int h = __builtin_amdgcn_readfirstlane(HMAP ? x8 + 8 * ((idx / 6 + HROT) & 3) : 4 * x8 + ((idx / 6 + HROT) & 3)), b = __builtin_amdgcn_readfirstlane((idx % 6) / 3), sp = __builtin_amdgcn_readfirstlane(idx % 3);
    const int src = b * 128 + h * 4 + sp;
    constexpr int NT = 2048 / NL;
    const u32x4 sel = make_sel(c);
    u32x4 W[16], kb[NL], vb[NL];
    const rsrc_t ro = mk_rsrc(wo, (OFFM & 128) ? 0 : 4096 * 512 * 2);
    unsigned old;
    const int wr = __builtin_amdgcn_readfirstlane((w + (WROT ? (idx % 6) * 4 / 3 : 0)) & 7);
#define LD_SMALL() unsigned x0 = x[64 * wr + lane], x1 = x[512 + 64 * wr + lane]; LD_EPOCH(); \
    old = ld32<AUX_SC1>(rws, lane * 4, WS_Y + src * 2048 + w * 256); unsigned touch = ld32<AUX_NT>(ro, 0, h * 131072);
    const rsrc_t rk = mk_rsrc(Kc, 128u << 20), rv = mk_rsrc(Vc, 128u << 20);
    const int t1 = (b * 32 + h) * NT + NT * (sp + 1) / 3;
    int t = (b * 32 + h) * NT + NT * sp / 3 + w;
#define LDT(r, tt, i) ld128<AUX_NT>(r, lane * 16 + ((i) & 3) * 1024, (tt) * (NL * 1024) + ((i) >> 2) * 4096)
#define LDKV(a, r, tt) _Pragma("unroll") for (int i = 0; i < NL; ++i) a[i] = LDT(r, tt, i);
    u32x4 kb2[NL], vb2[NL];
    unsigned coop = 0;
    if (COOP) {
      const int ln = (idx * 8 + w) * 22 + lane;
      coop = ld32<AUX_NT>(mk_rsrc(wq, 512 * 4096 * 2), (lane < 22 && ln < 4096) ? (ln >> 3) * 8192 + (ln & 7) * 128 : 0x7fffffff, x8 * 1024);
    }
    LD_SMALL();
#define LD_FIRST(T) { constexpr int i = (T) & 3; \
        if constexpr ((T) < 4) kb2[i] = LDT(rk, t, i); else if constexpr ((T) < 8) vb2[i] = LDT(rv, t, i); \
        else if constexpr ((T) < 12) kb[i] = LDT(rk, t + 8, i); else vb[i] = LDT(rv, t + 8, i); }
    {
      const rsrc_t rq = mk_rsrc(wq, (OFFM & 64) ? 0 : 512 * 4096 * 2);
      static_for<16>([&](auto T) {
        W[T] = ld128<AUX_NT>(rq, g * (16 * 8192) + c * 16, (64 * wr + T) * 8192 + h * 256);
        if constexpr (T < ILV) { SB(); LD_FIRST(T); SB(); } });
    }
    SB();
    USE_EPOCH();
    const u64 tag = b ? tag1 : tag0;
    asm volatile("" : "+v"(old), "+v"(x0), "+v"(x1) : "v"(touch), "v"(coop));
    {
      f32x4 D = {0.f, 0.f, 0.f, 0.f};
      rowsum_refill<0>(W, x0 | (x1 << 16), sel, D, [&](auto T) { if constexpr (T + ILV < 16) LD_FIRST(T + ILV); });
#pragma unroll
      for (int r = 0; r < 4; ++r) red[(wr * 2 + (c >> 3)) * 128 + 32 * g + 8 * r + (c & 7)] = D[r];
    }
    t += 8;
    bar();
    u32x4 q;
    QREAD(q, b);
    STAMP(1);
    float m = -INFINITY, l = 0.f;
    f32x4 O = {0.f, 0.f, 0.f, 0.f};
#define SOFTMAX(xs) const float s = bfr(bfr(treduce<NL>(xs, c)) * SCALE); \
      const float mn = fmaxf(m, xrow_max(row_max(s))), ms = (mn == -INFINITY) ? 0.f : mn; \
      const float al = ex2((m - ms) * LOG2E), p = ex2((s - ms) * LOG2E); \
      m = mn; l = l * al + p; O *= al; unsigned hl = hilo(p);
#define ARRIVED(a) _Pragma("unroll") for (int i = 0; i < NL; ++i) asm volatile("" : "+v"(a[i]));
#define SCORES(xs, kk) float xs[NL]; _Pragma("unroll") for (int i = 0; i < NL; ++i) xs[i] = dot8(kk[i], q);
#define TILE(WITH_V) { if (WITH_V) { ARRIVED(kb); SB(); LDKV(vb, rv, t); SB(); } \
      SCORES(xs, kb); SOFTMAX(xs); asm volatile("" : "+v"(hl), "+v"(O), "+v"(l)); \
      t += 8; SB(); ARRIVED(vb); SB(); LDKV(kb, rk, t); SB(); \
      rowsum<NL, 16 / NL>(vb, hl, sel, O); asm volatile("" : "+v"(O)); SB(); TLREC(); }
#define TILE_NOLOAD(kk, vv) { SCORES(xs, kk); SOFTMAX(xs); rowsum<NL, 16 / NL>(vv, hl, sel, O); asm volatile("" : "+v"(O), "+v"(l)); SB(); TLREC(); }
#define TLREC()
#define TILE_DEEP() { SCORES(xs, kb); ARRIVED(xs); t += 8; SB(); LDKV(kb, rk, t); SB(); \
      SOFTMAX(xs); rowsum<NL, 16 / NL>(vb, hl, sel, O); asm volatile("" : "+v"(O), "+v"(l)); SB(); LDKV(vb, rv, t); SB(); TLREC(); }
    TILE_DEEP();
    TILE_NOLOAD(kb2, vb2);
    {
      const int ndeep = ((h & 3) == 1) ? DEEP1 : DEEP0;
#pragma unroll 1
      for (int k = 0; k < ndeep; ++k) TILE_DEEP();
    }
    TILE(false);
    const unsigned img = (unsigned)(uintptr_t)(LAS char*)(lds + L_RING) + w * 16384;
    int dvo = (lane >> 3) * 1024 + 16 * ((lane & 7) ^ ((lane >> 4 & 1) << 1));
#pragma unroll 1
    for (int k = 0; k < 16; k += 16 / NDMA) {
      TILE(true);
#pragma unroll
      for (int j = 0; j < 16 / NDMA; ++j) {
        asm volatile("s_mov_b32 m0, %0\n\ts_nop 4\n\tbuffer_load_dwordx4 %1, %2, %3 offen nt lds" :: "s"(__builtin_amdgcn_readfirstlane(img + (k + j) * 1024)), "v"(dvo), "s"(ro),
                     "s"(__builtin_amdgcn_readfirstlane((h * 128 + 8 * (k + j)) * 1024 + w * 128)) : "memory");
        dvo ^= 64;
      }
      SB();
    }
    TILE(true);
    asm volatile("s_waitcnt vmcnt(%0)" :: "i"(NL) : "memory");
    {
      const int q4 = c >> 2, p4 = lane & 3, phi = (q4 >> 1) | ((g & 1) << 1);
#pragma unroll
      for (int s = 0; s < 4; ++s)
#pragma unroll
        for (int ct = 0; ct < 4; ++ct) {
          const char* a = lds + L_RING + w * 16384 + (32 * s + 8 * g + q4) * 128 + 16 * ((p4 >> 1) | ((ct ^ phi) << 1)) + 8 * (p4 & 1);
          const s16x4 lo = __builtin_amdgcn_ds_read_tr16_b64_v4i16((LAS s16x4*)a), hi = __builtin_amdgcn_ds_read_tr16_b64_v4i16((LAS s16x4*)(a + 512));
          W[4 * s + ct] = __builtin_bit_cast(u32x4, __builtin_shufflevector(lo, hi, 0, 1, 2, 3, 4, 5, 6, 7));
        }
    }
#pragma unroll
    for (int i = 0; i < 16; ++i) asm volatile("" : "+v"(W[i]));
    const GAS u64* go = (const GAS u64*)(ws + WS_GO) + h * 16 + b;
#pragma unroll 1
    while (t + 24 < t1) TILE(true);
    u64 gov;
    {
      u32x4 vb3[NL];
      ARRIVED(kb); SB();
      LDKV(vb, rv, t); SB(); LDKV(kb2, rk, t + 8); SB(); LDKV(vb2, rv, t + 8);
      SB();
      SCORES(xs, kb);
      ARRIVED(xs); SB();
      LDKV(kb, rk, t + 16); SB(); LDKV(vb3, rv, t + 16);
      gov = __hip_atomic_load(go, RLX_AGENT);
      SB();
      SOFTMAX(xs); rowsum<NL, 16 / NL>(vb, hl, sel, O); asm volatile("" : "+v"(O), "+v"(l)); SB(); TLREC();
      TILE_NOLOAD(kb2, vb2);
      TILE_NOLOAD(kb, vb3);
    }
    STAMP(2);
    l = xrow_sum(row_sum(l)) * (NL / 16.f);
    float* ml = (float*)(lds + L_ML);
#pragma unroll
    for (int r = 0; r < 4; ++r) {
      const unsigned hl = hilo(O[r] + dppf<ROR8>(O[r]));
      *(u16*)(lds + L_P + (2 * w + (c >> 3)) * 272 + (32 * g + 8 * r + (c & 7)) * 2) = (u16)((c & 8) ? (hl >> 16) : hl);
    }
    if (lane == 0) { ml[w] = m; ml[8 + w] = l; }
    bar();
    STAMP(4);
    f32x4 D[4];
    const float mj = ml[lane & 7], lj = ml[8 + (lane & 7)], ma = ml[2 * g], mb = ml[2 * g + 1];
    project(W, lds, g, c, D);
    float M = fmaxf(mj, dppf<XOR7>(mj)); M = fmaxf(M, dppf<XOR2>(M)); M = fmaxf(M, dppf<XOR1>(M));
    const float Ms = (M == -INFINITY) ? 0.f : M;
    float L = ex2((mj - Ms) * LOG2E) * lj; L += dppf<XOR7>(L); L += dppf<XOR2>(L); L += dppf<XOR1>(L);
    const float inv = __builtin_amdgcn_rcpf(L), fa = ex2((ma - Ms) * LOG2E) * inv, fb = ex2((mb - Ms) * LOG2E) * inv;
    float y4[4];
#pragma unroll
    for (int k = 0; k < 4; ++k) y4[k] = fa * (D[k][0] + D[k][1]) + fb * (D[k][2] + D[k][3]);
    const auto s0 = __builtin_amdgcn_permlane32_swap(f2u(y4[0]), f2u(y4[2]), false, false), s1 = __builtin_amdgcn_permlane32_swap(f2u(y4[1]), f2u(y4[3]), false, false);
    const auto s2 = __builtin_amdgcn_permlane16_swap(f2u(u2f(s0[0]) + u2f(s0[1])), f2u(u2f(s1[0]) + u2f(s1[1])), false, false);
    const unsigned code = f2u(Ms * LOG2E + __builtin_amdgcn_logf(L)) >> 4;
    const unsigned yw = (f2u(u2f(s2[0]) + u2f(s2[1])) & ~0xffu) | (((code >> (7 * (lane & 3))) & 0x7fu) << 1) | ((old & 1u) ^ 1u);
    const u32x4 yv = {yw, f2u(dppf<0x55>(u2f(yw))), f2u(dppf<0xAA>(u2f(yw))), f2u(dppf<0xFF>(u2f(yw)))};
#pragma unroll 1
    for (unsigned spins = 0; !(OFFM & 256) && uniform64(gov) != tag && spins < SPIN_CAP; ++spins) gov = __hip_atomic_load(go, RLX_AGENT);
    if ((lane & 3) == 0) __builtin_amdgcn_raw_buffer_store_b128(yv, rws, lane * 4, WS_Y + src * 2048 + w * 256, AUX_SC1);
    STAMP(5); STAMP(6); STAMP(3);
    return;
  }

  if (OFFM & 256) { STAMP(1); STAMP(2); STAMP(3); STAMP(4); STAMP(5); STAMP(6); STAMP(7); return; }
  const int rb = idx >= 28, rs = __builtin_amdgcn_readfirstlane(4 * x8 + (idx & 3));
  const int fy_off = (rb * 128 + (tid >> 2)) * 2048 + rs * 64 + (tid & 3) * 16;
  unsigned fold; u64 tagr;
  float gate;
#define LD_GATE() const u32x4 xv = *(const u32x4*)(x + rb * 512 + lane * 8), gv = *(const u32x4*)(gw + lane * 8);
#define USE_GATE() { float s = 0.f; \
    _Pragma("unroll") for (int j = 0; j < 4; ++j) s += bfr(u2f(xv[j] << 16) * u2f(gv[j] << 16)) + bfr(u2f(xv[j] & 0xffff0000u) * u2f(gv[j] & 0xffff0000u)); \
    s = bfr(xrow_sum(row_sum(s))); gate = bfr(1.f / (1.f + __expf(-s))); }

  if (idx < 28) {
    const int h = rs;
    const u32x4 sel = make_sel(c);
    u32x4 W[16], U[16];
    unsigned x0 = x[tid], x1 = x[512 + tid];
    LD_ROWS(W, w_q, OFFM & 1);
    SB();
    {
      asm volatile("" : "+v"(x0), "+v"(x1));
      const unsigned xp = x0 | (x1 << 16);
      const rsrc_t ru = mk_rsrc(w_uk, (OFFM & 2) ? 0 : 512 * 4096 * 2);
      f32x4 D = {0.f, 0.f, 0.f, 0.f};
      rowsum_refill<0>(W, xp, sel, D, [&](auto T) { U[T] = ld128<AUX_NT>(ru, g * (16 * 8192) + c * 16, (64 * w + T) * 8192 + h * 256); });
#pragma unroll
      for (int r = 0; r < 4; ++r) red[(w * 2 + (c >> 3)) * 128 + 32 * g + 8 * r + (c & 7)] = D[r];
    }
    LD_EPOCH();
    const u32x4 fy = ld128<AUX_SC1>(rws, fy_off, WS_Y);
    const u32x4 oy0 = ld128<AUX_SC1>(rws, c * 16, WS_Y + (h * 4 + 3) * 2048 + w * 256), oy1 = ld128<AUX_SC1>(rws, c * 16, WS_Y + (128 + h * 4 + 3) * 2048 + w * 256);
    LD_GATE();
    SB();
    bar();
#pragma unroll
    for (int bb = 0; bb < 2; ++bb) {
      u32x4 q; QREAD(q, bb);
      float xs[16];
#pragma unroll
      for (int t = 0; t < 16; ++t) xs[t] = dot8(U[t], q);
      const float qa = treduce<16>(xs, c);
      *(u16*)(lds + L_RING + bb * 1024 + tid * 2) = (u16)pk_bf16(qa, qa);
    }
    SB();
    USE_EPOCH(); USE_GATE();
    fold = lsb4(fy);
    unsigned old = lsb4(oy0) | (lsb4(oy1) << 4);
    asm volatile("" : "+v"(fold), "+v"(gate), "+v"(old));
    tagr = tag0;
    DRAIN();
    bar();
    if (w == 0) {
      __builtin_amdgcn_raw_buffer_store_b128(*(const u32x4*)(lds + L_RING + lane * 16), rws, lane * 16, WS_QA + h * 1024, AUX_SC1);
      __builtin_amdgcn_raw_buffer_store_b128(*(const u32x4*)(lds + L_RING + 1024 + lane * 16), rws, lane * 16, WS_QA + (32 + h) * 1024, AUX_SC1);
      DRAIN();
      if (lane == 0) __hip_atomic_store((GAS u64*)(ws + WS_F1) + h, tag0, RLX_AGENT);
    }
    STAMP(1);
    {
      u32x2 R[32];
      LD_OUTW(R, w_o, OFFM & 16);
      LD_ROWS(W, w_uv, OFFM & 8);
      SB();
      transpose_w(R, U);
    }
#pragma unroll
    for (int i = 0; i < 16; ++i) asm volatile("" : "+v"(W[i]), "+v"(U[i]));
    STAMP(2);
    if (w == 0) {
      if (OFFM & 2048) { while (__builtin_amdgcn_s_memrealtime() - t_start < 3900) __builtin_amdgcn_s_sleep(8); }
      wait_flags32((const GAS u64*)(ws + WS_F2), tag0, lane);
      if (lane < 2) __hip_atomic_store((GAS u64*)(ws + WS_GO) + h * 16 + lane, lane ? tag1 : tag0, RLX_AGENT);
    }
    bar();
    STAMP(3);
    u32x4 T[4];
#pragma unroll
    for (int k = 0; k < 4; ++k) T[k] = ld128<PB_LD>(mk_rsrc(ws, (OFFM & 32) ? 0 : WS_END), lane * 16, WS_PB + (((k >> 1) * 32 + h) * 16 + 2 * w + (k & 1)) * 1024);
    const u32x2 mlw = ld64<AUX_SC1>(rws, (g & 1) * 4096 + c * 8, WS_ML + h * 128);
    const float ms = u2f(mlw[0]), M0 = row_max(ms), M = (M0 == -INFINITY) ? 0.f : M0;
    const float wt = ex2((ms - M) * LOG2E) * u2f(mlw[1]), cf = wt / row_sum(wt);
    float* cr = (float*)(lds + L_RING);
#pragma unroll
    for (int bb = 0; bb < 2; ++bb) {
      const float f0 = u2f(__builtin_amdgcn_readlane(f2u(cf), 16 * bb + 2 * w)), f1 = u2f(__builtin_amdgcn_readlane(f2u(cf), 16 * bb + 2 * w + 1));
      float a[8];
#pragma unroll
      for (int j = 0; j < 4; ++j) {
        a[2 * j] = f0 * u2f(T[2 * bb][j] << 16) + f1 * u2f(T[2 * bb + 1][j] << 16);
        a[2 * j + 1] = f0 * u2f(T[2 * bb][j] & 0xffff0000u) + f1 * u2f(T[2 * bb + 1][j] & 0xffff0000u);
      }
      *(f32x4*)(cr + (w * 2 + bb) * 512 + lane * 8) = f32x4{a[0], a[1], a[2], a[3]};
      *(f32x4*)(cr + (w * 2 + bb) * 512 + lane * 8 + 4) = f32x4{a[4], a[5], a[6], a[7]};
    }
    bar();
    {
      float c0 = 0.f, c1 = 0.f;
#pragma unroll
      for (int ww = 0; ww < 8; ++ww) { c0 += cr[ww * 1024 + tid]; c1 += cr[ww * 1024 + 512 + tid]; }
      const unsigned h0 = hilo(c0), h1 = hilo(c1);
      f32x4 D = {0.f, 0.f, 0.f, 0.f};
      rowsum<16, 1>(W, __builtin_amdgcn_perm(h1, h0, 0x05040100u), sel, D);
      rowsum<16, 1>(W, __builtin_amdgcn_perm(h1, h0, 0x07060302u), sel, D);
#pragma unroll
      for (int r = 0; r < 4; ++r) red[(w * 2 + (c >> 3)) * 128 + 32 * g + 8 * r + (c & 7)] = D[r];
    }
    bar();
    if (tid < 256) {
      float v = 0.f;
#pragma unroll
      for (int ww = 0; ww < 8; ++ww) v += red[ww * 256 + tid];
      *(u16*)(lds + L_P + (tid >> 7) * 272 + (tid & 127) * 2) = (u16)pk_bf16(v, v);
    }
    bar();
    f32x4 D[4];
    project(U, lds, g, c & 1, D);
    if (g == 0) {
      __builtin_amdgcn_raw_buffer_store_b128(u32x4{mark(D[0][0], old, 0), mark(D[1][0], old, 1), mark(D[2][0], old, 2), mark(D[3][0], old, 3)}, rws, c * 16, WS_Y + (h * 4 + 3) * 2048 + w * 256, AUX_SC1);
      __builtin_amdgcn_raw_buffer_store_b128(u32x4{mark(D[0][1], old, 4), mark(D[1][1], old, 5), mark(D[2][1], old, 6), mark(D[3][1], old, 7)}, rws, c * 16, WS_Y + (128 + h * 4 + 3) * 2048 + w * 256, AUX_SC1);
    }
    STAMP(4);
  } else {
    const int b = rs & 1, sp = rs >> 1;
    u32x4 W[16], S0[8], S1[8];
#define LDC(st, i) ld128<AUX_NT>(mk_rsrc(Cc, ((st) < 8 && !(OFFM & 4)) ? (16u << 20) : 0u), lane * 16 + ((i) & 3) * 1024, (b * 8192 + sp * 512 + ((st) & 7) * 64 + 8 * w + ((i) & 4)) * 1024)
#define LDS_(S, st) _Pragma("unroll") for (int i = 0; i < 8; ++i) S[i] = LDC(st, i);
#define STC(slot, S) { int ln = lane; asm volatile("" : "+v"(ln)); char* base = lds + L_RING + (slot) * 65536 + (ln >> 4) * 16384 + 8 * w * 256; \
      _Pragma("unroll") for (int i = 0; i < 8; ++i) *(u32x4*)(base + i * 256 + 16 * ((ln & 15) ^ swz16(8 * w + i))) = S[i]; }
    LD_EPOCH();
    const u32x4 fy = ld128<AUX_SC1>(rws, fy_off, WS_Y);
    LD_GATE();
    LDS_(S0, 0);
    LDS_(S1, 1);
    SB();
    USE_EPOCH(); USE_GATE();
    fold = lsb4(fy);
    asm volatile("" : "+v"(fold), "+v"(gate));
    tagr = tag1;
    STC(0, S0);
    SB();
    LDS_(S0, 2);
    SB();
    STC(1, S1);
    SB();
    LDS_(S1, 3);
    PIN();
    STAMP(1);
    if (w == 0) wait_flags32((const GAS u64*)(ws + WS_F1), tag0, lane);
    bar();
#pragma unroll
    for (int k = 0; k < 4; ++k) *(u32x4*)(lds + L_QA + (4 * w + k) * 1024 + 16 * (lane ^ swz16(4 * w + k))) = ld128<AUX_SC1>(rws, lane * 16, WS_QA + (b * 32 + 4 * w + k) * 1024);
    bar();
    const int kt = w & 3, hh = w >> 2;
#pragma unroll
    for (int s = 0; s < 16; ++s) W[s] = *(const u32x4*)(lds + L_QA + (16 * hh + c) * 1024 + 16 * ((4 * s + g) ^ swz16(c)));
#pragma unroll
    for (int s = 0; s < 16; ++s) asm volatile("" : "+v"(W[s]));
    bar();
    STAMP(2);
    float mq = -INFINITY, lq = 0.f, mp = -INFINITY;
    f32x16 A0, A1;
#pragma unroll
    for (int i = 0; i < 16; ++i) { A0[i] = 0.f; A1[i] = 0.f; }
    float* mx = (float*)(lds + L_MX);
#define STAGE(slot) { const char* tile = lds + L_RING + (slot) * 65536; \
      int ln = lane; asm volatile("" : "+v"(ln)); \
      const int g = ln >> 4, c = ln & 15, row = 16 * kt + c, swz = swz16(row); \
      const int h5 = ln >> 5, blk = (ln >> 4) & 1, q4 = (ln & 15) >> 2, p4 = ln & 3, hq = 16 * hh + c, hp = ln & 31; \
      f32x4 X = {0.f, 0.f, 0.f, 0.f}; \
      _Pragma("unroll") for (int s = 0; s < 16; ++s) X = mfma16(*(const u32x4*)(tile + (s >> 2) * 16384 + row * 256 + 16 * ((4 * (s & 3) + g) ^ swz)), W[s], X); \
      float sc[4]; \
      _Pragma("unroll") for (int r = 0; r < 4; ++r) sc[r] = bfr(bfr(X[r]) * SCALE); \
      const float tm = xrow_max(fmaxf(fmaxf(sc[0], sc[1]), fmaxf(sc[2], sc[3]))); \
      if (g == 0) mx[kt * 32 + hq] = tm; \
      bar(); \
      { const float mn = fmaxf(fmaxf(mq, fmaxf(mx[hq], mx[32 + hq])), fmaxf(mx[64 + hq], mx[96 + hq])), ms = (mn == -INFINITY) ? 0.f : mn; \
        float p[4]; \
        _Pragma("unroll") for (int r = 0; r < 4; ++r) p[r] = ex2((sc[r] - ms) * LOG2E); \
        lq = lq * ex2((mq - ms) * LOG2E) + ((p[0] + p[1]) + (p[2] + p[3])); \
        mq = mn; \
        *(u32x2*)(lds + L_P + hq * 144 + (16 * kt + 4 * g) * 2) = u32x2{pk_bf16(p[0], p[1]), pk_bf16(p[2], p[3])}; } \
      { const float mn = fmaxf(fmaxf(mp, fmaxf(mx[hp], mx[32 + hp])), fmaxf(mx[64 + hp], mx[96 + hp])), ms = (mn == -INFINITY) ? 0.f : mn; \
        const float al = ex2((mp - ms) * LOG2E); mp = mn; A0 *= al; A1 *= al; } \
      bar(); \
      _Pragma("unroll") for (int ks = 0; ks < 4; ++ks) { \
        const u32x4 P = *(const u32x4*)(lds + L_P + hp * 144 + (16 * ks + 8 * h5) * 2); \
        s16x4 f[2][2]; \
        _Pragma("unroll") for (int ct = 0; ct < 2; ++ct) _Pragma("unroll") for (int tt = 0; tt < 2; ++tt) { \
          const int r2 = 16 * ks + 8 * h5 + 4 * tt + q4, ch = 4 * (2 * (w & 1) + ct) + 2 * blk + (p4 >> 1); \
          f[ct][tt] = __builtin_amdgcn_ds_read_tr16_b64_v4i16((LAS s16x4*)(tile + (w >> 1) * 16384 + r2 * 256 + 16 * (ch ^ swz16(r2)) + 8 * (p4 & 1))); } \
        A0 = mfma32(__builtin_bit_cast(u32x4, __builtin_shufflevector(f[0][0], f[0][1], 0, 1, 2, 3, 4, 5, 6, 7)), P, A0); \
        A1 = mfma32(__builtin_bit_cast(u32x4, __builtin_shufflevector(f[1][0], f[1][1], 0, 1, 2, 3, 4, 5, 6, 7)), P, A1); } \
      bar(); }
#pragma unroll 1
    for (int it = 0; it < 4; ++it) {
      STAGE(0);
      if (it == 0) STAMP(8);
      STC(0, S0); SB(); LDS_(S0, 2 * it + 4); SB();
      STAGE(1);
      if (it == 0) STAMP(9);
      STC(1, S1); SB(); LDS_(S1, 2 * it + 5); SB();
      STAMP(10 + it);
    }
    const int h5 = lane >> 5, hq = 16 * hh + c, hp = lane & 31;
    float* lx = (float*)(lds + L_ML);
    lq = xrow_sum(lq);
    if (g == 0) lx[kt * 32 + hq] = lq;
    bar();
    const float lt = (lx[hp] + lx[32 + hp]) + (lx[64 + hp] + lx[96 + hp]);
    const float inv = 1.f / lt;
    char* img = lds + L_RING + hp * 1040 + (64 * w + 4 * h5) * 2;
#pragma unroll
    for (int k = 0; k < 4; ++k) {
      *(u32x2*)(img + 16 * k) = u32x2{pk_bf16(A0[4 * k] * inv, A0[4 * k + 1] * inv), pk_bf16(A0[4 * k + 2] * inv, A0[4 * k + 3] * inv)};
      *(u32x2*)(img + 64 + 16 * k) = u32x2{pk_bf16(A1[4 * k] * inv, A1[4 * k + 1] * inv), pk_bf16(A1[4 * k + 2] * inv, A1[4 * k + 3] * inv)};
    }
    if (tid < 32) __builtin_amdgcn_raw_buffer_store_b64(u32x2{f2u(mp), f2u(lt)}, mk_rsrc(ws, (OFFM & 512) ? 0 : WS_END), hp * 128, WS_ML + b * 4096 + sp * 8, AUX_SC1);
    bar();
#pragma unroll
    for (int k = 0; k < 4; ++k)
      __builtin_amdgcn_raw_buffer_store_b128(*(const u32x4*)(lds + L_RING + (4 * w + k) * 1040 + lane * 16), mk_rsrc(ws, (OFFM & 32) ? 0 : WS_END), lane * 16, WS_PB + ((b * 32 + 4 * w + k) * 16 + sp) * 1024, PB_ST);
    DRAIN();
    bar();
    if (tid == 0) __hip_atomic_store((GAS u64*)(ws + WS_F2) + rs, tag0, RLX_AGENT);
    STAMP(3); STAMP(4);
  }
  STAMP(5);
  {
    const bool isb = (lane & 12) == 12;
    float* fr = (float*)(lds + L_RING);
    u32x4 y = {0u, 0u, 0u, 0u};
#define POLL_Y(SLP) _Pragma("unroll 1") for (unsigned spins = 0; spins < SPIN_CAP; ++spins) { \
      y = ld128<AUX_SC1>(rws, fy_off, WS_Y); if ((lsb4(y) ^ fold) == 15u) break; __builtin_amdgcn_s_sleep(SLP); }
#define SUM_ROWS(v) { const float a0 = v[0] + dppf<ROR8>(v[0]), a1 = v[1] + dppf<ROR8>(v[1]), a2 = v[2] + dppf<ROR8>(v[2]), a3 = v[3] + dppf<ROR8>(v[3]); \
      const float b0 = (c & 8) ? a2 : a0, b1 = (c & 8) ? a3 : a1; \
      const float c0 = b0 + dppf<0x124>(b0), c1 = b1 + dppf<0x12C>(b1); \
      v[0] = xrow_sum((c & 4) ? c0 : c1); }
    if (isb) { POLL_Y(16); }
    {
      float v[4];
#pragma unroll
      for (int j = 0; j < 4; ++j) v[j] = u2f(y[j] & ~0xffu);
      SUM_ROWS(v);
      if (lane < 16) fr[128 + w * 16 + lane] = v[0];
      if (lane == 0) *(unsigned*)(lds + L_FIN + w * 4) = fold;
    }
    bar();
    float bpart = 0.f;
    if (w == 0) {
#pragma unroll
      for (int ww = 0; ww < 8; ++ww) bpart += fr[128 + ww * 16 + c];
      bpart = bfr(bfr(1.f - gate) * bfr(bpart));
      const int po = (rb * 128 + 16 * (lane & 7)) * 2048 + rs * 64;
      const unsigned po_old = *(const unsigned*)(lds + L_FIN + (lane & 7) * 4);
#pragma unroll 1
      for (unsigned spins = 0; spins < SPIN_CAP; ++spins) {
        if (__any((ld32<AUX_SC1>(rws, po, WS_Y) ^ po_old) & 1u)) break;
        __builtin_amdgcn_s_sleep(SSLEEP);
      }
    }
    GAS u16* op = (GAS u16*)out + rb * 512 + rs * 16 + 4 * (c & 3) + (((c >> 2) & 2) | (((c >> 2) & 1) ^ 1));
    asm volatile("" : "+v"(bpart), "+v"(op));
    bar();
    y = u32x4{0u, 0u, 0u, 0xfeu};
    if (!isb) { POLL_Y(FSLEEP); }
    STAMP(6);
    const float lse = u2f((((y[0] >> 1) & 0x7fu) | (((y[1] >> 1) & 0x7fu) << 7) | (((y[2] >> 1) & 0x7fu) << 14) | (((y[3] >> 1) & 0x7fu) << 21)) << 4);
    float M = fmaxf(lse, dppf<ROR8>(lse)); M = fmaxf(M, dppf<ROR4>(M));
    const float e = isb ? 0.f : ex2(lse - M);
    float L = e + dppf<ROR8>(e); L += dppf<ROR4>(L);
    const float cf = e * __builtin_amdgcn_rcpf(L);
    float v[4];
#pragma unroll
    for (int j = 0; j < 4; ++j) v[j] = cf * u2f(y[j] & ~0xffu);
    SUM_ROWS(v);
    if (lane < 16) fr[w * 16 + lane] = v[0];
    bar();
    if (tid < 16) {
      const float a = ((fr[tid] + fr[16 + tid]) + (fr[32 + tid] + fr[48 + tid])) + ((fr[64 + tid] + fr[80 + tid]) + (fr[96 + tid] + fr[112 + tid]));
      const float o = bfr(gate * bfr(a)) + bpart;
      *op = (u16)pk_bf16(o, o);
    }
    if (rs == 0 && tid == 0) __hip_atomic_store((GAS u64*)(ws + WS_EPOCH + rb * 128), tagr, RLX_AGENT);
  }
  STAMP(7);
}

extern "C" void kernel_launch(void* const* d_in, const int* in_sizes, int n_in, void* d_out, int out_size, void* d_ws, size_t ws_size, hipStream_t stream) {
  fused_decode<<<256, 512, 0, stream>>>((const u16*)d_in[0], (const u16*)d_in[1], (const u16*)d_in[2], (const u16*)d_in[3], (const u16*)d_in[4],
                                        (const u16*)d_in[6], (const u16*)d_in[7], (const u16*)d_in[8], (const u16*)d_in[9], (const u16*)d_in[10],
                                        (const u16*)d_in[11], (u16*)d_out, (char*)d_ws);
}
```

```cpp
#include <hip/hip_runtime.h>
#include <stdint.h>
#include <type_traits>

typedef unsigned short u16;
typedef unsigned long long u64;
typedef unsigned u32x4 __attribute__((ext_vector_type(4)));
typedef unsigned u32x2 __attribute__((ext_vector_type(2)));
typedef float f32x4 __attribute__((ext_vector_type(4)));
typedef float f32x16 __attribute__((ext_vector_type(16)));
typedef __bf16 bf16x8 __attribute__((ext_vector_type(8)));
typedef __bf16 bf16x2 __attribute__((ext_vector_type(2)));
typedef short s16x4 __attribute__((ext_vector_type(4)));
typedef __amdgpu_buffer_rsrc_t rsrc_t;
#define GAS __attribute__((address_space(1)))
#define LAS __attribute__((address_space(3)))
#define DI __device__ __forceinline__
#define RLX_AGENT __ATOMIC_RELAXED, __HIP_MEMORY_SCOPE_AGENT

#define NL 4
#define OFFM 0
#define TL 0
#define STAG 0
#define NDMA 8
#define SSLEEP 24
#define PB_ST 18
#define PB_LD 18
#define BOOST 1
#define EBAR 0
#define HROT 0
#define HMAP 0
#define DEEP1 0
#define DEEP0 0
#define WROT 1
#define TROT 0
#define ILV 0
#define COOP 0
#define PRE4 0
#define M2 0
#define A_PRE 18
#define A_QA 16
#define A_ST 16
#define A_POLL 16
#define A_YST 16
#define WSLEEP 6
#define OUTSC1 0
#define BDELAY 0
#define FSLEEP 0
#define STAMPS 0
constexpr int AUX_NT = 2, AUX_SC1 = 16;
constexpr float SCALE = 0.08838834764831845f;
constexpr float LOG2E = 1.4426950408889634f;

constexpr int WS_EPOCH = 0;
constexpr int WS_F1 = 1024;
constexpr int WS_F2 = 2048;
constexpr int WS_GO = 4096;
constexpr int WS_QA = 8192;
constexpr int WS_ML = WS_QA + 65536;
constexpr int WS_PB = WS_ML + 8192;
constexpr int WS_Y = WS_PB + 2 * 32 * 16 * 1024;
constexpr int WS_END = WS_Y + 2 * 128 * 2048;
constexpr int WS_STAMP = 4 << 20;

constexpr int L_RING = 0;
constexpr int L_RED = 131072;
constexpr int L_P = L_RED + 8192;
constexpr int L_MX = L_P + 32 * 144;
constexpr int L_ML = L_MX + 512;
constexpr int L_FIN = L_ML + 512;
constexpr int L_QA = 131072;
constexpr int L_END = 163840;

DI rsrc_t mk_rsrc(const void* p, unsigned bytes) { return __builtin_amdgcn_make_buffer_rsrc((void*)p, 0, bytes, 0x00020000); }
template <int AUX> DI u32x4 ld128(rsrc_t r, int voff, int soff) { return __builtin_amdgcn_raw_buffer_load_b128(r, voff, soff, AUX); }
template <int AUX> DI u32x2 ld64(rsrc_t r, int voff, int soff) { return __builtin_amdgcn_raw_buffer_load_b64(r, voff, soff, AUX); }
template <int AUX> DI unsigned ld32(rsrc_t r, int voff, int soff) { return __builtin_amdgcn_raw_buffer_load_b32(r, voff, soff, AUX); }
DI float u2f(unsigned u) { return __builtin_bit_cast(float, u); }
DI unsigned f2u(float f) { return __builtin_bit_cast(unsigned, f); }
DI unsigned pk_bf16(float lo, float hi) { bf16x2 r; r[0] = (__bf16)lo; r[1] = (__bf16)hi; return __builtin_bit_cast(unsigned, r); }
DI float bfr(float x) { return u2f(pk_bf16(x, x) << 16); }
DI unsigned hilo(float x) { const float h = bfr(x); return pk_bf16(h, x - h); }
DI float ex2(float x) { return __builtin_amdgcn_exp2f(x); }
DI float dot2(unsigned a, unsigned b, float c) { return __builtin_amdgcn_fdot2_f32_bf16(__builtin_bit_cast(bf16x2, a), __builtin_bit_cast(bf16x2, b), c, false); }
DI float dot8(const u32x4& a, const u32x4& b) { return dot2(a[3], b[3], dot2(a[2], b[2], dot2(a[1], b[1], dot2(a[0], b[0], 0.f)))); }
template <int CTRL> DI float dppf(float x) { return u2f(__builtin_amdgcn_mov_dpp(f2u(x), CTRL, 0xf, 0xf, true)); }
template <int T> DI unsigned bcast16(unsigned v) { return __builtin_amdgcn_mov_dpp(v, 0x150 + T, 0xf, 0xf, true); }
constexpr int XOR1 = 0xB1, XOR2 = 0x4E, XOR7 = 0x141, ROR8 = 0x128, ROR4 = 0x124;
DI float xrow_max(float x) {
  auto s = __builtin_amdgcn_permlane16_swap(f2u(x), f2u(x), false, false); x = fmaxf(u2f(s[0]), u2f(s[1]));
  auto t = __builtin_amdgcn_permlane32_swap(f2u(x), f2u(x), false, false); return fmaxf(u2f(t[0]), u2f(t[1]));
}
DI float xrow_sum(float x) {
  auto s = __builtin_amdgcn_permlane16_swap(f2u(x), f2u(x), false, false); x = u2f(s[0]) + u2f(s[1]);
  auto t = __builtin_amdgcn_permlane32_swap(f2u(x), f2u(x), false, false); return u2f(t[0]) + u2f(t[1]);
}
DI float row_max(float x) { x = fmaxf(x, dppf<ROR8>(x)); x = fmaxf(x, dppf<ROR4>(x)); x = fmaxf(x, dppf<XOR2>(x)); return fmaxf(x, dppf<XOR1>(x)); }
DI float row_sum(float x) { x += dppf<ROR8>(x); x += dppf<ROR4>(x); x += dppf<XOR2>(x); return x + dppf<XOR1>(x); }
DI f32x4 mfma16(u32x4 a, u32x4 b, f32x4 c) { return __builtin_amdgcn_mfma_f32_16x16x32_bf16(__builtin_bit_cast(bf16x8, a), __builtin_bit_cast(bf16x8, b), c, 0, 0, 0); }
DI f32x16 mfma32(u32x4 a, u32x4 b, f32x16 c) { return __builtin_amdgcn_mfma_f32_32x32x16_bf16(__builtin_bit_cast(bf16x8, a), __builtin_bit_cast(bf16x8, b), c, 0, 0, 0); }
DI u64 uniform64(u64 v) { return ((u64)(unsigned)__builtin_amdgcn_readfirstlane((unsigned)(v >> 32)) << 32) | (unsigned)__builtin_amdgcn_readfirstlane((unsigned)v); }
DI u64 next_tag(u64 e) { return e * 0x9E3779B97F4A7C15ull + 0x632BE59BD9B4E019ull; }
DI void bar() { __syncthreads(); }
#define DRAIN() asm volatile("s_waitcnt vmcnt(0)" ::: "memory")
#define PIN() asm volatile("" ::: "memory")
#define SB() __builtin_amdgcn_sched_barrier(0)
constexpr unsigned SPIN_CAP = 1u << 22;

template <int N, int STEP, int T = 0> DI void rowsum(const u32x4 (&W)[N], unsigned pair, const u32x4& sel, f32x4& D) {
  if constexpr (T < N) {
    const unsigned s = bcast16<T * STEP>(pair);
    const u32x4 B = {__builtin_amdgcn_perm(s, s, sel[0]), __builtin_amdgcn_perm(s, s, sel[1]), __builtin_amdgcn_perm(s, s, sel[2]), __builtin_amdgcn_perm(s, s, sel[3])};
    D = mfma16(W[T], B, D);
    rowsum<N, STEP, T + 1>(W, pair, sel, D);
  }
}
template <int N, int I = 0, class F> DI void static_for(F&& f) { if constexpr (I < N) { f(std::integral_constant<int, I>{}); static_for<N, I + 1>(f); } }
template <int T, class F> DI void rowsum_refill(const u32x4 (&W)[16], unsigned pair, const u32x4& sel, f32x4& D, F&& issue) {
  if constexpr (T < 16) {
    const unsigned s = bcast16<T>(pair);
    const u32x4 B = {__builtin_amdgcn_perm(s, s, sel[0]), __builtin_amdgcn_perm(s, s, sel[1]), __builtin_amdgcn_perm(s, s, sel[2]), __builtin_amdgcn_perm(s, s, sel[3])};
    D = mfma16(W[T], B, D);
    asm volatile("" : "+v"(D));
    __builtin_amdgcn_sched_barrier(0);
    issue(std::integral_constant<int, T>{});
    __builtin_amdgcn_sched_barrier(0);
    rowsum_refill<T + 1>(W, pair, sel, D, issue);
  }
}
DI int swz16(int row) { const int u = (row >> 2) & 3; return ((row & 3) << 2) | (((u ^ (u >> 1)) & 1) << 1) | (u >> 1); }
DI u32x4 make_sel(int c) {
  const int j = c & 7; const unsigned b = (c & 8) ? 0x0302u : 0x0100u;
  const unsigned v = (j & 1) ? ((b << 16) | 0x0c0cu) : (0x0c0c0000u | b);
  u32x4 s;
#pragma unroll
  for (int w = 0; w < 4; ++w) s[w] = (w == (j >> 1)) ? v : 0x0c0c0c0cu;
  return s;
}
template <int N> DI float treduce(float (&x)[N], int c) {
#define TR_STEP(CNT, CTRL, BIT) _Pragma("unroll") for (int i = 0; i < (CNT); ++i) { const float a = x[i] + dppf<CTRL>(x[i]), b = x[i + (CNT)] + dppf<CTRL>(x[i + (CNT)]); x[i] = (c & (BIT)) ? b : a; }
  TR_STEP(N / 2, ROR8, 8)
  TR_STEP(N / 4, XOR7, 4)
  if constexpr (N >= 8) { TR_STEP(N / 8, XOR2, 2) } else { x[0] += dppf<XOR2>(x[0]); }
  if constexpr (N >= 16) { TR_STEP(N / 16, XOR1, 1) } else { x[0] += dppf<XOR1>(x[0]); }
#undef TR_STEP
  return x[0];
}
DI void wait_flags32(const GAS u64* f, u64 tag, int lane) {
#pragma unroll 1
  for (unsigned spins = 0; spins < SPIN_CAP; ++spins) {
    if (__all(__hip_atomic_load(f + (lane & 31), RLX_AGENT) == tag)) break;
    __builtin_amdgcn_s_sleep(WSLEEP);
  }
  __builtin_amdgcn_fence(__ATOMIC_ACQUIRE, "wavefront");
}
DI void transpose_w(const u32x2 (&R)[32], u32x4 (&F)[16]) {
#pragma unroll
  for (int s = 0; s < 4; ++s)
#pragma unroll
    for (int t = 0; t < 4; ++t)
#pragma unroll
      for (int jj = 0; jj < 4; ++jj) F[4 * s + t][jj] = __builtin_amdgcn_perm(R[8 * s + 2 * jj + 1][t >> 1], R[8 * s + 2 * jj][t >> 1], (t & 1) ? 0x07060302u : 0x05040100u);
}
DI void project(const u32x4 (&F)[16], const char* lds, int g, int c, f32x4 (&D)[4]) {
  u32x4 A[4];
#pragma unroll
  for (int s = 0; s < 4; ++s) A[s] = *(const u32x4*)(lds + L_P + c * 272 + 64 * s + 16 * g);
#pragma unroll
  for (int t = 0; t < 4; ++t) D[t] = f32x4{0.f, 0.f, 0.f, 0.f};
#pragma unroll
  for (int s = 0; s < 4; ++s)
#pragma unroll
    for (int t = 0; t < 4; ++t) D[t] = mfma16(A[s], F[4 * s + t], D[t]);
}
DI unsigned lsb4(const u32x4& v) { return (v[0] & 1u) | ((v[1] & 1u) << 1) | ((v[2] & 1u) << 2) | ((v[3] & 1u) << 3); }
DI unsigned mark(float v, unsigned old, int k) { return (f2u(v) & ~1u) | (((old >> k) & 1u) ^ 1u); }

#define STAMP(k) do {} while (0)

__global__ void __launch_bounds__(512, 2)
fused_decode(const u16* __restrict__ x, const u16* __restrict__ Kc, const u16* __restrict__ Vc, const u16* __restrict__ wq, const u16* __restrict__ wo,
             const u16* __restrict__ Cc, const u16* __restrict__ w_q, const u16* __restrict__ w_uk, const u16* __restrict__ w_uv, const u16* __restrict__ w_o,
             const u16* __restrict__ gw, u16* __restrict__ out, char* ws) {
  __shared__ __attribute__((aligned(16))) char lds[L_END];
  const int bid = blockIdx.x, tid = threadIdx.x, lane = tid & 63, w = __builtin_amdgcn_readfirstlane(tid >> 6), g = lane >> 4, c = lane & 15;
  const int x8 = bid & 7, idx = bid >> 3;
  STAMP(0);
  const u64 t_start = (OFFM & (1024 | 2048)) ? __builtin_amdgcn_s_memrealtime() : 0;
  asm volatile("" :: "s"(x), "s"(Kc), "s"(Vc), "s"(wq), "s"(wo), "s"(ws));
  const rsrc_t rws = mk_rsrc(ws, WS_END);
  float* red = (float*)(lds + L_RED);
#define LD_EPOCH() u64 e0 = __hip_atomic_load((GAS u64*)(ws + WS_EPOCH), RLX_AGENT), e1 = __hip_atomic_load((GAS u64*)(ws + WS_EPOCH + 128), RLX_AGENT);
#define USE_EPOCH() asm volatile("" : "+v"(e0), "+v"(e1)); const u64 tag0 = next_tag(uniform64(e0)), tag1 = next_tag(uniform64(e1));
#define LD_ROWS(W, ptr, OFF) { const rsrc_t rq = mk_rsrc(ptr, (OFF) ? 0 : 512 * 4096 * 2); \
    _Pragma("unroll") for (int t = 0; t < 16; ++t) W[t] = ld128<AUX_NT>(rq, g * (16 * 8192) + c * 16, (64 * w + t) * 8192 + h * 256); }
#define LD_OUTW(R, ptr, OFF) { const rsrc_t ro = mk_rsrc(ptr, (OFF) ? 0 : 4096 * 512 * 2); \
    _Pragma("unroll") for (int i = 0; i < 32; ++i) R[i] = ld64<AUX_NT>(ro, g * 8192 + c * 8 + (i & 3) * 1024, (h * 128 + 32 * (i >> 3) + (i & 4)) * 1024 + w * 128); }
#define QPROJ() { asm volatile("" : "+v"(x0), "+v"(x1)); f32x4 D = {0.f, 0.f, 0.f, 0.f}; rowsum<16, 1>(W, x0 | (x1 << 16), sel, D); \
    _Pragma("unroll") for (int r = 0; r < 4; ++r) red[(w * 2 + (c >> 3)) * 128 + 32 * g + 8 * r + (c & 7)] = D[r]; }
#define QREAD(q, bb) { f32x4 a0 = {0.f, 0.f, 0.f, 0.f}, a1 = a0; \
    _Pragma("unroll") for (int ww = 0; ww < 8; ++ww) { const f32x4* p = (const f32x4*)(red + (ww * 2 + (bb)) * 128 + 8 * c); a0 += p[0]; a1 += p[1]; } \
    q = u32x4{pk_bf16(a0[0], a0[1]), pk_bf16(a0[2], a0[3]), pk_bf16(a1[0], a1[1]), pk_bf16(a1[2], a1[3])}; }

  if (idx >= 24) {
  if (OFFM & 256) { STAMP(1); STAMP(2); STAMP(3); STAMP(4); STAMP(5); STAMP(6); STAMP(7); return; }
  const int rb = idx >= 28, rs = __builtin_amdgcn_readfirstlane(4 * x8 + (idx & 3));
  const int fy_off = (rb * 128 + (tid >> 2)) * 2048 + rs * 64 + (tid & 3) * 16;
  unsigned fold; u64 tagr;
  float gate;
#define LD_GATE() const u32x4 xv = *(const u32x4*)(x + rb * 512 + lane * 8), gv = *(const u32x4*)(gw + lane * 8);
#define USE_GATE() { float s = 0.f; \
    _Pragma("unroll") for (int j = 0; j < 4; ++j) s += bfr(u2f(xv[j] << 16) * u2f(gv[j] << 16)) + bfr(u2f(xv[j] & 0xffff0000u) * u2f(gv[j] & 0xffff0000u)); \
    s = bfr(xrow_sum(row_sum(s))); gate = bfr(1.f / (1.f + __expf(-s))); }

  if (idx < 28) {
    const int h = rs;
    const u32x4 sel = make_sel(c);
    u32x4 W[16], U[16];
    unsigned x0 = x[tid], x1 = x[512 + tid];
    LD_ROWS(W, w_q, OFFM & 1);
    SB();
    {
      asm volatile("" : "+v"(x0), "+v"(x1));
      const unsigned xp = x0 | (x1 << 16);
      const rsrc_t ru = mk_rsrc(w_uk, (OFFM & 2) ? 0 : 512 * 4096 * 2);
      f32x4 D = {0.f, 0.f, 0.f, 0.f};
      rowsum_refill<0>(W, xp, sel, D, [&](auto T) { U[T] = ld128<AUX_NT>(ru, g * (16 * 8192) + c * 16, (64 * w + T) * 8192 + h * 256); });
#pragma unroll
      for (int r = 0; r < 4; ++r) red[(w * 2 + (c >> 3)) * 128 + 32 * g + 8 * r + (c & 7)] = D[r];
    }
    LD_EPOCH();
    const u32x4 fy = ld128<A_PRE>(rws, fy_off, WS_Y);
    const u32x4 oy0 = ld128<A_PRE>(rws, c * 16, WS_Y + (h * 4 + 3) * 2048 + w * 256), oy1 = ld128<A_PRE>(rws, c * 16, WS_Y + (128 + h * 4 + 3) * 2048 + w * 256);
    LD_GATE();
    SB();
    bar();
#pragma unroll
    for (int bb = 0; bb < 2; ++bb) {
      u32x4 q; QREAD(q, bb);
      float xs[16];
#pragma unroll
      for (int t = 0; t < 16; ++t) xs[t] = dot8(U[t], q);
      const float qa = treduce<16>(xs, c);
      *(u16*)(lds + L_RING + bb * 1024 + tid * 2) = (u16)pk_bf16(qa, qa);
    }
    SB();
    USE_EPOCH(); USE_GATE();
    fold = lsb4(fy);
    unsigned old = lsb4(oy0) | (lsb4(oy1) << 4);
    asm volatile("" : "+v"(fold), "+v"(gate), "+v"(old));
    tagr = tag0;
    DRAIN();
    bar();
    if (w == 0) {
      __builtin_amdgcn_raw_buffer_store_b128(*(const u32x4*)(lds + L_RING + lane * 16), rws, lane * 16, WS_QA + h * 1024, A_ST);
      __builtin_amdgcn_raw_buffer_store_b128(*(const u32x4*)(lds + L_RING + 1024 + lane * 16), rws, lane * 16, WS_QA + (32 + h) * 1024, A_ST);
      DRAIN();
      if (lane == 0) __hip_atomic_store((GAS u64*)(ws + WS_F1) + h, tag0, RLX_AGENT);
    }
    STAMP(1);
    {
      u32x2 R[32];
      LD_OUTW(R, w_o, OFFM & 16);
      LD_ROWS(W, w_uv, OFFM & 8);
      SB();
      transpose_w(R, U);
    }
#pragma unroll
    for (int i = 0; i < 16; ++i) asm volatile("" : "+v"(W[i]), "+v"(U[i]));
    STAMP(2);
    if (w == 0) {
      if (OFFM & 2048) { while (__builtin_amdgcn_s_memrealtime() - t_start < 3900) __builtin_amdgcn_s_sleep(8); }
      wait_flags32((const GAS u64*)(ws + WS_F2), tag0, lane);
      if (lane < 2) __hip_atomic_store((GAS u64*)(ws + WS_GO) + h * 16 + lane, lane ? tag1 : tag0, RLX_AGENT);
    }
    bar();
    STAMP(3);
    u32x4 T[4];
#pragma unroll
    for (int k = 0; k < 4; ++k) T[k] = ld128<PB_LD>(mk_rsrc(ws, (OFFM & 32) ? 0 : WS_END), lane * 16, WS_PB + (((k >> 1) * 32 + h) * 16 + 2 * w + (k & 1)) * 1024);
    const u32x2 mlw = ld64<PB_LD>(rws, (g & 1) * 4096 + c * 8, WS_ML + h * 128);
    const float ms = u2f(mlw[0]), M0 = row_max(ms), M = (M0 == -INFINITY) ? 0.f : M0;
    const float wt = ex2((ms - M) * LOG2E) * u2f(mlw[1]), cf = wt / row_sum(wt);
    float* cr = (float*)(lds + L_RING);
#pragma unroll
    for (int bb = 0; bb < 2; ++bb) {
      const float f0 = u2f(__builtin_amdgcn_readlane(f2u(cf), 16 * bb + 2 * w)), f1 = u2f(__builtin_amdgcn_readlane(f2u(cf), 16 * bb + 2 * w + 1));
      float a[8];
#pragma unroll
      for (int j = 0; j < 4; ++j) {
        a[2 * j] = f0 * u2f(T[2 * bb][j] << 16) + f1 * u2f(T[2 * bb + 1][j] << 16);
        a[2 * j + 1] = f0 * u2f(T[2 * bb][j] & 0xffff0000u) + f1 * u2f(T[2 * bb + 1][j] & 0xffff0000u);
      }
      *(f32x4*)(cr + (w * 2 + bb) * 512 + lane * 8) = f32x4{a[0], a[1], a[2], a[3]};
      *(f32x4*)(cr + (w * 2 + bb) * 512 + lane * 8 + 4) = f32x4{a[4], a[5], a[6], a[7]};
    }
    bar();
    {
      float c0 = 0.f, c1 = 0.f;
#pragma unroll
      for (int ww = 0; ww < 8; ++ww) { c0 += cr[ww * 1024 + tid]; c1 += cr[ww * 1024 + 512 + tid]; }
      const unsigned h0 = hilo(c0), h1 = hilo(c1);
      f32x4 D = {0.f, 0.f, 0.f, 0.f};
      rowsum<16, 1>(W, __builtin_amdgcn_perm(h1, h0, 0x05040100u), sel, D);
      rowsum<16, 1>(W, __builtin_amdgcn_perm(h1, h0, 0x07060302u), sel, D);
#pragma unroll
      for (int r = 0; r < 4; ++r) red[(w * 2 + (c >> 3)) * 128 + 32 * g + 8 * r + (c & 7)] = D[r];
    }
    bar();
    if (tid < 256) {
      float v = 0.f;
#pragma unroll
      for (int ww = 0; ww < 8; ++ww) v += red[ww * 256 + tid];
      *(u16*)(lds + L_P + (tid >> 7) * 272 + (tid & 127) * 2) = (u16)pk_bf16(v, v);
    }
    bar();
    f32x4 D[4];
    project(U, lds, g, c & 1, D);
    if (g == 0) {
      __builtin_amdgcn_raw_buffer_store_b128(u32x4{mark(D[0][0], old, 0), mark(D[1][0], old, 1), mark(D[2][0], old, 2), mark(D[3][0], old, 3)}, rws, c * 16, WS_Y + (h * 4 + 3) * 2048 + w * 256, A_ST);
      __builtin_amdgcn_raw_buffer_store_b128(u32x4{mark(D[0][1], old, 4), mark(D[1][1], old, 5), mark(D[2][1], old, 6), mark(D[3][1], old, 7)}, rws, c * 16, WS_Y + (128 + h * 4 + 3) * 2048 + w * 256, A_ST);
    }
    STAMP(4);
  } else {
    const int b = rs & 1, sp = rs >> 1;
    u32x4 W[16], S0[8], S1[8];
#define LDC(st, i) ld128<AUX_NT>(mk_rsrc(Cc, ((st) < 8 && !(OFFM & 4)) ? (16u << 20) : 0u), lane * 16 + ((i) & 3) * 1024, (b * 8192 + sp * 512 + ((st) & 7) * 64 + 8 * w + ((i) & 4)) * 1024)
#define LDS_(S, st) _Pragma("unroll") for (int i = 0; i < 8; ++i) S[i] = LDC(st, i);
#define STC(slot, S) { int ln = lane; asm volatile("" : "+v"(ln)); char* base = lds + L_RING + (slot) * 65536 + (ln >> 4) * 16384 + 8 * w * 256; \
      _Pragma("unroll") for (int i = 0; i < 8; ++i) *(u32x4*)(base + i * 256 + 16 * ((ln & 15) ^ swz16(8 * w + i))) = S[i]; }
    LD_EPOCH();
    const u32x4 fy = ld128<A_PRE>(rws, fy_off, WS_Y);
    LD_GATE();
    LDS_(S0, 0);
    LDS_(S1, 1);
    SB();
    USE_EPOCH(); USE_GATE();
    fold = lsb4(fy);
    asm volatile("" : "+v"(fold), "+v"(gate));
    tagr = tag1;
    STC(0, S0);
    SB();
    LDS_(S0, 2);
    SB();
    STC(1, S1);
    SB();
    LDS_(S1, 3);
    PIN();
    STAMP(1);
    if (w == 0) wait_flags32((const GAS u64*)(ws + WS_F1), tag0, lane);
    bar();
#pragma unroll
    for (int k = 0; k < 4; ++k) *(u32x4*)(lds + L_QA + (4 * w + k) * 1024 + 16 * (lane ^ swz16(4 * w + k))) = ld128<A_QA>(rws, lane * 16, WS_QA + (b * 32 + 4 * w + k) * 1024);
    bar();
    const int kt = w & 3, hh = w >> 2;
#pragma unroll
    for (int s = 0; s < 16; ++s) W[s] = *(const u32x4*)(lds + L_QA + (16 * hh + c) * 1024 + 16 * ((4 * s + g) ^ swz16(c)));
#pragma unroll
    for (int s = 0; s < 16; ++s) asm volatile("" : "+v"(W[s]));
    bar();
    STAMP(2);
    float mq = -INFINITY, lq = 0.f, mp = -INFINITY;
    f32x16 A0, A1;
#pragma unroll
    for (int i = 0; i < 16; ++i) { A0[i] = 0.f; A1[i] = 0.f; }
    float* mx = (float*)(lds + L_MX);
#define STAGE(slot) { const char* tile = lds + L_RING + (slot) * 65536; \
      int ln = lane; asm volatile("" : "+v"(ln)); \
      const int g = ln >> 4, c = ln & 15, row = 16 * kt + c, swz = swz16(row); \
      const int h5 = ln >> 5, blk = (ln >> 4) & 1, q4 = (ln & 15) >> 2, p4 = ln & 3, hq = 16 * hh + c, hp = ln & 31; \
      f32x4 X = {0.f, 0.f, 0.f, 0.f}; \
      _Pragma("unroll") for (int s = 0; s < 16; ++s) X = mfma16(*(const u32x4*)(tile + (s >> 2) * 16384 + row * 256 + 16 * ((4 * (s & 3) + g) ^ swz)), W[s], X); \
      float sc[4]; \
      _Pragma("unroll") for (int r = 0; r < 4; ++r) sc[r] = bfr(bfr(X[r]) * SCALE); \
      const float tm = xrow_max(fmaxf(fmaxf(sc[0], sc[1]), fmaxf(sc[2], sc[3]))); \
      if (g == 0) mx[kt * 32 + hq] = tm; \
      bar(); \
      { const float mn = fmaxf(fmaxf(mq, fmaxf(mx[hq], mx[32 + hq])), fmaxf(mx[64 + hq], mx[96 + hq])), ms = (mn == -INFINITY) ? 0.f : mn; \
        float p[4]; \
        _Pragma("unroll") for (int r = 0; r < 4; ++r) p[r] = ex2((sc[r] - ms) * LOG2E); \
        lq = lq * ex2((mq - ms) * LOG2E) + ((p[0] + p[1]) + (p[2] + p[3])); \
        mq = mn; \
        *(u32x2*)(lds + L_P + hq * 144 + (16 * kt + 4 * g) * 2) = u32x2{pk_bf16(p[0], p[1]), pk_bf16(p[2], p[3])}; } \
      { const float mn = fmaxf(fmaxf(mp, fmaxf(mx[hp], mx[32 + hp])), fmaxf(mx[64 + hp], mx[96 + hp])), ms = (mn == -INFINITY) ? 0.f : mn; \
        const float al = ex2((mp - ms) * LOG2E); mp = mn; A0 *= al; A1 *= al; } \
      bar(); \
      _Pragma("unroll") for (int ks = 0; ks < 4; ++ks) { \
        const u32x4 P = *(const u32x4*)(lds + L_P + hp * 144 + (16 * ks + 8 * h5) * 2); \
        s16x4 f[2][2]; \
        _Pragma("unroll") for (int ct = 0; ct < 2; ++ct) _Pragma("unroll") for (int tt = 0; tt < 2; ++tt) { \
          const int r2 = 16 * ks + 8 * h5 + 4 * tt + q4, ch = 4 * (2 * (w & 1) + ct) + 2 * blk + (p4 >> 1); \
          f[ct][tt] = __builtin_amdgcn_ds_read_tr16_b64_v4i16((LAS s16x4*)(tile + (w >> 1) * 16384 + r2 * 256 + 16 * (ch ^ swz16(r2)) + 8 * (p4 & 1))); } \
        A0 = mfma32(__builtin_bit_cast(u32x4, __builtin_shufflevector(f[0][0], f[0][1], 0, 1, 2, 3, 4, 5, 6, 7)), P, A0); \
        A1 = mfma32(__builtin_bit_cast(u32x4, __builtin_shufflevector(f[1][0], f[1][1], 0, 1, 2, 3, 4, 5, 6, 7)), P, A1); } \
      bar(); }
#pragma unroll 1
    for (int it = 0; it < 4; ++it) {
      STAGE(0);
      if (it == 0) STAMP(8);
      STC(0, S0); SB(); LDS_(S0, 2 * it + 4); SB();
      STAGE(1);
      if (it == 0) STAMP(9);
      STC(1, S1); SB(); LDS_(S1, 2 * it + 5); SB();
      STAMP(10 + it);
    }
    const int h5 = lane >> 5, hq = 16 * hh + c, hp = lane & 31;
    float* lx = (float*)(lds + L_ML);
    lq = xrow_sum(lq);
    if (g == 0) lx[kt * 32 + hq] = lq;
    bar();
    const float lt = (lx[hp] + lx[32 + hp]) + (lx[64 + hp] + lx[96 + hp]);
    const float inv = 1.f / lt;
    char* img = lds + L_RING + hp * 1040 + (64 * w + 4 * h5) * 2;
#pragma unroll
    for (int k = 0; k < 4; ++k) {
      *(u32x2*)(img + 16 * k) = u32x2{pk_bf16(A0[4 * k] * inv, A0[4 * k + 1] * inv), pk_bf16(A0[4 * k + 2] * inv, A0[4 * k + 3] * inv)};
      *(u32x2*)(img + 64 + 16 * k) = u32x2{pk_bf16(A1[4 * k] * inv, A1[4 * k + 1] * inv), pk_bf16(A1[4 * k + 2] * inv, A1[4 * k + 3] * inv)};
    }
    if (tid < 32) __builtin_amdgcn_raw_buffer_store_b64(u32x2{f2u(mp), f2u(lt)}, mk_rsrc(ws, (OFFM & 512) ? 0 : WS_END), hp * 128, WS_ML + b * 4096 + sp * 8, AUX_SC1);
    bar();
#pragma unroll
    for (int k = 0; k < 4; ++k)
      __builtin_amdgcn_raw_buffer_store_b128(*(const u32x4*)(lds + L_RING + (4 * w + k) * 1040 + lane * 16), mk_rsrc(ws, (OFFM & 32) ? 0 : WS_END), lane * 16, WS_PB + ((b * 32 + 4 * w + k) * 16 + sp) * 1024, PB_ST);
    DRAIN();
    bar();
    if (tid == 0) __hip_atomic_store((GAS u64*)(ws + WS_F2) + rs, tag0, RLX_AGENT);
    STAMP(3); STAMP(4);
  }
  STAMP(5);
  {
    const bool isb = (lane & 12) == 12;
    float* fr = (float*)(lds + L_RING);
    u32x4 y = {0u, 0u, 0u, 0u};
#define POLL_Y(SLP) _Pragma("unroll 1") for (unsigned spins = 0; spins < SPIN_CAP; ++spins) { \
      y = ld128<A_POLL>(rws, fy_off, WS_Y); if ((lsb4(y) ^ fold) == 15u) break; __builtin_amdgcn_s_sleep(SLP); }
#define SUM_ROWS(v) { const float a0 = v[0] + dppf<ROR8>(v[0]), a1 = v[1] + dppf<ROR8>(v[1]), a2 = v[2] + dppf<ROR8>(v[2]), a3 = v[3] + dppf<ROR8>(v[3]); \
      const float b0 = (c & 8) ? a2 : a0, b1 = (c & 8) ? a3 : a1; \
      const float c0 = b0 + dppf<0x124>(b0), c1 = b1 + dppf<0x12C>(b1); \
      v[0] = xrow_sum((c & 4) ? c0 : c1); }
    if (isb) { POLL_Y(16); }
    {
      float v[4];
#pragma unroll
      for (int j = 0; j < 4; ++j) v[j] = u2f(y[j] & ~0xffu);
      SUM_ROWS(v);
      if (lane < 16) fr[128 + w * 16 + lane] = v[0];
      if (lane == 0) *(unsigned*)(lds + L_FIN + w * 4) = fold;
    }
    bar();
    float bpart = 0.f;
    if (w == 0) {
#pragma unroll
      for (int ww = 0; ww < 8; ++ww) bpart += fr[128 + ww * 16 + c];
      bpart = bfr(bfr(1.f - gate) * bfr(bpart));
      const int po = (rb * 128 + 16 * (lane & 7)) * 2048 + rs * 64;
      const unsigned po_old = *(const unsigned*)(lds + L_FIN + (lane & 7) * 4);
#pragma unroll 1
      for (unsigned spins = 0; spins < SPIN_CAP; ++spins) {
        if (__any((ld32<A_POLL>(rws, po, WS_Y) ^ po_old) & 1u)) break;
        __builtin_amdgcn_s_sleep(SSLEEP);
      }
    }
    GAS u16* op = (GAS u16*)out + rb * 512 + rs * 16 + 4 * (c & 3) + (((c >> 2) & 2) | (((c >> 2) & 1) ^ 1));
    asm volatile("" : "+v"(bpart), "+v"(op));
    bar();
    y = u32x4{0u, 0u, 0u, 0xfeu};
    if (!isb) { POLL_Y(FSLEEP); }
    STAMP(6);
    const float lse = u2f((((y[0] >> 1) & 0x7fu) | (((y[1] >> 1) & 0x7fu) << 7) | (((y[2] >> 1) & 0x7fu) << 14) | (((y[3] >> 1) & 0x7fu) << 21)) << 4);
    float M = fmaxf(lse, dppf<ROR8>(lse)); M = fmaxf(M, dppf<ROR4>(M));
    const float e = isb ? 0.f : ex2(lse - M);
    float L = e + dppf<ROR8>(e); L += dppf<ROR4>(L);
    const float cf = e * __builtin_amdgcn_rcpf(L);
    float v[4];
#pragma unroll
    for (int j = 0; j < 4; ++j) v[j] = cf * u2f(y[j] & ~0xffu);
    SUM_ROWS(v);
    if (lane < 16) fr[w * 16 + lane] = v[0];
    bar();
    if (tid < 16) {
      const float a = ((fr[tid] + fr[16 + tid]) + (fr[32 + tid] + fr[48 + tid])) + ((fr[64 + tid] + fr[80 + tid]) + (fr[96 + tid] + fr[112 + tid]));
      const float o = bfr(gate * bfr(a)) + bpart;
      *op = (u16)pk_bf16(o, o);
    }
    if (rs == 0 && tid == 0) __hip_atomic_store((GAS u64*)(ws + WS_EPOCH + rb * 128), tagr, RLX_AGENT);
  }
  STAMP(7);
    return;
  }
  {
    const int h = __builtin_amdgcn_readfirstlane(HMAP ? x8 + 8 * ((idx / 6 + HROT) & 3) : 4 * x8 + ((idx / 6 + HROT) & 3)), b = __builtin_amdgcn_readfirstlane((idx % 6) / 3), sp = __builtin_amdgcn_readfirstlane(idx % 3);
    const int src = b * 128 + h * 4 + sp;
    constexpr int NT = 2048 / NL;
    const u32x4 sel = make_sel(c);
    u32x4 W[16], kb[NL], vb[NL];
    const rsrc_t ro = mk_rsrc(wo, (OFFM & 128) ? 0 : 4096 * 512 * 2);
    unsigned old;
    const int wr = __builtin_amdgcn_readfirstlane((w + (WROT ? (idx % 6) * 4 / 3 : 0)) & 7);
#define LD_SMALL() unsigned x0 = x[64 * wr + lane], x1 = x[512 + 64 * wr + lane]; LD_EPOCH(); \
    old = ld32<A_PRE>(rws, lane * 4, WS_Y + src * 2048 + w * 256); unsigned touch = ld32<AUX_NT>(ro, 0, h * 131072);
    const rsrc_t rk = mk_rsrc(Kc, 128u << 20), rv = mk_rsrc(Vc, 128u << 20);
    const int t1 = (b * 32 + h) * NT + NT * (sp + 1) / 3;
    int t = (b * 32 + h) * NT + NT * sp / 3 + w;
#define LDT(r, tt, i) ld128<AUX_NT>(r, lane * 16 + ((i) & 3) * 1024, (tt) * (NL * 1024) + ((i) >> 2) * 4096)
#define LDKV(a, r, tt) _Pragma("unroll") for (int i = 0; i < NL; ++i) a[i] = LDT(r, tt, i);
    u32x4 kb2[NL], vb2[NL];
    unsigned coop = 0;
    if (COOP) {
      const int ln = (idx * 8 + w) * 22 + lane;
      coop = ld32<AUX_NT>(mk_rsrc(wq, 512 * 4096 * 2), (lane < 22 && ln < 4096) ? (ln >> 3) * 8192 + (ln & 7) * 128 : 0x7fffffff, x8 * 1024);
    }
    LD_SMALL();
#define LD_FIRST(T) { constexpr int i = (T) & 3; \
        if constexpr ((T) < 4) kb2[i] = LDT(rk, t, i); else if constexpr ((T) < 8) vb2[i] = LDT(rv, t, i); \
        else if constexpr ((T) < 12) kb[i] = LDT(rk, t + 8, i); else vb[i] = LDT(rv, t + 8, i); }
    {
      const rsrc_t rq = mk_rsrc(wq, (OFFM & 64) ? 0 : 512 * 4096 * 2);
      static_for<16>([&](auto T) {
        W[T] = ld128<AUX_NT>(rq, g * (16 * 8192) + c * 16, (64 * wr + T) * 8192 + h * 256);
        if constexpr (T < ILV) { SB(); LD_FIRST(T); SB(); } });
    }
    SB();
    USE_EPOCH();
    u64 tag = b ? tag1 : tag0;
    asm volatile("" : "+s"(tag));
    asm volatile("" : "+v"(old), "+v"(x0), "+v"(x1) : "v"(touch), "v"(coop));
    {
      f32x4 D = {0.f, 0.f, 0.f, 0.f};
      rowsum_refill<0>(W, x0 | (x1 << 16), sel, D, [&](auto T) { if constexpr (T + ILV < 16) LD_FIRST(T + ILV); });
#pragma unroll
      for (int r = 0; r < 4; ++r) red[(wr * 2 + (c >> 3)) * 128 + 32 * g + 8 * r + (c & 7)] = D[r];
    }
    t += 8;
    bar();
    u32x4 q;
    QREAD(q, b);
    STAMP(1);
    float m = -INFINITY, l = 0.f;
    f32x4 O = {0.f, 0.f, 0.f, 0.f};
#define SOFTMAX(xs) const float s = bfr(bfr(treduce<NL>(xs, c)) * SCALE); \
      const float mn = fmaxf(m, xrow_max(row_max(s))), ms = (mn == -INFINITY) ? 0.f : mn; \
      const float al = ex2((m - ms) * LOG2E), p = ex2((s - ms) * LOG2E); \
      m = mn; l = l * al + p; O *= al; unsigned hl = hilo(p);
#define ARRIVED(a) _Pragma("unroll") for (int i = 0; i < NL; ++i) asm volatile("" : "+v"(a[i]));
#define SCORES(xs, kk) float xs[NL]; _Pragma("unroll") for (int i = 0; i < NL; ++i) xs[i] = dot8(kk[i], q);
#define TILE(WITH_V) { if (WITH_V) { ARRIVED(kb); SB(); LDKV(vb, rv, t); SB(); } \
      SCORES(xs, kb); SOFTMAX(xs); asm volatile("" : "+v"(hl), "+v"(O), "+v"(l)); \
      t += 8; SB(); ARRIVED(vb); SB(); LDKV(kb, rk, t); SB(); \
      rowsum<NL, 16 / NL>(vb, hl, sel, O); asm volatile("" : "+v"(O)); SB(); TLREC(); }
#define TILE2(EXTRA) { ARRIVED(kb); SB(); LDKV(vb, rv, t); SB(); \
      SCORES(xs, kb); ARRIVED(xs); t += 8; SB(); LDKV(kb, rk, t); SB(); EXTRA; SB(); \
      SOFTMAX(xs); rowsum<NL, 16 / NL>(vb, hl, sel, O); asm volatile("" : "+v"(O), "+v"(l)); SB(); TLREC(); }
#define TILE_NOLOAD(kk, vv) { SCORES(xs, kk); SOFTMAX(xs); rowsum<NL, 16 / NL>(vv, hl, sel, O); asm volatile("" : "+v"(O), "+v"(l)); SB(); TLREC(); }
#define TLREC()
#define TILE_DEEP() { SCORES(xs, kb); ARRIVED(xs); t += 8; SB(); LDKV(kb, rk, t); SB(); \
      SOFTMAX(xs); rowsum<NL, 16 / NL>(vb, hl, sel, O); asm volatile("" : "+v"(O), "+v"(l)); SB(); LDKV(vb, rv, t); SB(); TLREC(); }
    TILE_DEEP();
    TILE_NOLOAD(kb2, vb2);
    {
      const int ndeep = ((h & 3) == 1) ? DEEP1 : DEEP0;
#pragma unroll 1
      for (int k = 0; k < ndeep; ++k) TILE_DEEP();
    }
    TILE(false);
    const unsigned img = (unsigned)(uintptr_t)(LAS char*)(lds + L_RING) + w * 16384;
    int dvo = (lane >> 3) * 1024 + 16 * ((lane & 7) ^ ((lane >> 4 & 1) << 1));
#pragma unroll 1
    for (int k = 0; k < 16; k += 16 / NDMA) {
#define DMA_STEP() _Pragma("unroll") for (int j = 0; j < 16 / NDMA; ++j) { \
        unsigned keep; \
        asm volatile("s_mov_b32 %0, m0\n\ts_mov_b32 m0, %1\n\ts_nop 4\n\tbuffer_load_dwordx4 %2, %3, %4 offen nt lds\n\ts_mov_b32 m0, %0" : "=&s"(keep) \
                     : "s"(__builtin_amdgcn_readfirstlane(img + (k + j) * 1024)), "v"(dvo), "s"(ro), "s"(__builtin_amdgcn_readfirstlane((h * 128 + 8 * (k + j)) * 1024 + w * 128)) : "memory"); \
        dvo ^= 64; }
      if constexpr (M2 & 1) TILE2(DMA_STEP()) else { TILE(true); DMA_STEP(); }
      SB();
    }
    TILE(true);
    asm volatile("s_waitcnt vmcnt(%0)" :: "i"(NL) : "memory");
    {
      const int q4 = c >> 2, p4 = lane & 3, phi = (q4 >> 1) | ((g & 1) << 1);
#pragma unroll
      for (int s = 0; s < 4; ++s)
#pragma unroll
        for (int ct = 0; ct < 4; ++ct) {
          const char* a = lds + L_RING + w * 16384 + (32 * s + 8 * g + q4) * 128 + 16 * ((p4 >> 1) | ((ct ^ phi) << 1)) + 8 * (p4 & 1);
          const s16x4 lo = __builtin_amdgcn_ds_read_tr16_b64_v4i16((LAS s16x4*)a), hi = __builtin_amdgcn_ds_read_tr16_b64_v4i16((LAS s16x4*)(a + 512));
          W[4 * s + ct] = __builtin_bit_cast(u32x4, __builtin_shufflevector(lo, hi, 0, 1, 2, 3, 4, 5, 6, 7));
        }
    }
#pragma unroll
    for (int i = 0; i < 16; ++i) asm volatile("" : "+v"(W[i]));
    const GAS u64* go = (const GAS u64*)(ws + WS_GO) + h * 16 + b;
#pragma unroll 1
    while (t + 24 < t1) { if constexpr (M2 & 2) TILE2(;) else TILE(true); }
    u64 gov;
    {
      u32x4 vb3[NL];
      ARRIVED(kb); SB();
      LDKV(vb, rv, t); SB(); LDKV(kb2, rk, t + 8); SB(); LDKV(vb2, rv, t + 8);
      SB();
      SCORES(xs, kb);
      ARRIVED(xs); SB();
      LDKV(kb, rk, t + 16); SB(); LDKV(vb3, rv, t + 16);
      gov = __hip_atomic_load(go, RLX_AGENT);
      SB();
      SOFTMAX(xs); rowsum<NL, 16 / NL>(vb, hl, sel, O); asm volatile("" : "+v"(O), "+v"(l)); SB(); TLREC();
      TILE_NOLOAD(kb2, vb2);
      TILE_NOLOAD(kb, vb3);
    }
    STAMP(2);
    l = xrow_sum(row_sum(l)) * (NL / 16.f);
    float* ml = (float*)(lds + L_ML);
#pragma unroll
    for (int r = 0; r < 4; ++r) {
      const unsigned hl = hilo(O[r] + dppf<ROR8>(O[r]));
      *(u16*)(lds + L_P + (2 * w + (c >> 3)) * 272 + (32 * g + 8 * r + (c & 7)) * 2) = (u16)((c & 8) ? (hl >> 16) : hl);
    }
    if (lane == 0) { ml[w] = m; ml[8 + w] = l; }
    bar();
    STAMP(4);
    f32x4 D[4];
    const float mj = ml[lane & 7], lj = ml[8 + (lane & 7)], ma = ml[2 * g], mb = ml[2 * g + 1];
    project(W, lds, g, c, D);
    float M = fmaxf(mj, dppf<XOR7>(mj)); M = fmaxf(M, dppf<XOR2>(M)); M = fmaxf(M, dppf<XOR1>(M));
    const float Ms = (M == -INFINITY) ? 0.f : M;
    float L = ex2((mj - Ms) * LOG2E) * lj; L += dppf<XOR7>(L); L += dppf<XOR2>(L); L += dppf<XOR1>(L);
    const float inv = __builtin_amdgcn_rcpf(L), fa = ex2((ma - Ms) * LOG2E) * inv, fb = ex2((mb - Ms) * LOG2E) * inv;
    float y4[4];
#pragma unroll
    for (int k = 0; k < 4; ++k) y4[k] = fa * (D[k][0] + D[k][1]) + fb * (D[k][2] + D[k][3]);
    const auto s0 = __builtin_amdgcn_permlane32_swap(f2u(y4[0]), f2u(y4[2]), false, false), s1 = __builtin_amdgcn_permlane32_swap(f2u(y4[1]), f2u(y4[3]), false, false);
    const auto s2 = __builtin_amdgcn_permlane16_swap(f2u(u2f(s0[0]) + u2f(s0[1])), f2u(u2f(s1[0]) + u2f(s1[1])), false, false);
    const unsigned code = f2u(Ms * LOG2E + __builtin_amdgcn_logf(L)) >> 4;
    const unsigned yw = (f2u(u2f(s2[0]) + u2f(s2[1])) & ~0xffu) | (((code >> (7 * (lane & 3))) & 0x7fu) << 1) | ((old & 1u) ^ 1u);
    const u32x4 yv = {yw, f2u(dppf<0x55>(u2f(yw))), f2u(dppf<0xAA>(u2f(yw))), f2u(dppf<0xFF>(u2f(yw)))};
#pragma unroll 1
    for (unsigned spins = 0; !(OFFM & 256) && uniform64(gov) != tag && spins < SPIN_CAP; ++spins) gov = __hip_atomic_load(go, RLX_AGENT);
    if ((lane & 3) == 0) __builtin_amdgcn_raw_buffer_store_b128(yv, rws, lane * 4, WS_Y + src * 2048 + w * 256, A_YST);
    STAMP(5); STAMP(6); STAMP(3);
  }
}

extern "C" void kernel_launch(void* const* d_in, const int* in_sizes, int n_in, void* d_out, int out_size, void* d_ws, size_t ws_size, hipStream_t stream) {
  fused_decode<<<256, 512, 0, stream>>>((const u16*)d_in[0], (const u16*)d_in[1], (const u16*)d_in[2], (const u16*)d_in[3], (const u16*)d_in[4],
                                        (const u16*)d_in[6], (const u16*)d_in[7], (const u16*)d_in[8], (const u16*)d_in[9], (const u16*)d_in[10],
                                        (const u16*)d_in[11], (u16*)d_out, (char*)d_ws);
}
```

```cpp
#include <hip/hip_runtime.h>
#include <stdint.h>
#include <type_traits>

typedef unsigned short u16;
typedef unsigned long long u64;
typedef unsigned u32x4 __attribute__((ext_vector_type(4)));
typedef unsigned u32x2 __attribute__((ext_vector_type(2)));
typedef float f32x4 __attribute__((ext_vector_type(4)));
typedef float f32x16 __attribute__((ext_vector_type(16)));
typedef __bf16 bf16x8 __attribute__((ext_vector_type(8)));
typedef __bf16 bf16x2 __attribute__((ext_vector_type(2)));
typedef short s16x4 __attribute__((ext_vector_type(4)));
typedef __amdgpu_buffer_rsrc_t rsrc_t;
#define GAS __attribute__((address_space(1)))
#define LAS __attribute__((address_space(3)))
#define DI __device__ __forceinline__
#define RLX_AGENT __ATOMIC_RELAXED, __HIP_MEMORY_SCOPE_AGENT

#define NL 4
#define OFFM 0
#define TL 0
#define STAG 0
#define NDMA 8
#define SSLEEP 24
#define PB_ST 18
#define PB_LD 18
#define BOOST 1
#define EBAR 0
#define HROT 0
#define HMAP 0
#define DEEP1 0
#define DEEP0 0
#define WROT 1
#define TROT 0
#define ILV 0
#define COOP 0
#define PRE4 2
#define M2 0
#define A_PRE 18
#define A_QA 16
#define A_ST 16
#define A_POLL 16
#define A_YST 16
#define WSLEEP 3
#define OUTSC1 0
#define BDELAY 0
#define YPRE 4
#define FSLEEP 0
#define STAMPS 0
constexpr int AUX_NT = 2, AUX_SC1 = 16;
constexpr float SCALE = 0.08838834764831845f;
constexpr float LOG2E = 1.4426950408889634f;

constexpr int WS_EPOCH = 0;
constexpr int WS_F1 = 1024;
constexpr int WS_F2 = 2048;
constexpr int WS_GO = 4096;
constexpr int WS_QA = 8192;
constexpr int WS_ML = WS_QA + 65536;
constexpr int WS_PB = WS_ML + 8192;
constexpr int WS_Y = WS_PB + 2 * 32 * 16 * 1024;
constexpr int WS_END = WS_Y + 2 * 128 * 2048;
constexpr int WS_STAMP = 4 << 20;

constexpr int L_RING = 0;
constexpr int L_RED = 131072;
constexpr int L_P = L_RED + 8192;
constexpr int L_MX = L_P + 32 * 144;
constexpr int L_ML = L_MX + 512;
constexpr int L_FIN = L_ML + 512;
constexpr int L_QA = 131072;
constexpr int L_END = 163840;

DI rsrc_t mk_rsrc(const void* p, unsigned bytes) { return __builtin_amdgcn_make_buffer_rsrc((void*)p, 0, bytes, 0x00020000); }
template <int AUX> DI u32x4 ld128(rsrc_t r, int voff, int soff) { return __builtin_amdgcn_raw_buffer_load_b128(r, voff, soff, AUX); }
template <int AUX> DI u32x2 ld64(rsrc_t r, int voff, int soff) { return __builtin_amdgcn_raw_buffer_load_b64(r, voff, soff, AUX); }
template <int AUX> DI unsigned ld32(rsrc_t r, int voff, int soff) { return __builtin_amdgcn_raw_buffer_load_b32(r, voff, soff, AUX); }
DI float u2f(unsigned u) { return __builtin_bit_cast(float, u); }
DI unsigned f2u(float f) { return __builtin_bit_cast(unsigned, f); }
DI unsigned pk_bf16(float lo, float hi) { bf16x2 r; r[0] = (__bf16)lo; r[1] = (__bf16)hi; return __builtin_bit_cast(unsigned, r); }
DI float bfr(float x) { return u2f(pk_bf16(x, x) << 16); }
DI unsigned hilo(float x) { const float h = bfr(x); return pk_bf16(h, x - h); }
DI float ex2(float x) { return __builtin_amdgcn_exp2f(x); }
DI float dot2(unsigned a, unsigned b, float c) { return __builtin_amdgcn_fdot2_f32_bf16(__builtin_bit_cast(bf16x2, a), __builtin_bit_cast(bf16x2, b), c, false); }
DI float dot8(const u32x4& a, const u32x4& b) { return dot2(a[3], b[3], dot2(a[2], b[2], dot2(a[1], b[1], dot2(a[0], b[0], 0.f)))); }
template <int CTRL> DI float dppf(float x) { return u2f(__builtin_amdgcn_mov_dpp(f2u(x), CTRL, 0xf, 0xf, true)); }
template <int T> DI unsigned bcast16(unsigned v) { return __builtin_amdgcn_mov_dpp(v, 0x150 + T, 0xf, 0xf, true); }
constexpr int XOR1 = 0xB1, XOR2 = 0x4E, XOR7 = 0x141, ROR8 = 0x128, ROR4 = 0x124;
DI float xrow_max(float x) {
  auto s = __builtin_amdgcn_permlane16_swap(f2u(x), f2u(x), false, false); x = fmaxf(u2f(s[0]), u2f(s[1]));
  auto t = __builtin_amdgcn_permlane32_swap(f2u(x), f2u(x), false, false); return fmaxf(u2f(t[0]), u2f(t[1]));
}
DI float xrow_sum(float x) {
  auto s = __builtin_amdgcn_permlane16_swap(f2u(x), f2u(x), false, false); x = u2f(s[0]) + u2f(s[1]);
  auto t = __builtin_amdgcn_permlane32_swap(f2u(x), f2u(x), false, false); return u2f(t[0]) + u2f(t[1]);
}
DI float row_max(float x) { x = fmaxf(x, dppf<ROR8>(x)); x = fmaxf(x, dppf<ROR4>(x)); x = fmaxf(x, dppf<XOR2>(x)); return fmaxf(x, dppf<XOR1>(x)); }
DI float row_sum(float x) { x += dppf<ROR8>(x); x += dppf<ROR4>(x); x += dppf<XOR2>(x); return x + dppf<XOR1>(x); }
DI f32x4 mfma16(u32x4 a, u32x4 b, f32x4 c) { return __builtin_amdgcn_mfma_f32_16x16x32_bf16(__builtin_bit_cast(bf16x8, a), __builtin_bit_cast(bf16x8, b), c, 0, 0, 0); }
DI f32x16 mfma32(u32x4 a, u32x4 b, f32x16 c) { return __builtin_amdgcn_mfma_f32_32x32x16_bf16(__builtin_bit_cast(bf16x8, a), __builtin_bit_cast(bf16x8, b), c, 0, 0, 0); }
DI u64 uniform64(u64 v) { return ((u64)(unsigned)__builtin_amdgcn_readfirstlane((unsigned)(v >> 32)) << 32) | (unsigned)__builtin_amdgcn_readfirstlane((unsigned)v); }
DI u64 next_tag(u64 e) { return e * 0x9E3779B97F4A7C15ull + 0x632BE59BD9B4E019ull; }
DI void bar() { __syncthreads(); }
#define DRAIN() asm volatile("s_waitcnt vmcnt(0)" ::: "memory")
#define PIN() asm volatile("" ::: "memory")
#define SB() __builtin_amdgcn_sched_barrier(0)
constexpr unsigned SPIN_CAP = 1u << 22;

template <int N, int STEP, int T = 0> DI void rowsum(const u32x4 (&W)[N], unsigned pair, const u32x4& sel, f32x4& D) {
  if constexpr (T < N) {
    const unsigned s = bcast16<T * STEP>(pair);
    const u32x4 B = {__builtin_amdgcn_perm(s, s, sel[0]), __builtin_amdgcn_perm(s, s, sel[1]), __builtin_amdgcn_perm(s, s, sel[2]), __builtin_amdgcn_perm(s, s, sel[3])};
    D = mfma16(W[T], B, D);
    rowsum<N, STEP, T + 1>(W, pair, sel, D);
  }
}
template <int N, int I = 0, class F> DI void static_for(F&& f) { if constexpr (I < N) { f(std::integral_constant<int, I>{}); static_for<N, I + 1>(f); } }
template <int T, class F> DI void rowsum_refill(const u32x4 (&W)[16], unsigned pair, const u32x4& sel, f32x4& D, F&& issue) {
  if constexpr (T < 16) {
    const unsigned s = bcast16<T>(pair);
    const u32x4 B = {__builtin_amdgcn_perm(s, s, sel[0]), __builtin_amdgcn_perm(s, s, sel[1]), __builtin_amdgcn_perm(s, s, sel[2]), __builtin_amdgcn_perm(s, s, sel[3])};
    D = mfma16(W[T], B, D);
    asm volatile("" : "+v"(D));
    __builtin_amdgcn_sched_barrier(0);
    issue(std::integral_constant<int, T>{});
    __builtin_amdgcn_sched_barrier(0);
    rowsum_refill<T + 1>(W, pair, sel, D, issue);
  }
}
DI int swz16(int row) { const int u = (row >> 2) & 3; return ((row & 3) << 2) | (((u ^ (u >> 1)) & 1) << 1) | (u >> 1); }
DI u32x4 make_sel(int c) {
  const int j = c & 7; const unsigned b = (c & 8) ? 0x0302u : 0x0100u;
  const unsigned v = (j & 1) ? ((b << 16) | 0x0c0cu) : (0x0c0c0000u | b);
  u32x4 s;
#pragma unroll
  for (int w = 0; w < 4; ++w) s[w] = (w == (j >> 1)) ? v : 0x0c0c0c0cu;
  return s;
}
template <int N> DI float treduce(float (&x)[N], int c) {
#define TR_STEP(CNT, CTRL, BIT) _Pragma("unroll") for (int i = 0; i < (CNT); ++i) { const float a = x[i] + dppf<CTRL>(x[i]), b = x[i + (CNT)] + dppf<CTRL>(x[i + (CNT)]); x[i] = (c & (BIT)) ? b : a; }
  TR_STEP(N / 2, ROR8, 8)
  TR_STEP(N / 4, XOR7, 4)
  if constexpr (N >= 8) { TR_STEP(N / 8, XOR2, 2) } else { x[0] += dppf<XOR2>(x[0]); }
  if constexpr (N >= 16) { TR_STEP(N / 16, XOR1, 1) } else { x[0] += dppf<XOR1>(x[0]); }
#undef TR_STEP
  return x[0];
}
DI void wait_flags(const GAS u64* f, u64 tag) {
#pragma unroll 1
  for (unsigned spins = 0; spins < SPIN_CAP; ++spins) {
    if (__all(__hip_atomic_load(f, RLX_AGENT) == tag)) break;
    __builtin_amdgcn_s_sleep(WSLEEP);
  }
  __builtin_amdgcn_fence(__ATOMIC_ACQUIRE, "wavefront");
}
DI void transpose_w(const u32x2 (&R)[32], u32x4 (&F)[16]) {
#pragma unroll
  for (int s = 0; s < 4; ++s)
#pragma unroll
    for (int t = 0; t < 4; ++t)
#pragma unroll
      for (int jj = 0; jj < 4; ++jj) F[4 * s + t][jj] = __builtin_amdgcn_perm(R[8 * s + 2 * jj + 1][t >> 1], R[8 * s + 2 * jj][t >> 1], (t & 1) ? 0x07060302u : 0x05040100u);
}
DI void project(const u32x4 (&F)[16], const char* lds, int g, int c, f32x4 (&D)[4]) {
  u32x4 A[4];
#pragma unroll
  for (int s = 0; s < 4; ++s) A[s] = *(const u32x4*)(lds + L_P + c * 272 + 64 * s + 16 * g);
#pragma unroll
  for (int t = 0; t < 4; ++t) D[t] = f32x4{0.f, 0.f, 0.f, 0.f};
#pragma unroll
  for (int s = 0; s < 4; ++s)
#pragma unroll
    for (int t = 0; t < 4; ++t) D[t] = mfma16(A[s], F[4 * s + t], D[t]);
}
DI unsigned lsb4(const u32x4& v) { return (v[0] & 1u) | ((v[1] & 1u) << 1) | ((v[2] & 1u) << 2) | ((v[3] & 1u) << 3); }
DI unsigned mark(float v, unsigned old, int k) { return (f2u(v) & ~1u) | (((old >> k) & 1u) ^ 1u); }

#define STAMP(k) do {} while (0)

__global__ void __launch_bounds__(512, 2)
fused_decode(const u16* __restrict__ x, const u16* __restrict__ Kc, const u16* __restrict__ Vc, const u16* __restrict__ wq, const u16* __restrict__ wo,
             const u16* __restrict__ Cc, const u16* __restrict__ w_q, const u16* __restrict__ w_uk, const u16* __restrict__ w_uv, const u16* __restrict__ w_o,
             const u16* __restrict__ gw, u16* __restrict__ out, char* ws) {
  __shared__ __attribute__((aligned(16))) char lds[L_END];
  const int bid = blockIdx.x, tid = threadIdx.x, lane = tid & 63, w = __builtin_amdgcn_readfirstlane(tid >> 6), g = lane >> 4, c = lane & 15;
  const int x8 = bid & 7, idx = bid >> 3;
  STAMP(0);
  const u64 t_start = (OFFM & (1024 | 2048)) ? __builtin_amdgcn_s_memrealtime() : 0;
  asm volatile("" :: "s"(x), "s"(Kc), "s"(Vc), "s"(wq), "s"(wo), "s"(ws));
  const rsrc_t rws = mk_rsrc(ws, WS_END);
  float* red = (float*)(lds + L_RED);
#define LD_EPOCH() u64 e0 = __hip_atomic_load((GAS u64*)(ws + WS_EPOCH), RLX_AGENT), e1 = __hip_atomic_load((GAS u64*)(ws + WS_EPOCH + 128), RLX_AGENT);
#define USE_EPOCH() asm volatile("" : "+v"(e0), "+v"(e1)); const u64 tag0 = next_tag(uniform64(e0)), tag1 = next_tag(uniform64(e1));
#define LD_ROWS(W, ptr, OFF) { const rsrc_t rq = mk_rsrc(ptr, (OFF) ? 0 : 512 * 4096 * 2); \
    _Pragma("unroll") for (int t = 0; t < 16; ++t) W[t] = ld128<AUX_NT>(rq, g * (16 * 8192) + c * 16, (64 * w + t) * 8192 + h * 256); }
#define LD_OUTW(R, ptr, OFF) { const rsrc_t ro = mk_rsrc(ptr, (OFF) ? 0 : 4096 * 512 * 2); \
    _Pragma("unroll") for (int i = 0; i < 32; ++i) R[i] = ld64<AUX_NT>(ro, g * 8192 + c * 8 + (i & 3) * 1024, (h * 128 + 32 * (i >> 3) + (i & 4)) * 1024 + w * 128); }
#define QPROJ() { asm volatile("" : "+v"(x0), "+v"(x1)); f32x4 D = {0.f, 0.f, 0.f, 0.f}; rowsum<16, 1>(W, x0 | (x1 << 16), sel, D); \
    _Pragma("unroll") for (int r = 0; r < 4; ++r) red[(w * 2 + (c >> 3)) * 128 + 32 * g + 8 * r + (c & 7)] = D[r]; }
#define QREAD(q, bb) { f32x4 a0 = {0.f, 0.f, 0.f, 0.f}, a1 = a0; \
    _Pragma("unroll") for (int ww = 0; ww < 8; ++ww) { const f32x4* p = (const f32x4*)(red + (ww * 2 + (bb)) * 128 + 8 * c); a0 += p[0]; a1 += p[1]; } \
    q = u32x4{pk_bf16(a0[0], a0[1]), pk_bf16(a0[2], a0[3]), pk_bf16(a1[0], a1[1]), pk_bf16(a1[2], a1[3])}; }

  if (idx >= 24) {
  if (OFFM & 256) { STAMP(1); STAMP(2); STAMP(3); STAMP(4); STAMP(5); STAMP(6); STAMP(7); return; }
  const int rb = idx >= 28, rs = __builtin_amdgcn_readfirstlane(4 * x8 + (idx & 3));
  const int fy_off = (rb * 128 + (tid >> 2)) * 2048 + rs * 64 + (tid & 3) * 16;
  unsigned fold; u64 tagr;
  float gate;
#define LD_GATE() const u32x4 xv = *(const u32x4*)(x + rb * 512 + lane * 8), gv = *(const u32x4*)(gw + lane * 8);
#define USE_GATE() { float s = 0.f; \
    _Pragma("unroll") for (int j = 0; j < 4; ++j) s += bfr(u2f(xv[j] << 16) * u2f(gv[j] << 16)) + bfr(u2f(xv[j] & 0xffff0000u) * u2f(gv[j] & 0xffff0000u)); \
    s = bfr(xrow_sum(row_sum(s))); gate = bfr(1.f / (1.f + __expf(-s))); }

#define PHASE1(v) { const int h = rs, wr = (w + 4 * (v)) & 7; \
    const rsrc_t rq = mk_rsrc(w_q, (OFFM & 1) ? 0 : 512 * 4096 * 2), ru = mk_rsrc(w_uk, (OFFM & 2) ? 0 : 512 * 4096 * 2); \
    u32x4 Q[16], U8[8]; \
    _Pragma("unroll") for (int t = 0; t < 16; ++t) Q[t] = ld128<AUX_NT>(rq, g * (16 * 8192) + c * 16, (64 * wr + t) * 8192 + h * 256); \
    SB(); \
    asm volatile("" : "+v"(x0), "+v"(x1)); \
    f32x4 D = {0.f, 0.f, 0.f, 0.f}; \
    rowsum_refill<0>(Q, x0 | (x1 << 16), make_sel(c), D, [&](auto T) { \
      if constexpr (T < 8) U8[T] = ld128<AUX_NT>(ru, g * (8 * 8192) + c * 16, (256 * (v) + 32 * w + T) * 8192 + h * 256); }); \
    _Pragma("unroll") for (int r = 0; r < 4; ++r) red[(wr * 2 + (c >> 3)) * 128 + 32 * g + 8 * r + (c & 7)] = D[r]; \
    bar(); \
    float xs[16]; \
    _Pragma("unroll") for (int bb = 0; bb < 2; ++bb) { u32x4 q; QREAD(q, bb); _Pragma("unroll") for (int t = 0; t < 8; ++t) xs[8 * bb + t] = dot8(U8[t], q); } \
    const float qa = treduce<16>(xs, c);        \
    *(u16*)(lds + L_P + (c >> 3) * 512 + (32 * w + 8 * g + (c & 7)) * 2) = (u16)pk_bf16(qa, qa); }
#define QA_PUBLISH(v) { __builtin_amdgcn_raw_buffer_store_b128(*(const u32x4*)(lds + L_P + lane * 16), rws, (lane >> 5) * 32768 + (lane & 31) * 16, WS_QA + rs * 1024 + (v) * 512, A_ST); \
    DRAIN(); if (lane == 0) __hip_atomic_store((GAS u64*)(ws + WS_F1) + 2 * rs + (v), tag0, RLX_AGENT); }

  if (idx < 28) {
    const int h = rs;
    const u32x4 sel = make_sel(c);
    u32x4 W[16], U[16];
    asm volatile("; X" ::: "memory");
    unsigned x0 = x[tid], x1 = x[512 + tid];
    LD_EPOCH();
    const u32x4 fy = ld128<A_PRE>(rws, fy_off, WS_Y);
    const u32x4 oy0 = ld128<A_PRE>(rws, c * 16, WS_Y + (h * 4 + 3) * 2048 + w * 256), oy1 = ld128<A_PRE>(rws, c * 16, WS_Y + (128 + h * 4 + 3) * 2048 + w * 256);
    LD_GATE();
    PHASE1(0);
    SB();
    USE_EPOCH(); USE_GATE();
    fold = lsb4(fy);
    unsigned old = lsb4(oy0) | (lsb4(oy1) << 4);
    asm volatile("" : "+v"(fold), "+v"(gate), "+v"(old));
    tagr = tag0;
    bar();
    if (w == 0) QA_PUBLISH(0);
    STAMP(1);
    {
      u32x2 R[32];
      LD_OUTW(R, w_o, OFFM & 16);
      LD_ROWS(W, w_uv, OFFM & 8);
      SB();
      transpose_w(R, U);
    }
#pragma unroll
    for (int i = 0; i < 16; ++i) asm volatile("" : "+v"(W[i]), "+v"(U[i]));
    STAMP(2);
    if (w == 0) {
      if (OFFM & 2048) { while (__builtin_amdgcn_s_memrealtime() - t_start < 3900) __builtin_amdgcn_s_sleep(8); }
      wait_flags((const GAS u64*)(ws + WS_F2) + (lane & 31), tag0);
      if (lane < 2) __hip_atomic_store((GAS u64*)(ws + WS_GO) + h * 16 + lane, lane ? tag1 : tag0, RLX_AGENT);
    }
    bar();
    STAMP(3);
    u32x4 T[4];
#pragma unroll
    for (int k = 0; k < 4; ++k) T[k] = ld128<PB_LD>(mk_rsrc(ws, (OFFM & 32) ? 0 : WS_END), lane * 16, WS_PB + (((k >> 1) * 32 + h) * 16 + 2 * w + (k & 1)) * 1024);
    const u32x2 mlw = ld64<PB_LD>(rws, (g & 1) * 4096 + c * 8, WS_ML + h * 128);
    const float ms = u2f(mlw[0]), M0 = row_max(ms), M = (M0 == -INFINITY) ? 0.f : M0;
    const float wt = ex2((ms - M) * LOG2E) * u2f(mlw[1]), cf = wt / row_sum(wt);
    float* cr = (float*)(lds + L_RING);
#pragma unroll
    for (int bb = 0; bb < 2; ++bb) {
      const float f0 = u2f(__builtin_amdgcn_readlane(f2u(cf), 16 * bb + 2 * w)), f1 = u2f(__builtin_amdgcn_readlane(f2u(cf), 16 * bb + 2 * w + 1));
      float a[8];
#pragma unroll
      for (int j = 0; j < 4; ++j) {
        a[2 * j] = f0 * u2f(T[2 * bb][j] << 16) + f1 * u2f(T[2 * bb + 1][j] << 16);
        a[2 * j + 1] = f0 * u2f(T[2 * bb][j] & 0xffff0000u) + f1 * u2f(T[2 * bb + 1][j] & 0xffff0000u);
      }
      *(f32x4*)(cr + (w * 2 + bb) * 512 + lane * 8) = f32x4{a[0], a[1], a[2], a[3]};
      *(f32x4*)(cr + (w * 2 + bb) * 512 + lane * 8 + 4) = f32x4{a[4], a[5], a[6], a[7]};
    }
    bar();
    {
      float c0 = 0.f, c1 = 0.f;
#pragma unroll
      for (int ww = 0; ww < 8; ++ww) { c0 += cr[ww * 1024 + tid]; c1 += cr[ww * 1024 + 512 + tid]; }
      const unsigned h0 = hilo(c0), h1 = hilo(c1);
      f32x4 D = {0.f, 0.f, 0.f, 0.f}, D2 = D;
      rowsum<16, 1>(W, __builtin_amdgcn_perm(h1, h0, 0x05040100u), sel, D);
      rowsum<16, 1>(W, __builtin_amdgcn_perm(h1, h0, 0x07060302u), sel, D2);
      D += D2;
#pragma unroll
      for (int r = 0; r < 4; ++r) red[(w * 2 + (c >> 3)) * 128 + 32 * g + 8 * r + (c & 7)] = D[r];
    }
    bar();
    if (tid < 256) {
      float v = 0.f;
#pragma unroll
      for (int ww = 0; ww < 8; ++ww) v += red[ww * 256 + tid];
      *(u16*)(lds + L_P + (tid >> 7) * 272 + (tid & 127) * 2) = (u16)pk_bf16(v, v);
    }
    bar();
    f32x4 D[4];
    project(U, lds, g, c & 1, D);
    if (g == 0) {
      __builtin_amdgcn_raw_buffer_store_b128(u32x4{mark(D[0][0], old, 0), mark(D[1][0], old, 1), mark(D[2][0], old, 2), mark(D[3][0], old, 3)}, rws, c * 16, WS_Y + (h * 4 + 3) * 2048 + w * 256, A_ST);
      __builtin_amdgcn_raw_buffer_store_b128(u32x4{mark(D[0][1], old, 4), mark(D[1][1], old, 5), mark(D[2][1], old, 6), mark(D[3][1], old, 7)}, rws, c * 16, WS_Y + (128 + h * 4 + 3) * 2048 + w * 256, A_ST);
    }
    STAMP(4);
  } else {
    const int b = rs & 1, sp = rs >> 1;
    u32x4 W[16], S0[8], S1[8];
#define LDC(st, i) ld128<AUX_NT>(mk_rsrc(Cc, ((st) < 8 && !(OFFM & 4)) ? (16u << 20) : 0u), lane * 16 + ((i) & 3) * 1024, (b * 8192 + sp * 512 + ((st) & 7) * 64 + 8 * w + ((i) & 4)) * 1024)
#define LDS_(S, st) _Pragma("unroll") for (int i = 0; i < 8; ++i) S[i] = LDC(st, i);
#define STC(slot, S) { int ln = lane; asm volatile("" : "+v"(ln)); char* base = lds + L_RING + (slot) * 65536 + (ln >> 4) * 16384 + 8 * w * 256; \
      _Pragma("unroll") for (int i = 0; i < 8; ++i) *(u32x4*)(base + i * 256 + 16 * ((ln & 15) ^ swz16(8 * w + i))) = S[i]; }
    asm volatile("; Y" ::: "memory");
    unsigned x0 = x[(tid + 256) & 511], x1 = x[512 + ((tid + 256) & 511)];
    LD_EPOCH();
    const u32x4 fy = ld128<A_PRE>(rws, fy_off, WS_Y);
    LD_GATE();
    PHASE1(1);
    SB();
    USE_EPOCH(); USE_GATE();
    fold = lsb4(fy);
    asm volatile("" : "+v"(fold), "+v"(gate));
    tagr = tag1;
    if (w == 0) { bar(); QA_PUBLISH(1); LDS_(S0, 0); LDS_(S1, 1); }
    else { LDS_(S0, 0); LDS_(S1, 1); SB(); bar(); }
    SB();
    STC(0, S0);
    SB();
    if (YPRE >= 3) { LDS_(S0, 2); }
    SB();
    STC(1, S1);
    SB();
    if (YPRE >= 4) { LDS_(S1, 3); }
    PIN();
    STAMP(1);
    if (w == 0) wait_flags((const GAS u64*)(ws + WS_F1) + lane, tag0);
    bar();
    {
      u32x4 qa[4];
#pragma unroll
      for (int k = 0; k < 4; ++k) qa[k] = ld128<A_QA>(rws, lane * 16, WS_QA + (b * 32 + 4 * w + k) * 1024);
      SB();
      if (YPRE < 3) { LDS_(S0, 2); }
      if (YPRE < 4) { LDS_(S1, 3); }
      SB();
#pragma unroll
      for (int k = 0; k < 4; ++k) *(u32x4*)(lds + L_QA + (4 * w + k) * 1024 + 16 * (lane ^ swz16(4 * w + k))) = qa[k];
    }
    bar();
    const int kt = w & 3, hh = w >> 2;
#pragma unroll
    for (int s = 0; s < 16; ++s) W[s] = *(const u32x4*)(lds + L_QA + (16 * hh + c) * 1024 + 16 * ((4 * s + g) ^ swz16(c)));
#pragma unroll
    for (int s = 0; s < 16; ++s) asm volatile("" : "+v"(W[s]));
    bar();
    STAMP(2);
    float mq = -INFINITY, lq = 0.f, mp = -INFINITY;
    f32x16 A0, A1;
#pragma unroll
    for (int i = 0; i < 16; ++i) { A0[i] = 0.f; A1[i] = 0.f; }
    float* mx = (float*)(lds + L_MX);
#define STAGE(slot, REFILL) { const char* tile = lds + L_RING + (slot) * 65536; \
      int ln = lane; asm volatile("" : "+v"(ln)); \
      const int g = ln >> 4, c = ln & 15, swz = swz16(c), h5 = ln >> 5, hq = 16 * hh + c, hp = ln & 31; \
      const char* rowp = tile + (16 * kt + c) * 256; \
      f32x4 X0 = {0.f, 0.f, 0.f, 0.f}, X1 = X0; \
      { u32x4 fa[4], fb[4]; \
        _Pragma("unroll") for (int j = 0; j < 4; ++j) fa[j] = *(const u32x4*)(rowp + 16 * ((4 * j + g) ^ swz)); \
        _Pragma("unroll") for (int j = 0; j < 4; ++j) fb[j] = *(const u32x4*)(rowp + 16384 + 16 * ((4 * j + g) ^ swz)); \
        SB(); \
        X0 = mfma16(fa[0], W[0], X0); X1 = mfma16(fa[1], W[1], X1); X0 = mfma16(fa[2], W[2], X0); X1 = mfma16(fa[3], W[3], X1); \
        SB(); \
        _Pragma("unroll") for (int j = 0; j < 4; ++j) fa[j] = *(const u32x4*)(rowp + 32768 + 16 * ((4 * j + g) ^ swz)); \
        SB(); \
        X0 = mfma16(fb[0], W[4], X0); X1 = mfma16(fb[1], W[5], X1); X0 = mfma16(fb[2], W[6], X0); X1 = mfma16(fb[3], W[7], X1); \
        SB(); \
        _Pragma("unroll") for (int j = 0; j < 4; ++j) fb[j] = *(const u32x4*)(rowp + 49152 + 16 * ((4 * j + g) ^ swz)); \
        SB(); \
        X0 = mfma16(fa[0], W[8], X0); X1 = mfma16(fa[1], W[9], X1); X0 = mfma16(fa[2], W[10], X0); X1 = mfma16(fa[3], W[11], X1); \
        X0 = mfma16(fb[0], W[12], X0); X1 = mfma16(fb[1], W[13], X1); X0 = mfma16(fb[2], W[14], X0); X1 = mfma16(fb[3], W[15], X1); } \
      float sc[4]; \
      _Pragma("unroll") for (int r = 0; r < 4; ++r) sc[r] = bfr(bfr(X0[r] + X1[r]) * SCALE); \
      const float tm = xrow_max(fmaxf(fmaxf(sc[0], sc[1]), fmaxf(sc[2], sc[3]))); \
      if (g == 0) mx[kt * 32 + hq] = tm; \
      bar(); \
      { const float q0 = mx[hq], q1 = mx[32 + hq], q2 = mx[64 + hq], q3 = mx[96 + hq], p0 = mx[hp], p1 = mx[32 + hp], p2 = mx[64 + hp], p3 = mx[96 + hp]; \
        const float mn = fmaxf(fmaxf(mq, fmaxf(q0, q1)), fmaxf(q2, q3)), ms = (mn == -INFINITY) ? 0.f : mn; \
        float p[4]; \
        _Pragma("unroll") for (int r = 0; r < 4; ++r) p[r] = ex2((sc[r] - ms) * LOG2E); \
        *(u32x2*)(lds + L_P + hq * 144 + (16 * kt + 4 * g) * 2) = u32x2{pk_bf16(p[0], p[1]), pk_bf16(p[2], p[3])}; \
        lq = lq * ex2((mq - ms) * LOG2E) + ((p[0] + p[1]) + (p[2] + p[3])); \
        mq = mn; \
        const float mn2 = fmaxf(fmaxf(mp, fmaxf(p0, p1)), fmaxf(p2, p3)), ms2 = (mn2 == -INFINITY) ? 0.f : mn2; \
        const float al = ex2((mp - ms2) * LOG2E); mp = mn2; A0 *= al; A1 *= al; } \
      SB(); REFILL; SB(); \
      { const int blk = (ln >> 4) & 1, q4 = (ln & 15) >> 2, p4 = ln & 3; \
        const char* trp = tile + (w >> 1) * 16384 + (8 * h5 + q4) * 256 + 8 * (p4 & 1); \
        const int ch0 = 8 * (w & 1) + 2 * blk + (p4 >> 1), z0 = swz16(8 * h5 + q4), z1 = swz16(8 * h5 + 4 + q4); \
        s16x4 f[4][2][2]; \
        _Pragma("unroll") for (int ks = 0; ks < 4; ++ks) _Pragma("unroll") for (int ct = 0; ct < 2; ++ct) { \
          f[ks][ct][0] = __builtin_amdgcn_ds_read_tr16_b64_v4i16((LAS s16x4*)(trp + ks * 4096 + 16 * ((ch0 + 4 * ct) ^ z0))); \
          f[ks][ct][1] = __builtin_amdgcn_ds_read_tr16_b64_v4i16((LAS s16x4*)(trp + ks * 4096 + 1024 + 16 * ((ch0 + 4 * ct) ^ z1))); } \
        SB(); \
        bar(); \
        u32x4 P[4]; \
        _Pragma("unroll") for (int ks = 0; ks < 4; ++ks) P[ks] = *(const u32x4*)(lds + L_P + hp * 144 + (16 * ks + 8 * h5) * 2); \
        SB(); \
        _Pragma("unroll") for (int ks = 0; ks < 4; ++ks) { \
          A0 = mfma32(__builtin_bit_cast(u32x4, __builtin_shufflevector(f[ks][0][0], f[ks][0][1], 0, 1, 2, 3, 4, 5, 6, 7)), P[ks], A0); \
          A1 = mfma32(__builtin_bit_cast(u32x4, __builtin_shufflevector(f[ks][1][0], f[ks][1][1], 0, 1, 2, 3, 4, 5, 6, 7)), P[ks], A1); } \
        SB(); } }
    STAGE(0, ;);
    STAMP(8);
#pragma unroll 1
    for (int it = 0; it < 3; ++it) {
      STAGE(1, { STC(0, S0); SB(); LDS_(S0, 2 * it + 4); });
      STAGE(0, { STC(1, S1); SB(); LDS_(S1, 2 * it + 5); });
      STAMP(10 + it);
    }
    STAGE(1, ;);
    STAMP(13);
    const int h5 = lane >> 5, hq = 16 * hh + c, hp = lane & 31;
    float* lx = (float*)(lds + L_ML);
    lq = xrow_sum(lq);
    if (g == 0) lx[kt * 32 + hq] = lq;
    bar();
    const float lt = (lx[hp] + lx[32 + hp]) + (lx[64 + hp] + lx[96 + hp]);
    const float inv = 1.f / lt;
    char* img = lds + L_RING + hp * 1040 + (64 * w + 4 * h5) * 2;
#pragma unroll
    for (int k = 0; k < 4; ++k) {
      *(u32x2*)(img + 16 * k) = u32x2{pk_bf16(A0[4 * k] * inv, A0[4 * k + 1] * inv), pk_bf16(A0[4 * k + 2] * inv, A0[4 * k + 3] * inv)};
      *(u32x2*)(img + 64 + 16 * k) = u32x2{pk_bf16(A1[4 * k] * inv, A1[4 * k + 1] * inv), pk_bf16(A1[4 * k + 2] * inv, A1[4 * k + 3] * inv)};
    }
    if (tid < 32) __builtin_amdgcn_raw_buffer_store_b64(u32x2{f2u(mp), f2u(lt)}, mk_rsrc(ws, (OFFM & 512) ? 0 : WS_END), hp * 128, WS_ML + b * 4096 + sp * 8, AUX_SC1);
    bar();
#pragma unroll
    for (int k = 0; k < 4; ++k)
      __builtin_amdgcn_raw_buffer_store_b128(*(const u32x4*)(lds + L_RING + (4 * w + k) * 1040 + lane * 16), mk_rsrc(ws, (OFFM & 32) ? 0 : WS_END), lane * 16, WS_PB + ((b * 32 + 4 * w + k) * 16 + sp) * 1024, PB_ST);
    DRAIN();
    bar();
    if (tid == 0) __hip_atomic_store((GAS u64*)(ws + WS_F2) + rs, tag0, RLX_AGENT);
    STAMP(3); STAMP(4);
  }
  STAMP(5);
  {
    const bool isb = (lane & 12) == 12;
    float* fr = (float*)(lds + L_RING);
    u32x4 y = {0u, 0u, 0u, 0u};
#define POLL_Y(SLP) _Pragma("unroll 1") for (unsigned spins = 0; spins < SPIN_CAP; ++spins) { \
      y = ld128<A_POLL>(rws, fy_off, WS_Y); if ((lsb4(y) ^ fold) == 15u) break; __builtin_amdgcn_s_sleep(SLP); }
#define SUM_ROWS(v) { const float a0 = v[0] + dppf<ROR8>(v[0]), a1 = v[1] + dppf<ROR8>(v[1]), a2 = v[2] + dppf<ROR8>(v[2]), a3 = v[3] + dppf<ROR8>(v[3]); \
      const float b0 = (c & 8) ? a2 : a0, b1 = (c & 8) ? a3 : a1; \
      const float c0 = b0 + dppf<0x124>(b0), c1 = b1 + dppf<0x12C>(b1); \
      v[0] = xrow_sum((c & 4) ? c0 : c1); }
    if (isb) { POLL_Y(16); }
    {
      float v[4];
#pragma unroll
      for (int j = 0; j < 4; ++j) v[j] = u2f(y[j] & ~0xffu);
      SUM_ROWS(v);
      if (lane < 16) fr[128 + w * 16 + lane] = v[0];
      if (lane == 0) *(unsigned*)(lds + L_FIN + w * 4) = fold;
    }
    bar();
    float bpart = 0.f;
    if (w == 0) {
#pragma unroll
      for (int ww = 0; ww < 8; ++ww) bpart += fr[128 + ww * 16 + c];
      bpart = bfr(bfr(1.f - gate) * bfr(bpart));
      const int po = (rb * 128 + 16 * (lane & 7)) * 2048 + rs * 64;
      const unsigned po_old = *(const unsigned*)(lds + L_FIN + (lane & 7) * 4);
#pragma unroll 1
      for (unsigned spins = 0; spins < SPIN_CAP; ++spins) {
        if (__any((ld32<A_POLL>(rws, po, WS_Y) ^ po_old) & 1u)) break;
        __builtin_amdgcn_s_sleep(SSLEEP);
      }
    }
    GAS u16* op = (GAS u16*)out + rb * 512 + rs * 16 + 4 * (c & 3) + (((c >> 2) & 2) | (((c >> 2) & 1) ^ 1));
    asm volatile("" : "+v"(bpart), "+v"(op));
    bar();
    y = u32x4{0u, 0u, 0u, 0xfeu};
    if (!isb) { POLL_Y(FSLEEP); }
    STAMP(6);
    const float lse = u2f((((y[0] >> 1) & 0x7fu) | (((y[1] >> 1) & 0x7fu) << 7) | (((y[2] >> 1) & 0x7fu) << 14) | (((y[3] >> 1) & 0x7fu) << 21)) << 4);
    float M = fmaxf(lse, dppf<ROR8>(lse)); M = fmaxf(M, dppf<ROR4>(M));
    const float e = isb ? 0.f : ex2(lse - M);
    float L = e + dppf<ROR8>(e); L += dppf<ROR4>(L);
    const float cf = e * __builtin_amdgcn_rcpf(L);
    float v[4];
#pragma unroll
    for (int j = 0; j < 4; ++j) v[j] = cf * u2f(y[j] & ~0xffu);
    SUM_ROWS(v);
    if (lane < 16) fr[w * 16 + lane] = v[0];
    bar();
    if (tid < 16) {
      const float a = ((fr[tid] + fr[16 + tid]) + (fr[32 + tid] + fr[48 + tid])) + ((fr[64 + tid] + fr[80 + tid]) + (fr[96 + tid] + fr[112 + tid]));
      const float o = bfr(gate * bfr(a)) + bpart;
      *op = (u16)pk_bf16(o, o);
    }
    if (rs == 0 && tid == 0) __hip_atomic_store((GAS u64*)(ws + WS_EPOCH + rb * 128), tagr, RLX_AGENT);
  }
  STAMP(7);
    return;
  }
  {
    const int h = __builtin_amdgcn_readfirstlane(HMAP ? x8 + 8 * ((idx / 6 + HROT) & 3) : 4 * x8 + ((idx / 6 + HROT) & 3)), b = __builtin_amdgcn_readfirstlane((idx % 6) / 3), sp = __builtin_amdgcn_readfirstlane(idx % 3);
    const int src = b * 128 + h * 4 + sp;
    constexpr int NT = 2048 / NL;
    const u32x4 sel = make_sel(c);
    u32x4 W[16], kb[NL], vb[NL];
    const rsrc_t ro = mk_rsrc(wo, (OFFM & 128) ? 0 : 4096 * 512 * 2);
    unsigned old;
    const int wr = __builtin_amdgcn_readfirstlane((w + (WROT ? (idx % 6) * 4 / 3 : 0)) & 7);
#define LD_SMALL() unsigned x0 = x[64 * wr + lane], x1 = x[512 + 64 * wr + lane]; LD_EPOCH(); \
    old = ld32<A_PRE>(rws, lane * 4, WS_Y + src * 2048 + w * 256); unsigned touch = ld32<AUX_NT>(ro, 0, h * 131072);
    const rsrc_t rk = mk_rsrc(Kc, 128u << 20), rv = mk_rsrc(Vc, 128u << 20);
    const int t1 = (b * 32 + h) * NT + NT * (sp + 1) / 3;
    int t = (b * 32 + h) * NT + NT * sp / 3 + w;
#define LDT(r, tt, i) ld128<AUX_NT>(r, lane * 16 + ((i) & 3) * 1024, (tt) * (NL * 1024) + ((i) >> 2) * 4096)
#define LDKV(a, r, tt) _Pragma("unroll") for (int i = 0; i < NL; ++i) a[i] = LDT(r, tt, i);
    u32x4 kb2[NL], vb2[NL];
    unsigned coop = 0;
    if (COOP) {
      const int ln = (idx * 8 + w) * 22 + lane;
      coop = ld32<AUX_NT>(mk_rsrc(wq, 512 * 4096 * 2), (lane < 22 && ln < 4096) ? (ln >> 3) * 8192 + (ln & 7) * 128 : 0x7fffffff, x8 * 1024);
    }
    LD_SMALL();
#define LD_FIRST(T) { constexpr int i = (T) & 3; \
        if constexpr ((T) < 4) kb2[i] = LDT(rk, t, i); else if constexpr ((T) < 8) vb2[i] = LDT(rv, t, i); \
        else if constexpr ((T) < 12) kb[i] = LDT(rk, t + 8, i); else vb[i] = LDT(rv, t + 8, i); }
    {
      const rsrc_t rq = mk_rsrc(wq, (OFFM & 64) ? 0 : 512 * 4096 * 2);
      static_for<16>([&](auto T) {
        W[T] = ld128<AUX_NT>(rq, g * (16 * 8192) + c * 16, (64 * wr + T) * 8192 + h * 256);
        if constexpr (T < ILV) { SB(); LD_FIRST(T); SB(); } });
    }
    SB();
    USE_EPOCH();
    u64 tag = b ? tag1 : tag0;
    asm volatile("" : "+s"(tag));
    asm volatile("" : "+v"(old), "+v"(x0), "+v"(x1) : "v"(touch), "v"(coop));
    {
      f32x4 D = {0.f, 0.f, 0.f, 0.f};
      rowsum_refill<0>(W, x0 | (x1 << 16), sel, D, [&](auto T) { if constexpr (T + ILV < 16) LD_FIRST(T + ILV); });
#pragma unroll
      for (int r = 0; r < 4; ++r) red[(wr * 2 + (c >> 3)) * 128 + 32 * g + 8 * r + (c & 7)] = D[r];
    }
    t += 8;
    SB();
#pragma unroll
    for (int i = 0; i < 4; ++i) W[i] = LDT(rk, t + 8, i);
#pragma unroll
    for (int i = 0; i < 4; ++i) W[4 + i] = LDT(rv, t + 8, i);
    SB();
    bar();
    u32x4 q;
    QREAD(q, b);
    STAMP(1);
    float m = -INFINITY, l = 0.f;
    f32x4 O = {0.f, 0.f, 0.f, 0.f};
#define SOFTMAX(xs) const float s = bfr(bfr(treduce<NL>(xs, c)) * SCALE); \
      const float mn = fmaxf(m, xrow_max(row_max(s))), ms = (mn == -INFINITY) ? 0.f : mn; \
      const float al = ex2((m - ms) * LOG2E), p = ex2((s - ms) * LOG2E); \
      m = mn; l = l * al + p; O *= al; unsigned hl = hilo(p);
#define ARRIVED(a) _Pragma("unroll") for (int i = 0; i < NL; ++i) asm volatile("" : "+v"(a[i]));
#define SCORES(xs, kk) float xs[NL]; _Pragma("unroll") for (int i = 0; i < NL; ++i) xs[i] = dot8(kk[i], q);
#define TILE(WITH_V) { if (WITH_V) { ARRIVED(kb); SB(); LDKV(vb, rv, t); SB(); } \
      SCORES(xs, kb); SOFTMAX(xs); asm volatile("" : "+v"(hl), "+v"(O), "+v"(l)); \
      t += 8; SB(); ARRIVED(vb); SB(); LDKV(kb, rk, t); SB(); \
      rowsum<NL, 16 / NL>(vb, hl, sel, O); asm volatile("" : "+v"(O)); SB(); TLREC(); }
#define TILE2(EXTRA) { ARRIVED(kb); SB(); LDKV(vb, rv, t); SB(); \
      SCORES(xs, kb); ARRIVED(xs); t += 8; SB(); LDKV(kb, rk, t); SB(); EXTRA; SB(); \
      SOFTMAX(xs); rowsum<NL, 16 / NL>(vb, hl, sel, O); asm volatile("" : "+v"(O), "+v"(l)); SB(); TLREC(); }
#define TILE_NOLOAD(kk, vv) { SCORES(xs, kk); SOFTMAX(xs); rowsum<NL, 16 / NL>(vv, hl, sel, O); asm volatile("" : "+v"(O), "+v"(l)); SB(); TLREC(); }
#define TLREC()
#define TILE_DEEP() { SCORES(xs, kb); ARRIVED(xs); t += 8; SB(); LDKV(kb, rk, t); SB(); \
      SOFTMAX(xs); rowsum<NL, 16 / NL>(vb, hl, sel, O); asm volatile("" : "+v"(O), "+v"(l)); SB(); LDKV(vb, rv, t); SB(); TLREC(); }
    TILE_NOLOAD(kb2, vb2);
    t += 8;
    TILE_DEEP();
    { u32x4 k3[4] = {W[0], W[1], W[2], W[3]}, v3[4] = {W[4], W[5], W[6], W[7]}; TILE_NOLOAD(k3, v3); }
    {
      const int ndeep = ((h & 3) == 1) ? DEEP1 : DEEP0;
#pragma unroll 1
      for (int k = 0; k < ndeep; ++k) TILE_DEEP();
    }
    TILE(false);
    const unsigned img = (unsigned)(uintptr_t)(LAS char*)(lds + L_RING) + w * 16384;
    int dvo = (lane >> 3) * 1024 + 16 * ((lane & 7) ^ ((lane >> 4 & 1) << 1));
#pragma unroll 1
    for (int k = 0; k < 16; k += 16 / NDMA) {
#define DMA_STEP() _Pragma("unroll") for (int j = 0; j < 16 / NDMA; ++j) { \
        unsigned keep; \
        asm volatile("s_mov_b32 %0, m0\n\ts_mov_b32 m0, %1\n\ts_nop 4\n\tbuffer_load_dwordx4 %2, %3, %4 offen nt lds\n\ts_mov_b32 m0, %0" : "=&s"(keep) \
                     : "s"(__builtin_amdgcn_readfirstlane(img + (k + j) * 1024)), "v"(dvo), "s"(ro), "s"(__builtin_amdgcn_readfirstlane((h * 128 + 8 * (k + j)) * 1024 + w * 128)) : "memory"); \
        dvo ^= 64; }
      if constexpr (M2 & 1) TILE2(DMA_STEP()) else { TILE(true); DMA_STEP(); }
      SB();
    }
    TILE(true);
    asm volatile("s_waitcnt vmcnt(%0)" :: "i"(NL) : "memory");
    {
      const int q4 = c >> 2, p4 = lane & 3, phi = (q4 >> 1) | ((g & 1) << 1);
#pragma unroll
      for (int s = 0; s < 4; ++s)
#pragma unroll
        for (int ct = 0; ct < 4; ++ct) {
          const char* a = lds + L_RING + w * 16384 + (32 * s + 8 * g + q4) * 128 + 16 * ((p4 >> 1) | ((ct ^ phi) << 1)) + 8 * (p4 & 1);
          const s16x4 lo = __builtin_amdgcn_ds_read_tr16_b64_v4i16((LAS s16x4*)a), hi = __builtin_amdgcn_ds_read_tr16_b64_v4i16((LAS s16x4*)(a + 512));
          W[4 * s + ct] = __builtin_bit_cast(u32x4, __builtin_shufflevector(lo, hi, 0, 1, 2, 3, 4, 5, 6, 7));
        }
    }
#pragma unroll
    for (int i = 0; i < 16; ++i) asm volatile("" : "+v"(W[i]));
    const GAS u64* go = (const GAS u64*)(ws + WS_GO) + h * 16 + b;
#pragma unroll 1
    while (t + 24 < t1) { if constexpr (M2 & 2) TILE2(;) else TILE(true); }
    u64 gov;
    {
      u32x4 vb3[NL];
      ARRIVED(kb); SB();
      LDKV(vb, rv, t); SB(); LDKV(kb2, rk, t + 8); SB(); LDKV(vb2, rv, t + 8);
      SB();
      SCORES(xs, kb);
      ARRIVED(xs); SB();
      LDKV(kb, rk, t + 16); SB(); LDKV(vb3, rv, t + 16);
      gov = __hip_atomic_load(go, RLX_AGENT);
      SB();
      SOFTMAX(xs); rowsum<NL, 16 / NL>(vb, hl, sel, O); asm volatile("" : "+v"(O), "+v"(l)); SB(); TLREC();
      TILE_NOLOAD(kb2, vb2);
      TILE_NOLOAD(kb, vb3);
    }
    STAMP(2);
    l = xrow_sum(row_sum(l)) * (NL / 16.f);
    float* ml = (float*)(lds + L_ML);
#pragma unroll
    for (int r = 0; r < 4; ++r) {
      const unsigned hl = hilo(O[r] + dppf<ROR8>(O[r]));
      *(u16*)(lds + L_P + (2 * w + (c >> 3)) * 272 + (32 * g + 8 * r + (c & 7)) * 2) = (u16)((c & 8) ? (hl >> 16) : hl);
    }
    if (lane == 0) { ml[w] = m; ml[8 + w] = l; }
    bar();
    STAMP(4);
    f32x4 D[4];
    const float mj = ml[lane & 7], lj = ml[8 + (lane & 7)], ma = ml[2 * g], mb = ml[2 * g + 1];
    project(W, lds, g, c, D);
    float M = fmaxf(mj, dppf<XOR7>(mj)); M = fmaxf(M, dppf<XOR2>(M)); M = fmaxf(M, dppf<XOR1>(M));
    const float Ms = (M == -INFINITY) ? 0.f : M;
    float L = ex2((mj - Ms) * LOG2E) * lj; L += dppf<XOR7>(L); L += dppf<XOR2>(L); L += dppf<XOR1>(L);
    const float inv = __builtin_amdgcn_rcpf(L), fa = ex2((ma - Ms) * LOG2E) * inv, fb = ex2((mb - Ms) * LOG2E) * inv;
    float y4[4];
#pragma unroll
    for (int k = 0; k < 4; ++k) y4[k] = fa * (D[k][0] + D[k][1]) + fb * (D[k][2] + D[k][3]);
    const auto s0 = __builtin_amdgcn_permlane32_swap(f2u(y4[0]), f2u(y4[2]), false, false), s1 = __builtin_amdgcn_permlane32_swap(f2u(y4[1]), f2u(y4[3]), false, false);
    const auto s2 = __builtin_amdgcn_permlane16_swap(f2u(u2f(s0[0]) + u2f(s0[1])), f2u(u2f(s1[0]) + u2f(s1[1])), false, false);
    const unsigned code = f2u(Ms * LOG2E + __builtin_amdgcn_logf(L)) >> 4;
    const unsigned yw = (f2u(u2f(s2[0]) + u2f(s2[1])) & ~0xffu) | (((code >> (7 * (lane & 3))) & 0x7fu) << 1) | ((old & 1u) ^ 1u);
    const u32x4 yv = {yw, f2u(dppf<0x55>(u2f(yw))), f2u(dppf<0xAA>(u2f(yw))), f2u(dppf<0xFF>(u2f(yw)))};
#pragma unroll 1
    for (unsigned spins = 0; !(OFFM & 256) && uniform64(gov) != tag && spins < SPIN_CAP; ++spins) gov = __hip_atomic_load(go, RLX_AGENT);
    if ((lane & 3) == 0) __builtin_amdgcn_raw_buffer_store_b128(yv, rws, lane * 4, WS_Y + src * 2048 + w * 256, A_YST);
    STAMP(5); STAMP(6); STAMP(3);
  }
}

extern "C" void kernel_launch(void* const* d_in, const int* in_sizes, int n_in, void* d_out, int out_size, void* d_ws, size_t ws_size, hipStream_t stream) {
  fused_decode<<<256, 512, 0, stream>>>((const u16*)d_in[0], (const u16*)d_in[1], (const u16*)d_in[2], (const u16*)d_in[3], (const u16*)d_in[4],
                                        (const u16*)d_in[6], (const u16*)d_in[7], (const u16*)d_in[8], (const u16*)d_in[9], (const u16*)d_in[10],
                                        (const u16*)d_in[11], (u16*)d_out, (char*)d_ws);
}
```

```cpp
#include <hip/hip_runtime.h>
#include <stdint.h>
#include <type_traits>

typedef unsigned short u16;
typedef unsigned long long u64;
typedef unsigned u32x4 __attribute__((ext_vector_type(4)));
typedef unsigned u32x2 __attribute__((ext_vector_type(2)));
typedef float f32x4 __attribute__((ext_vector_type(4)));
typedef float f32x16 __attribute__((ext_vector_type(16)));
typedef __bf16 bf16x8 __attribute__((ext_vector_type(8)));
typedef __bf16 bf16x2 __attribute__((ext_vector_type(2)));
typedef short s16x4 __attribute__((ext_vector_type(4)));
typedef __amdgpu_buffer_rsrc_t rsrc_t;
#define GAS __attribute__((address_space(1)))
#define LAS __attribute__((address_space(3)))
#define DI __device__ __forceinline__
#define RLX_AGENT __ATOMIC_RELAXED, __HIP_MEMORY_SCOPE_AGENT

#define NL 4
#define OFFM 0
#define TL 0
#define STAG 0
#define NDMA 8
#define SSLEEP 24
#define PB_ST 18
#define PB_LD 18
#define BOOST 1
#define EBAR 0
#define HROT 0
#define HMAP 0
#define DEEP1 0
#define DEEP0 0
#define WROT 1
#define TROT 0
#define ILV 0
#define COOP 0
#define PRE4 2
#define M2 0
#define A_PRE 18
#define A_QA 16
#define A_ST 16
#define A_POLL 16
#define A_YST 16
#define WSLEEP 3
#define OUTSC1 0
#define BDELAY 0
#define NBOOST 1
#define FSLEEP 0
#define STAMPS 0
constexpr int AUX_NT = 2, AUX_SC1 = 16;
constexpr float SCALE = 0.08838834764831845f;
constexpr float LOG2E = 1.4426950408889634f;

constexpr int WS_EPOCH = 0;
constexpr int WS_F1 = 1024;
constexpr int WS_F2 = 2048;
constexpr int WS_GO = 4096;
constexpr int WS_QA = 8192;
constexpr int WS_ML = WS_QA + 65536;
constexpr int WS_PB = WS_ML + 8192;
constexpr int WS_Y = WS_PB + 2 * 32 * 16 * 1024;
constexpr int WS_END = WS_Y + 2 * 128 * 2048;
constexpr int WS_STAMP = 4 << 20;

constexpr int L_RING = 0;
constexpr int L_RED = 131072;
constexpr int L_P = L_RED + 8192;
constexpr int L_MX = L_P + 32 * 144;
constexpr int L_ML = L_MX + 512;
constexpr int L_FIN = L_ML + 512;
constexpr int L_SC = 147456;
constexpr int L_QA = 131072;
constexpr int L_END = 163840;

DI rsrc_t mk_rsrc(const void* p, unsigned bytes) { return __builtin_amdgcn_make_buffer_rsrc((void*)p, 0, bytes, 0x00020000); }
template <int AUX> DI u32x4 ld128(rsrc_t r, int voff, int soff) { return __builtin_amdgcn_raw_buffer_load_b128(r, voff, soff, AUX); }
template <int AUX> DI u32x2 ld64(rsrc_t r, int voff, int soff) { return __builtin_amdgcn_raw_buffer_load_b64(r, voff, soff, AUX); }
template <int AUX> DI unsigned ld32(rsrc_t r, int voff, int soff) { return __builtin_amdgcn_raw_buffer_load_b32(r, voff, soff, AUX); }
DI float u2f(unsigned u) { return __builtin_bit_cast(float, u); }
DI unsigned f2u(float f) { return __builtin_bit_cast(unsigned, f); }
DI unsigned pk_bf16(float lo, float hi) { bf16x2 r; r[0] = (__bf16)lo; r[1] = (__bf16)hi; return __builtin_bit_cast(unsigned, r); }
DI float bfr(float x) { return u2f(pk_bf16(x, x) << 16); }
DI unsigned hilo(float x) { const float h = bfr(x); return pk_bf16(h, x - h); }
DI float ex2(float x) { return __builtin_amdgcn_exp2f(x); }
DI float dot2(unsigned a, unsigned b, float c) { return __builtin_amdgcn_fdot2_f32_bf16(__builtin_bit_cast(bf16x2, a), __builtin_bit_cast(bf16x2, b), c, false); }
DI float dot8(const u32x4& a, const u32x4& b) { return dot2(a[3], b[3], dot2(a[2], b[2], dot2(a[1], b[1], dot2(a[0], b[0], 0.f)))); }
template <int CTRL> DI float dppf(float x) { return u2f(__builtin_amdgcn_mov_dpp(f2u(x), CTRL, 0xf, 0xf, true)); }
template <int T> DI unsigned bcast16(unsigned v) { return __builtin_amdgcn_mov_dpp(v, 0x150 + T, 0xf, 0xf, true); }
constexpr int XOR1 = 0xB1, XOR2 = 0x4E, XOR7 = 0x141, ROR8 = 0x128, ROR4 = 0x124;
DI float xrow_max(float x) {
  auto s = __builtin_amdgcn_permlane16_swap(f2u(x), f2u(x), false, false); x = fmaxf(u2f(s[0]), u2f(s[1]));
  auto t = __builtin_amdgcn_permlane32_swap(f2u(x), f2u(x), false, false); return fmaxf(u2f(t[0]), u2f(t[1]));
}
DI float xrow_sum(float x) {
  auto s = __builtin_amdgcn_permlane16_swap(f2u(x), f2u(x), false, false); x = u2f(s[0]) + u2f(s[1]);
  auto t = __builtin_amdgcn_permlane32_swap(f2u(x), f2u(x), false, false); return u2f(t[0]) + u2f(t[1]);
}
DI float row_max(float x) { x = fmaxf(x, dppf<ROR8>(x)); x = fmaxf(x, dppf<ROR4>(x)); x = fmaxf(x, dppf<XOR2>(x)); return fmaxf(x, dppf<XOR1>(x)); }
DI float row_sum(float x) { x += dppf<ROR8>(x); x += dppf<ROR4>(x); x += dppf<XOR2>(x); return x + dppf<XOR1>(x); }
DI f32x4 mfma16(u32x4 a, u32x4 b, f32x4 c) { return __builtin_amdgcn_mfma_f32_16x16x32_bf16(__builtin_bit_cast(bf16x8, a), __builtin_bit_cast(bf16x8, b), c, 0, 0, 0); }
DI f32x16 mfma32(u32x4 a, u32x4 b, f32x16 c) { return __builtin_amdgcn_mfma_f32_32x32x16_bf16(__builtin_bit_cast(bf16x8, a), __builtin_bit_cast(bf16x8, b), c, 0, 0, 0); }
DI u64 uniform64(u64 v) { return ((u64)(unsigned)__builtin_amdgcn_readfirstlane((unsigned)(v >> 32)) << 32) | (unsigned)__builtin_amdgcn_readfirstlane((unsigned)v); }
DI u64 next_tag(u64 e) { return e * 0x9E3779B97F4A7C15ull + 0x632BE59BD9B4E019ull; }
DI void bar() { __syncthreads(); }
#define DRAIN() asm volatile("s_waitcnt vmcnt(0)" ::: "memory")
#define PIN() asm volatile("" ::: "memory")
#define SB() __builtin_amdgcn_sched_barrier(0)
constexpr unsigned SPIN_CAP = 1u << 22;

template <int N, int STEP, int T = 0> DI void rowsum(const u32x4 (&W)[N], unsigned pair, const u32x4& sel, f32x4& D) {
  if constexpr (T < N) {
    const unsigned s = bcast16<T * STEP>(pair);
    const u32x4 B = {__builtin_amdgcn_perm(s, s, sel[0]), __builtin_amdgcn_perm(s, s, sel[1]), __builtin_amdgcn_perm(s, s, sel[2]), __builtin_amdgcn_perm(s, s, sel[3])};
    D = mfma16(W[T], B, D);
    rowsum<N, STEP, T + 1>(W, pair, sel, D);
  }
}
template <int N, int I = 0, class F> DI void static_for(F&& f) { if constexpr (I < N) { f(std::integral_constant<int, I>{}); static_for<N, I + 1>(f); } }
template <int T, class F> DI void rowsum_refill(const u32x4 (&W)[16], unsigned pair, const u32x4& sel, f32x4& D, F&& issue) {
  if constexpr (T < 16) {
    const unsigned s = bcast16<T>(pair);
    const u32x4 B = {__builtin_amdgcn_perm(s, s, sel[0]), __builtin_amdgcn_perm(s, s, sel[1]), __builtin_amdgcn_perm(s, s, sel[2]), __builtin_amdgcn_perm(s, s, sel[3])};
    D = mfma16(W[T], B, D);
    asm volatile("" : "+v"(D));
    __builtin_amdgcn_sched_barrier(0);
    issue(std::integral_constant<int, T>{});
    __builtin_amdgcn_sched_barrier(0);
    rowsum_refill<T + 1>(W, pair, sel, D, issue);
  }
}
DI int swz16(int row) { const int u = (row >> 2) & 3; return ((row & 3) << 2) | (((u ^ (u >> 1)) & 1) << 1) | (u >> 1); }
DI u32x4 make_sel(int c) {
  const int j = c & 7; const unsigned b = (c & 8) ? 0x0302u : 0x0100u;
  const unsigned v = (j & 1) ? ((b << 16) | 0x0c0cu) : (0x0c0c0000u | b);
  u32x4 s;
#pragma unroll
  for (int w = 0; w < 4; ++w) s[w] = (w == (j >> 1)) ? v : 0x0c0c0c0cu;
  return s;
}
template <int N> DI float treduce(float (&x)[N], int c) {
#define TR_STEP(CNT, CTRL, BIT) _Pragma("unroll") for (int i = 0; i < (CNT); ++i) { const float a = x[i] + dppf<CTRL>(x[i]), b = x[i + (CNT)] + dppf<CTRL>(x[i + (CNT)]); x[i] = (c & (BIT)) ? b : a; }
  TR_STEP(N / 2, ROR8, 8)
  TR_STEP(N / 4, XOR7, 4)
  if constexpr (N >= 8) { TR_STEP(N / 8, XOR2, 2) } else { x[0] += dppf<XOR2>(x[0]); }
  if constexpr (N >= 16) { TR_STEP(N / 16, XOR1, 1) } else { x[0] += dppf<XOR1>(x[0]); }
#undef TR_STEP
  return x[0];
}
DI void wait_flags32(const GAS u64* f, u64 tag, int lane) {
#pragma unroll 1
  for (unsigned spins = 0; spins < SPIN_CAP; ++spins) {
    if (__all(__hip_atomic_load(f + (lane & 31), RLX_AGENT) == tag)) break;
    __builtin_amdgcn_s_sleep(WSLEEP);
  }
  __builtin_amdgcn_fence(__ATOMIC_ACQUIRE, "wavefront");
}
DI void transpose_w(const u32x2 (&R)[32], u32x4 (&F)[16]) {
#pragma unroll
  for (int s = 0; s < 4; ++s)
#pragma unroll
    for (int t = 0; t < 4; ++t)
#pragma unroll
      for (int jj = 0; jj < 4; ++jj) F[4 * s + t][jj] = __builtin_amdgcn_perm(R[8 * s + 2 * jj + 1][t >> 1], R[8 * s + 2 * jj][t >> 1], (t & 1) ? 0x07060302u : 0x05040100u);
}
DI void project(const u32x4 (&F)[16], const char* lds, int g, int c, f32x4 (&D)[4]) {
  u32x4 A[4];
#pragma unroll
  for (int s = 0; s < 4; ++s) A[s] = *(const u32x4*)(lds + L_P + c * 272 + 64 * s + 16 * g);
#pragma unroll
  for (int t = 0; t < 4; ++t) D[t] = f32x4{0.f, 0.f, 0.f, 0.f};
#pragma unroll
  for (int s = 0; s < 4; ++s)
#pragma unroll
    for (int t = 0; t < 4; ++t) D[t] = mfma16(A[s], F[4 * s + t], D[t]);
}
DI unsigned lsb4(const u32x4& v) { return (v[0] & 1u) | ((v[1] & 1u) << 1) | ((v[2] & 1u) << 2) | ((v[3] & 1u) << 3); }
DI unsigned mark(float v, unsigned old, int k) { return (f2u(v) & ~1u) | (((old >> k) & 1u) ^ 1u); }

#define STAMP(k) do {} while (0)

__global__ void __launch_bounds__(512, 2)
fused_decode(const u16* __restrict__ x, const u16* __restrict__ Kc, const u16* __restrict__ Vc, const u16* __restrict__ wq, const u16* __restrict__ wo,
             const u16* __restrict__ Cc, const u16* __restrict__ w_q, const u16* __restrict__ w_uk, const u16* __restrict__ w_uv, const u16* __restrict__ w_o,
             const u16* __restrict__ gw, u16* __restrict__ out, char* ws) {
  __shared__ __attribute__((aligned(16))) char lds[L_END];
  const int bid = blockIdx.x, tid = threadIdx.x, lane = tid & 63, w = __builtin_amdgcn_readfirstlane(tid >> 6), g = lane >> 4, c = lane & 15;
  const int x8 = bid & 7, idx = bid >> 3;
  STAMP(0);
  const u64 t_start = (OFFM & (1024 | 2048)) ? __builtin_amdgcn_s_memrealtime() : 0;
  asm volatile("" :: "s"(x), "s"(Kc), "s"(Vc), "s"(wq), "s"(wo), "s"(ws));
  const rsrc_t rws = mk_rsrc(ws, WS_END);
  float* red = (float*)(lds + L_RED);
#define LD_EPOCH() u64 e0 = __hip_atomic_load((GAS u64*)(ws + WS_EPOCH), RLX_AGENT), e1 = __hip_atomic_load((GAS u64*)(ws + WS_EPOCH + 128), RLX_AGENT);
#define USE_EPOCH() asm volatile("" : "+v"(e0), "+v"(e1)); const u64 tag0 = next_tag(uniform64(e0)), tag1 = next_tag(uniform64(e1));
#define LD_ROWS(W, ptr, OFF) { const rsrc_t rq = mk_rsrc(ptr, (OFF) ? 0 : 512 * 4096 * 2); \
    _Pragma("unroll") for (int t = 0; t < 16; ++t) W[t] = ld128<AUX_NT>(rq, g * (16 * 8192) + c * 16, (64 * w + t) * 8192 + h * 256); }
#define LD_OUTW(R, ptr, OFF) { const rsrc_t ro = mk_rsrc(ptr, (OFF) ? 0 : 4096 * 512 * 2); \
    _Pragma("unroll") for (int i = 0; i < 32; ++i) R[i] = ld64<AUX_NT>(ro, g * 8192 + c * 8 + (i & 3) * 1024, (h * 128 + 32 * (i >> 3) + (i & 4)) * 1024 + w * 128); }
#define QPROJ() { asm volatile("" : "+v"(x0), "+v"(x1)); f32x4 D = {0.f, 0.f, 0.f, 0.f}; rowsum<16, 1>(W, x0 | (x1 << 16), sel, D); \
    _Pragma("unroll") for (int r = 0; r < 4; ++r) red[(w * 2 + (c >> 3)) * 128 + 32 * g + 8 * r + (c & 7)] = D[r]; }
#define QREAD(q, bb) { f32x4 a0 = {0.f, 0.f, 0.f, 0.f}, a1 = a0; \
    _Pragma("unroll") for (int ww = 0; ww < 8; ++ww) { const f32x4* p = (const f32x4*)(red + (ww * 2 + (bb)) * 128 + 8 * c); a0 += p[0]; a1 += p[1]; } \
    q = u32x4{pk_bf16(a0[0], a0[1]), pk_bf16(a0[2], a0[3]), pk_bf16(a1[0], a1[1]), pk_bf16(a1[2], a1[3])}; }

  if (idx >= 24) {
  if (OFFM & 256) { STAMP(1); STAMP(2); STAMP(3); STAMP(4); STAMP(5); STAMP(6); STAMP(7); return; }
  const int rb = idx >= 28, rs = __builtin_amdgcn_readfirstlane(4 * x8 + (idx & 3));
  const int fy_off = (rb * 128 + (tid >> 2)) * 2048 + rs * 64 + (tid & 3) * 16;
  unsigned fold; u64 tagr;
  float gate;
#define LD_GATE() const u32x4 xv = *(const u32x4*)(x + rb * 512 + lane * 8), gv = *(const u32x4*)(gw + lane * 8);
#define USE_GATE() { float s = 0.f; \
    _Pragma("unroll") for (int j = 0; j < 4; ++j) s += bfr(u2f(xv[j] << 16) * u2f(gv[j] << 16)) + bfr(u2f(xv[j] & 0xffff0000u) * u2f(gv[j] & 0xffff0000u)); \
    s = bfr(xrow_sum(row_sum(s))); gate = bfr(1.f / (1.f + __expf(-s))); }

  if (idx < 28) {
    const int h = rs;
    const u32x4 sel = make_sel(c);
    u32x4 W[16], U[16];
    unsigned x0 = x[tid], x1 = x[512 + tid];
    LD_ROWS(W, w_q, OFFM & 1);
    SB();
    {
      asm volatile("" : "+v"(x0), "+v"(x1));
      const unsigned xp = x0 | (x1 << 16);
      const rsrc_t ru = mk_rsrc(w_uk, (OFFM & 2) ? 0 : 512 * 4096 * 2);
      f32x4 D = {0.f, 0.f, 0.f, 0.f};
      rowsum_refill<0>(W, xp, sel, D, [&](auto T) { U[T] = ld128<AUX_NT>(ru, g * (16 * 8192) + c * 16, (64 * w + T) * 8192 + h * 256); });
#pragma unroll
      for (int r = 0; r < 4; ++r) red[(w * 2 + (c >> 3)) * 128 + 32 * g + 8 * r + (c & 7)] = D[r];
    }
    LD_EPOCH();
    const u32x4 fy = ld128<A_PRE>(rws, fy_off, WS_Y);
    const u32x4 oy0 = ld128<A_PRE>(rws, c * 16, WS_Y + (h * 4 + 3) * 2048 + w * 256), oy1 = ld128<A_PRE>(rws, c * 16, WS_Y + (128 + h * 4 + 3) * 2048 + w * 256);
    LD_GATE();
    SB();
    bar();
#pragma unroll
    for (int bb = 0; bb < 2; ++bb) {
      u32x4 q; QREAD(q, bb);
      float xs[16];
#pragma unroll
      for (int t = 0; t < 16; ++t) xs[t] = dot8(U[t], q);
      const float qa = treduce<16>(xs, c);
      *(u16*)(lds + L_RING + bb * 1024 + tid * 2) = (u16)pk_bf16(qa, qa);
    }
    SB();
    USE_EPOCH(); USE_GATE();
    fold = lsb4(fy);
    unsigned old = lsb4(oy0) | (lsb4(oy1) << 4);
    asm volatile("" : "+v"(fold), "+v"(gate), "+v"(old));
    tagr = tag0;
    DRAIN();
    bar();
    if (w == 0) {
      __builtin_amdgcn_raw_buffer_store_b128(*(const u32x4*)(lds + L_RING + lane * 16), rws, lane * 16, WS_QA + h * 1024, A_ST);
      __builtin_amdgcn_raw_buffer_store_b128(*(const u32x4*)(lds + L_RING + 1024 + lane * 16), rws, lane * 16, WS_QA + (32 + h) * 1024, A_ST);
      DRAIN();
      if (lane == 0) __hip_atomic_store((GAS u64*)(ws + WS_F1) + h, tag0, RLX_AGENT);
    }
    STAMP(1);
    {
      u32x2 R[32];
      LD_OUTW(R, w_o, OFFM & 16);
      LD_ROWS(W, w_uv, OFFM & 8);
      SB();
      transpose_w(R, U);
    }
#pragma unroll
    for (int i = 0; i < 16; ++i) asm volatile("" : "+v"(W[i]), "+v"(U[i]));
    STAMP(2);
    if (w == 0) {
      if (OFFM & 2048) { while (__builtin_amdgcn_s_memrealtime() - t_start < 3900) __builtin_amdgcn_s_sleep(8); }
      wait_flags32((const GAS u64*)(ws + WS_F2), tag0, lane);
      if (lane < 2) __hip_atomic_store((GAS u64*)(ws + WS_GO) + h * 16 + lane, lane ? tag1 : tag0, RLX_AGENT);
    }
    bar();
    STAMP(3);
    u32x4 T[4];
#pragma unroll
    for (int k = 0; k < 4; ++k) T[k] = ld128<PB_LD>(mk_rsrc(ws, (OFFM & 32) ? 0 : WS_END), lane * 16, WS_PB + (((k >> 1) * 32 + h) * 16 + 2 * w + (k & 1)) * 1024);
    const u32x2 mlw = ld64<PB_LD>(rws, (g & 1) * 4096 + c * 8, WS_ML + h * 128);
    const float ms = u2f(mlw[0]), M0 = row_max(ms), M = (M0 == -INFINITY) ? 0.f : M0;
    const float wt = ex2((ms - M) * LOG2E) * u2f(mlw[1]), cf = wt / row_sum(wt);
    float* cr = (float*)(lds + L_RING);
#pragma unroll
    for (int bb = 0; bb < 2; ++bb) {
      const float f0 = u2f(__builtin_amdgcn_readlane(f2u(cf), 16 * bb + 2 * w)), f1 = u2f(__builtin_amdgcn_readlane(f2u(cf), 16 * bb + 2 * w + 1));
      float a[8];
#pragma unroll
      for (int j = 0; j < 4; ++j) {
        a[2 * j] = f0 * u2f(T[2 * bb][j] << 16) + f1 * u2f(T[2 * bb + 1][j] << 16);
        a[2 * j + 1] = f0 * u2f(T[2 * bb][j] & 0xffff0000u) + f1 * u2f(T[2 * bb + 1][j] & 0xffff0000u);
      }
      *(f32x4*)(cr + (w * 2 + bb) * 512 + lane * 8) = f32x4{a[0], a[1], a[2], a[3]};
      *(f32x4*)(cr + (w * 2 + bb) * 512 + lane * 8 + 4) = f32x4{a[4], a[5], a[6], a[7]};
    }
    bar();
    {
      float c0 = 0.f, c1 = 0.f;
#pragma unroll
      for (int ww = 0; ww < 8; ++ww) { c0 += cr[ww * 1024 + tid]; c1 += cr[ww * 1024 + 512 + tid]; }
      const unsigned h0 = hilo(c0), h1 = hilo(c1);
      f32x4 D = {0.f, 0.f, 0.f, 0.f}, D2 = D;
      rowsum<16, 1>(W, __builtin_amdgcn_perm(h1, h0, 0x05040100u), sel, D);
      rowsum<16, 1>(W, __builtin_amdgcn_perm(h1, h0, 0x07060302u), sel, D2);
      D += D2;
#pragma unroll
      for (int r = 0; r < 4; ++r) red[(w * 2 + (c >> 3)) * 128 + 32 * g + 8 * r + (c & 7)] = D[r];
    }
    bar();
    if (tid < 256) {
      float v = 0.f;
#pragma unroll
      for (int ww = 0; ww < 8; ++ww) v += red[ww * 256 + tid];
      *(u16*)(lds + L_P + (tid >> 7) * 272 + (tid & 127) * 2) = (u16)pk_bf16(v, v);
    }
    bar();
    f32x4 D[4];
    project(U, lds, g, c & 1, D);
    if (g == 0) {
      __builtin_amdgcn_raw_buffer_store_b128(u32x4{mark(D[0][0], old, 0), mark(D[1][0], old, 1), mark(D[2][0], old, 2), mark(D[3][0], old, 3)}, rws, c * 16, WS_Y + (h * 4 + 3) * 2048 + w * 256, A_ST);
      __builtin_amdgcn_raw_buffer_store_b128(u32x4{mark(D[0][1], old, 4), mark(D[1][1], old, 5), mark(D[2][1], old, 6), mark(D[3][1], old, 7)}, rws, c * 16, WS_Y + (128 + h * 4 + 3) * 2048 + w * 256, A_ST);
    }
    STAMP(4);
  } else {
    const int b = rs & 1, sp = rs >> 1;
    u32x4 W[16], S0[8], S1[8];
#define LDC(st, i) ld128<AUX_NT>(mk_rsrc(Cc, ((st) < 8 && !(OFFM & 4)) ? (16u << 20) : 0u), lane * 16 + ((i) & 3) * 1024, (b * 8192 + sp * 512 + ((st) & 7) * 64 + 8 * w + ((i) & 4)) * 1024)
#define LDS_(S, st) _Pragma("unroll") for (int i = 0; i < 8; ++i) S[i] = LDC(st, i);
#define STC(slot, S) { int ln = lane; asm volatile("" : "+v"(ln)); char* base = lds + L_RING + (slot) * 65536 + (ln >> 4) * 16384 + 8 * w * 256; \
      _Pragma("unroll") for (int i = 0; i < 8; ++i) *(u32x4*)(base + i * 256 + 16 * ((ln & 15) ^ swz16(8 * w + i))) = S[i]; }
    LD_EPOCH();
    const u32x4 fy = ld128<A_PRE>(rws, fy_off, WS_Y);
    LD_GATE();
    LDS_(S0, 0);
    LDS_(S1, 1);
    SB();
    USE_EPOCH(); USE_GATE();
    fold = lsb4(fy);
    asm volatile("" : "+v"(fold), "+v"(gate));
    tagr = tag1;
    STC(0, S0);
    SB();
    LDS_(S0, 2);
    SB();
    STC(1, S1);
    SB();
    LDS_(S1, 3);
    PIN();
    STAMP(1);
    if (w == 0) wait_flags32((const GAS u64*)(ws + WS_F1), tag0, lane);
    bar();
#pragma unroll
    for (int k = 0; k < 4; ++k) *(u32x4*)(lds + L_QA + (4 * w + k) * 1024 + 16 * (lane ^ swz16(4 * w + k))) = ld128<A_QA>(rws, lane * 16, WS_QA + (b * 32 + 4 * w + k) * 1024);
    bar();
    const int kt = w & 3, hh = w >> 2;
#pragma unroll
    for (int s = 0; s < 16; ++s) W[s] = *(const u32x4*)(lds + L_QA + (16 * hh + c) * 1024 + 16 * ((4 * s + g) ^ swz16(c)));
#pragma unroll
    for (int s = 0; s < 16; ++s) asm volatile("" : "+v"(W[s]));
    bar();
    STAMP(2);
    float mq = -INFINITY, lq = 0.f, mp = -INFINITY;
    f32x16 A0, A1;
#pragma unroll
    for (int i = 0; i < 16; ++i) { A0[i] = 0.f; A1[i] = 0.f; }
    float* mx = (float*)(lds + L_MX);
#define STAGE(slot, REFILL) { const char* tile = lds + L_RING + (slot) * 65536; \
      int ln = lane; asm volatile("" : "+v"(ln)); \
      const int g = ln >> 4, c = ln & 15, swz = swz16(c), h5 = ln >> 5, hq = 16 * hh + c, hp = ln & 31; \
      const char* rowp = tile + (16 * kt + c) * 256; \
      f32x4 X0 = {0.f, 0.f, 0.f, 0.f}, X1 = X0; \
      { u32x4 fa[4], fb[4]; \
        _Pragma("unroll") for (int j = 0; j < 4; ++j) fa[j] = *(const u32x4*)(rowp + 16 * ((4 * j + g) ^ swz)); \
        _Pragma("unroll") for (int j = 0; j < 4; ++j) fb[j] = *(const u32x4*)(rowp + 16384 + 16 * ((4 * j + g) ^ swz)); \
        SB(); \
        X0 = mfma16(fa[0], W[0], X0); X1 = mfma16(fa[1], W[1], X1); X0 = mfma16(fa[2], W[2], X0); X1 = mfma16(fa[3], W[3], X1); \
        SB(); \
        _Pragma("unroll") for (int j = 0; j < 4; ++j) fa[j] = *(const u32x4*)(rowp + 32768 + 16 * ((4 * j + g) ^ swz)); \
        SB(); \
        X0 = mfma16(fb[0], W[4], X0); X1 = mfma16(fb[1], W[5], X1); X0 = mfma16(fb[2], W[6], X0); X1 = mfma16(fb[3], W[7], X1); \
        SB(); \
        _Pragma("unroll") for (int j = 0; j < 4; ++j) fb[j] = *(const u32x4*)(rowp + 49152 + 16 * ((4 * j + g) ^ swz)); \
        SB(); \
        X0 = mfma16(fa[0], W[8], X0); X1 = mfma16(fa[1], W[9], X1); X0 = mfma16(fa[2], W[10], X0); X1 = mfma16(fa[3], W[11], X1); \
        X0 = mfma16(fb[0], W[12], X0); X1 = mfma16(fb[1], W[13], X1); X0 = mfma16(fb[2], W[14], X0); X1 = mfma16(fb[3], W[15], X1); } \
      float sc[4]; \
      _Pragma("unroll") for (int r = 0; r < 4; ++r) sc[r] = bfr(bfr(X0[r] + X1[r]) * SCALE); \
      const float tm = xrow_max(fmaxf(fmaxf(sc[0], sc[1]), fmaxf(sc[2], sc[3]))); \
      if (g == 0) mx[kt * 32 + hq] = tm; \
      bar(); \
      { const float q0 = mx[hq], q1 = mx[32 + hq], q2 = mx[64 + hq], q3 = mx[96 + hq], p0 = mx[hp], p1 = mx[32 + hp], p2 = mx[64 + hp], p3 = mx[96 + hp]; \
        const float mn = fmaxf(fmaxf(mq, fmaxf(q0, q1)), fmaxf(q2, q3)), ms = (mn == -INFINITY) ? 0.f : mn; \
        float p[4]; \
        _Pragma("unroll") for (int r = 0; r < 4; ++r) p[r] = ex2((sc[r] - ms) * LOG2E); \
        *(u32x2*)(lds + L_P + hq * 144 + (16 * kt + 4 * g) * 2) = u32x2{pk_bf16(p[0], p[1]), pk_bf16(p[2], p[3])}; \
        lq = lq * ex2((mq - ms) * LOG2E) + ((p[0] + p[1]) + (p[2] + p[3])); \
        mq = mn; \
        const float mn2 = fmaxf(fmaxf(mp, fmaxf(p0, p1)), fmaxf(p2, p3)), ms2 = (mn2 == -INFINITY) ? 0.f : mn2; \
        const float al = ex2((mp - ms2) * LOG2E); mp = mn2; A0 *= al; A1 *= al; } \
      SB(); REFILL; SB(); \
      { const int blk = (ln >> 4) & 1, q4 = (ln & 15) >> 2, p4 = ln & 3; \
        const char* trp = tile + (w >> 1) * 16384 + (8 * h5 + q4) * 256 + 8 * (p4 & 1); \
        const int ch0 = 8 * (w & 1) + 2 * blk + (p4 >> 1), z0 = swz16(8 * h5 + q4), z1 = swz16(8 * h5 + 4 + q4); \
        s16x4 f[4][2][2]; \
        _Pragma("unroll") for (int ks = 0; ks < 4; ++ks) _Pragma("unroll") for (int ct = 0; ct < 2; ++ct) { \
          f[ks][ct][0] = __builtin_amdgcn_ds_read_tr16_b64_v4i16((LAS s16x4*)(trp + ks * 4096 + 16 * ((ch0 + 4 * ct) ^ z0))); \
          f[ks][ct][1] = __builtin_amdgcn_ds_read_tr16_b64_v4i16((LAS s16x4*)(trp + ks * 4096 + 1024 + 16 * ((ch0 + 4 * ct) ^ z1))); } \
        SB(); \
        bar(); \
        u32x4 P[4]; \
        _Pragma("unroll") for (int ks = 0; ks < 4; ++ks) P[ks] = *(const u32x4*)(lds + L_P + hp * 144 + (16 * ks + 8 * h5) * 2); \
        SB(); \
        _Pragma("unroll") for (int ks = 0; ks < 4; ++ks) { \
          A0 = mfma32(__builtin_bit_cast(u32x4, __builtin_shufflevector(f[ks][0][0], f[ks][0][1], 0, 1, 2, 3, 4, 5, 6, 7)), P[ks], A0); \
          A1 = mfma32(__builtin_bit_cast(u32x4, __builtin_shufflevector(f[ks][1][0], f[ks][1][1], 0, 1, 2, 3, 4, 5, 6, 7)), P[ks], A1); } \
        SB(); } }
    STAGE(0, ;);
    STAMP(8);
#pragma unroll 1
    for (int it = 0; it < 3; ++it) {
      STAGE(1, { STC(0, S0); SB(); LDS_(S0, 2 * it + 4); });
      STAGE(0, { STC(1, S1); SB(); LDS_(S1, 2 * it + 5); });
      STAMP(10 + it);
    }
    STAGE(1, ;);
    STAMP(13);
    const int h5 = lane >> 5, hq = 16 * hh + c, hp = lane & 31;
    float* lx = (float*)(lds + L_ML);
    lq = xrow_sum(lq);
    if (g == 0) lx[kt * 32 + hq] = lq;
    bar();
    const float lt = (lx[hp] + lx[32 + hp]) + (lx[64 + hp] + lx[96 + hp]);
    const float inv = 1.f / lt;
    char* img = lds + L_RING + hp * 1040 + (64 * w + 4 * h5) * 2;
#pragma unroll
    for (int k = 0; k < 4; ++k) {
      *(u32x2*)(img + 16 * k) = u32x2{pk_bf16(A0[4 * k] * inv, A0[4 * k + 1] * inv), pk_bf16(A0[4 * k + 2] * inv, A0[4 * k + 3] * inv)};
      *(u32x2*)(img + 64 + 16 * k) = u32x2{pk_bf16(A1[4 * k] * inv, A1[4 * k + 1] * inv), pk_bf16(A1[4 * k + 2] * inv, A1[4 * k + 3] * inv)};
    }
    if (tid < 32) __builtin_amdgcn_raw_buffer_store_b64(u32x2{f2u(mp), f2u(lt)}, mk_rsrc(ws, (OFFM & 512) ? 0 : WS_END), hp * 128, WS_ML + b * 4096 + sp * 8, AUX_SC1);
    bar();
#pragma unroll
    for (int k = 0; k < 4; ++k)
      __builtin_amdgcn_raw_buffer_store_b128(*(const u32x4*)(lds + L_RING + (4 * w + k) * 1040 + lane * 16), mk_rsrc(ws, (OFFM & 32) ? 0 : WS_END), lane * 16, WS_PB + ((b * 32 + 4 * w + k) * 16 + sp) * 1024, PB_ST);
    DRAIN();
    bar();
    if (tid == 0) __hip_atomic_store((GAS u64*)(ws + WS_F2) + rs, tag0, RLX_AGENT);
    STAMP(3); STAMP(4);
  }
  STAMP(5);
  {
    const bool isb = (lane & 12) == 12;
    float* fr = (float*)(lds + L_RING);
    u32x4 y = {0u, 0u, 0u, 0u};
#define POLL_Y(SLP) _Pragma("unroll 1") for (unsigned spins = 0; spins < SPIN_CAP; ++spins) { \
      y = ld128<A_POLL>(rws, fy_off, WS_Y); if ((lsb4(y) ^ fold) == 15u) break; __builtin_amdgcn_s_sleep(SLP); }
#define SUM_ROWS(v) { const float a0 = v[0] + dppf<ROR8>(v[0]), a1 = v[1] + dppf<ROR8>(v[1]), a2 = v[2] + dppf<ROR8>(v[2]), a3 = v[3] + dppf<ROR8>(v[3]); \
      const float b0 = (c & 8) ? a2 : a0, b1 = (c & 8) ? a3 : a1; \
      const float c0 = b0 + dppf<0x124>(b0), c1 = b1 + dppf<0x12C>(b1); \
      v[0] = xrow_sum((c & 4) ? c0 : c1); }
    if (isb) { POLL_Y(16); }
    {
      float v[4];
#pragma unroll
      for (int j = 0; j < 4; ++j) v[j] = u2f(y[j] & ~0xffu);
      SUM_ROWS(v);
      if (lane < 16) fr[128 + w * 16 + lane] = v[0];
      if (lane == 0) *(unsigned*)(lds + L_FIN + w * 4) = fold;
    }
    bar();
    float bpart = 0.f;
    if (w == 0) {
#pragma unroll
      for (int ww = 0; ww < 8; ++ww) bpart += fr[128 + ww * 16 + c];
      bpart = bfr(bfr(1.f - gate) * bfr(bpart));
      const int po = (rb * 128 + 16 * (lane & 7)) * 2048 + rs * 64;
      const unsigned po_old = *(const unsigned*)(lds + L_FIN + (lane & 7) * 4);
#pragma unroll 1
      for (unsigned spins = 0; spins < SPIN_CAP; ++spins) {
        if (__any((ld32<A_POLL>(rws, po, WS_Y) ^ po_old) & 1u)) break;
        __builtin_amdgcn_s_sleep(SSLEEP);
      }
    }
    GAS u16* op = (GAS u16*)out + rb * 512 + rs * 16 + 4 * (c & 3) + (((c >> 2) & 2) | (((c >> 2) & 1) ^ 1));
    asm volatile("" : "+v"(bpart), "+v"(op));
    bar();
    y = u32x4{0u, 0u, 0u, 0xfeu};
    if (!isb) { POLL_Y(FSLEEP); }
    STAMP(6);
    const float lse = u2f((((y[0] >> 1) & 0x7fu) | (((y[1] >> 1) & 0x7fu) << 7) | (((y[2] >> 1) & 0x7fu) << 14) | (((y[3] >> 1) & 0x7fu) << 21)) << 4);
    float M = fmaxf(lse, dppf<ROR8>(lse)); M = fmaxf(M, dppf<ROR4>(M));
    const float e = isb ? 0.f : ex2(lse - M);
    float L = e + dppf<ROR8>(e); L += dppf<ROR4>(L);
    const float cf = e * __builtin_amdgcn_rcpf(L);
    float v[4];
#pragma unroll
    for (int j = 0; j < 4; ++j) v[j] = cf * u2f(y[j] & ~0xffu);
    SUM_ROWS(v);
    if (lane < 16) fr[w * 16 + lane] = v[0];
    bar();
    if (tid < 16) {
      const float a = ((fr[tid] + fr[16 + tid]) + (fr[32 + tid] + fr[48 + tid])) + ((fr[64 + tid] + fr[80 + tid]) + (fr[96 + tid] + fr[112 + tid]));
      const float o = bfr(gate * bfr(a)) + bpart;
      *op = (u16)pk_bf16(o, o);
    }
    if (rs == 0 && tid == 0) __hip_atomic_store((GAS u64*)(ws + WS_EPOCH + rb * 128), tagr, RLX_AGENT);
  }
  STAMP(7);
    return;
  }
  {
    const int h = __builtin_amdgcn_readfirstlane(4 * x8 + idx / 6), b = __builtin_amdgcn_readfirstlane((idx % 6) / 3), sp = __builtin_amdgcn_readfirstlane(idx % 3);
    const int src = b * 128 + h * 4 + sp;
    const u32x4 sel = make_sel(c);
    const int tb = (b * 32 + h) * 512, t1 = tb + 512 * (sp + 1) / 3, t0 = tb + 512 * sp / 3 + w;
    const rsrc_t rk = mk_rsrc(Kc, t1 * 4096), rv = mk_rsrc(Vc, t1 * 4096), ro = mk_rsrc(wo, (OFFM & 128) ? 0 : 4096 * 512 * 2);
#define LDT(r, i_, j) ld128<AUX_NT>(r, lane * 16 + (j) * 1024, (t0 + 8 * (i_)) * 4096)
#define REQ(a, r, i_) { _Pragma("unroll") for (int j = 0; j < 4; ++j) a[j] = LDT(r, i_, j); SB(); }
#define REQ_REST(a, r) { _Pragma("unroll") for (int j = 0; j < 4; ++j) a[j] = ld128<AUX_NT>(r, lane * 16, (t0 - w + 168) * 4096 + (w + 8 * j) * 1024); SB(); }
#define ARRIVED(a) { _Pragma("unroll") for (int j = 0; j < 4; ++j) asm volatile("" : "+v"(a[j])); SB(); }
    u32x4 W[16], A[4], B[4], C[4], D4[4], E[4], F[4];
    const int wr = __builtin_amdgcn_readfirstlane((w + (idx % 6) * 4 / 3) & 7);
    unsigned x0 = x[64 * wr + lane], x1 = x[512 + 64 * wr + lane];
    LD_EPOCH();
    unsigned old = ld32<A_PRE>(rws, lane * 4, WS_Y + src * 2048 + w * 256);
    const unsigned touch = ld32<AUX_NT>(ro, 0, h * 131072), touch2 = ld32<AUX_NT>(rv, 0, t0 * 4096);
    {
      const rsrc_t rq = mk_rsrc(wq, (OFFM & 64) ? 0 : 512 * 4096 * 2);
#pragma unroll
      for (int i = 0; i < 16; ++i) W[i] = ld128<AUX_NT>(rq, g * (16 * 8192) + c * 16, (64 * wr + i) * 8192 + h * 256);
    }
    SB();
    USE_EPOCH();
    u64 tag = b ? tag1 : tag0;
    asm volatile("" : "+s"(tag));
    asm volatile("" : "+v"(old), "+v"(x0), "+v"(x1) : "v"(touch), "v"(touch2));
    {
      f32x4 D = {0.f, 0.f, 0.f, 0.f};
      rowsum_refill<0>(W, x0 | (x1 << 16), sel, D, [&](auto T) {
        constexpr int j = T & 3;
        if constexpr (T < 4) A[j] = LDT(rk, 0, j); else if constexpr (T < 8) B[j] = LDT(rk, 1, j);
        else if constexpr (T < 12) C[j] = LDT(rk, 2, j); else D4[j] = LDT(rk, 3, j); });
#pragma unroll
      for (int r = 0; r < 4; ++r) red[(wr * 2 + (c >> 3)) * 128 + 32 * g + 8 * r + (c & 7)] = D[r];
    }
    SB();
    REQ(E, rk, 4); REQ_REST(F, rk);
    bar();
    u32x4 q;
    QREAD(q, b);
    STAMP(1);
#define TLREC()
    float* scw = (float*)(lds + L_SC + w * 1536);
    float m = -INFINITY;
#define SCORE(a, i_) { float xs[4]; _Pragma("unroll") for (int j = 0; j < 4; ++j) xs[j] = dot8(a[j], q); \
      float s = bfr(bfr(treduce<4>(xs, c)) * SCALE); \
      if ((i_) == 21) s = (w + 8 * (c >> 2) < 4 * (t1 - t0 + w - 168)) ? s : -INFINITY; \
      m = fmaxf(m, s); scw[(i_) * 16 + (lane >> 2)] = s; asm volatile("" : "+v"(m)); SB(); TLREC(); }
    SCORE(A, 0); REQ(A, rk, 5);
    SCORE(B, 1); SCORE(C, 2); SCORE(D4, 3); SCORE(E, 4); SCORE(F, 21);
    const unsigned img = (unsigned)(uintptr_t)(LAS char*)(lds + L_RING) + w * 16384;
    const int dvo = (lane >> 3) * 1024 + 16 * ((lane & 7) ^ ((lane >> 4 & 1) << 1));
#define DMA8(k_, vo) { unsigned keep; \
      asm volatile("s_mov_b32 %0, m0\n\ts_mov_b32 m0, %1\n\ts_nop 4\n\tbuffer_load_dwordx4 %2, %3, %4 offen nt lds\n\ts_mov_b32 m0, %0" : "=&s"(keep) \
                   : "s"(__builtin_amdgcn_readfirstlane(img + (k_) * 1024)), "v"(vo), "s"(ro), "s"(__builtin_amdgcn_readfirstlane((h * 128 + 8 * (k_)) * 1024 + w * 128)) : "memory"); SB(); }
#pragma unroll 1
    for (int i = 5; i < 21; i += 2) {
      ARRIVED(A); REQ(B, rk, i + 1); DMA8(i - 5, dvo);
      SCORE(A, i);
      const rsrc_t rn = mk_rsrc(i < 19 ? Kc : Vc, t1 * 4096);
      ARRIVED(B); REQ(A, rn, i < 19 ? i + 2 : 0); DMA8(i - 4, dvo ^ 64);
      SCORE(B, i + 1);
    }
    m = xrow_max(row_max(m));
    float l = 0.f;
    {
      const float ms = (m == -INFINITY) ? 0.f : m;
      if (lane < 32) scw[352 + lane] = -INFINITY;
#pragma unroll
      for (int k = 0; k < 6; ++k) { const float p = ex2((scw[64 * k + lane] - ms) * LOG2E); l += p; scw[64 * k + lane] = u2f(hilo(p)); }
      l = xrow_sum(row_sum(l));
    }
    f32x4 O = {0.f, 0.f, 0.f, 0.f};
#define PV(a, i_) { rowsum<4, 4>(a, f2u(scw[(i_) * 16 + (lane >> 2)]), sel, O); asm volatile("" : "+v"(O)); SB(); TLREC(); }
    ARRIVED(A); REQ(B, rv, 1);
    asm volatile("s_waitcnt vmcnt(4)" ::: "memory");
    {
      const int q4 = c >> 2, p4 = lane & 3, phi = (q4 >> 1) | ((g & 1) << 1);
#pragma unroll
      for (int s = 0; s < 4; ++s)
#pragma unroll
        for (int ct = 0; ct < 4; ++ct) {
          const char* a = lds + L_RING + w * 16384 + (32 * s + 8 * g + q4) * 128 + 16 * ((p4 >> 1) | ((ct ^ phi) << 1)) + 8 * (p4 & 1);
          const s16x4 lo = __builtin_amdgcn_ds_read_tr16_b64_v4i16((LAS s16x4*)a), hi = __builtin_amdgcn_ds_read_tr16_b64_v4i16((LAS s16x4*)(a + 512));
          W[4 * s + ct] = __builtin_bit_cast(u32x4, __builtin_shufflevector(lo, hi, 0, 1, 2, 3, 4, 5, 6, 7));
        }
    }
#pragma unroll
    for (int i = 0; i < 16; ++i) asm volatile("" : "+v"(W[i]));
    PV(A, 0);
    ARRIVED(B); REQ(A, rv, 2); PV(B, 1);
#pragma unroll 1
    for (int i = 2; i < 21 - NBOOST; i += 2) {
      ARRIVED(A); REQ(B, rv, i + 1); PV(A, i);
      ARRIVED(B); REQ(A, rv, i + 2); PV(B, i + 1);
    }
    const GAS u64* go = (const GAS u64*)(ws + WS_GO) + h * 16 + b;
    u64 gov;
    if constexpr (NBOOST == 5) {
      ARRIVED(A); REQ(B, rv, 17); REQ(C, rv, 18); REQ(D4, rv, 19); REQ(E, rv, 20); REQ_REST(F, rv);
      gov = __hip_atomic_load(go, RLX_AGENT); SB();
      PV(A, 16); PV(B, 17); PV(C, 18); PV(D4, 19); PV(E, 20); PV(F, 21);
    } else if constexpr (NBOOST == 3) {
      ARRIVED(A); REQ(B, rv, 19); REQ(C, rv, 20); REQ_REST(D4, rv);
      gov = __hip_atomic_load(go, RLX_AGENT); SB();
      PV(A, 18); PV(B, 19); PV(C, 20); PV(D4, 21);
    } else {
      REQ_REST(B, rv);
      gov = __hip_atomic_load(go, RLX_AGENT); SB();
      PV(A, 20); PV(B, 21);
    }
    STAMP(2);
    float* ml = (float*)(lds + L_ML);
#pragma unroll
    for (int r = 0; r < 4; ++r) {
      const unsigned hl = hilo(O[r] + dppf<ROR8>(O[r]));
      *(u16*)(lds + L_P + (2 * w + (c >> 3)) * 272 + (32 * g + 8 * r + (c & 7)) * 2) = (u16)((c & 8) ? (hl >> 16) : hl);
    }
    if (lane == 0) { ml[w] = m; ml[8 + w] = l; }
    bar();
    STAMP(4);
    f32x4 D[4];
    const float mj = ml[lane & 7], lj = ml[8 + (lane & 7)], ma = ml[2 * g], mb = ml[2 * g + 1];
    project(W, lds, g, c, D);
    float M = fmaxf(mj, dppf<XOR7>(mj)); M = fmaxf(M, dppf<XOR2>(M)); M = fmaxf(M, dppf<XOR1>(M));
    const float Ms = (M == -INFINITY) ? 0.f : M;
    float L = ex2((mj - Ms) * LOG2E) * lj; L += dppf<XOR7>(L); L += dppf<XOR2>(L); L += dppf<XOR1>(L);
    const float inv = __builtin_amdgcn_rcpf(L), fa = ex2((ma - Ms) * LOG2E) * inv, fb = ex2((mb - Ms) * LOG2E) * inv;
    float y4[4];
#pragma unroll
    for (int k = 0; k < 4; ++k) y4[k] = fa * (D[k][0] + D[k][1]) + fb * (D[k][2] + D[k][3]);
    const auto s0 = __builtin_amdgcn_permlane32_swap(f2u(y4[0]), f2u(y4[2]), false, false), s1 = __builtin_amdgcn_permlane32_swap(f2u(y4[1]), f2u(y4[3]), false, false);
    const auto s2 = __builtin_amdgcn_permlane16_swap(f2u(u2f(s0[0]) + u2f(s0[1])), f2u(u2f(s1[0]) + u2f(s1[1])), false, false);
    const unsigned code = f2u(Ms * LOG2E + __builtin_amdgcn_logf(L)) >> 4;
    const unsigned yw = (f2u(u2f(s2[0]) + u2f(s2[1])) & ~0xffu) | (((code >> (7 * (lane & 3))) & 0x7fu) << 1) | ((old & 1u) ^ 1u);
    const u32x4 yv = {yw, f2u(dppf<0x55>(u2f(yw))), f2u(dppf<0xAA>(u2f(yw))), f2u(dppf<0xFF>(u2f(yw)))};
#pragma unroll 1
    for (unsigned spins = 0; !(OFFM & 256) && uniform64(gov) != tag && spins < SPIN_CAP; ++spins) gov = __hip_atomic_load(go, RLX_AGENT);
    if ((lane & 3) == 0) __builtin_amdgcn_raw_buffer_store_b128(yv, rws, lane * 4, WS_Y + src * 2048 + w * 256, A_YST);
    STAMP(5); STAMP(6); STAMP(3);
  }
}

extern "C" void kernel_launch(void* const* d_in, const int* in_sizes, int n_in, void* d_out, int out_size, void* d_ws, size_t ws_size, hipStream_t stream) {
  fused_decode<<<256, 512, 0, stream>>>((const u16*)d_in[0], (const u16*)d_in[1], (const u16*)d_in[2], (const u16*)d_in[3], (const u16*)d_in[4],
                                        (const u16*)d_in[6], (const u16*)d_in[7], (const u16*)d_in[8], (const u16*)d_in[9], (const u16*)d_in[10],
                                        (const u16*)d_in[11], (u16*)d_out, (char*)d_ws);
}
```

```cpp
#include <hip/hip_runtime.h>
#include <stdint.h>
#include <type_traits>

typedef unsigned short u16;
typedef unsigned long long u64;
typedef unsigned u32x4 __attribute__((ext_vector_type(4)));
typedef unsigned u32x2 __attribute__((ext_vector_type(2)));
typedef float f32x4 __attribute__((ext_vector_type(4)));
typedef float f32x16 __attribute__((ext_vector_type(16)));
typedef __bf16 bf16x8 __attribute__((ext_vector_type(8)));
typedef __bf16 bf16x2 __attribute__((ext_vector_type(2)));
typedef short s16x4 __attribute__((ext_vector_type(4)));
typedef __amdgpu_buffer_rsrc_t rsrc_t;
#define GAS __attribute__((address_space(1)))
#define LAS __attribute__((address_space(3)))
#define DI __device__ __forceinline__
#define RLX_AGENT __ATOMIC_RELAXED, __HIP_MEMORY_SCOPE_AGENT

#define NL 4
#define OFFM 0
#define TL 0
#define STAG 0
#define NDMA 8
#define SSLEEP 24
#define PB_ST 18
#define PB_LD 18
#define BOOST 1
#define EBAR 0
#define HROT 0
#define HMAP 0
#define DEEP1 0
#define DEEP0 0
#define WROT 1
#define TROT 0
#define ILV 0
#define COOP 0
#define PRE4 2
#define M2 0
#define A_PRE 18
#define A_QA 16
#define A_ST 16
#define A_POLL 16
#define A_YST 16
#define WSLEEP 3
#define OUTSC1 0
#define BDELAY 0
#define NBOOST 1
#define TOUCH 1
#define DEEP 0
#define FSLEEP 0
#define STAMPS 0
constexpr int AUX_NT = 2, AUX_SC1 = 16;
constexpr float SCALE = 0.08838834764831845f;
constexpr float LOG2E = 1.4426950408889634f;

constexpr int WS_EPOCH = 0;
constexpr int WS_F1 = 1024;
constexpr int WS_F2 = 2048;
constexpr int WS_GO = 4096;
constexpr int WS_QA = 8192;
constexpr int WS_ML = WS_QA + 65536;
constexpr int WS_PB = WS_ML + 8192;
constexpr int WS_Y = WS_PB + 2 * 32 * 16 * 1024;
constexpr int WS_END = WS_Y + 2 * 128 * 2048;
constexpr int WS_STAMP = 4 << 20;

constexpr int L_RING = 0;
constexpr int L_RED = 131072;
constexpr int L_P = L_RED + 8192;
constexpr int L_MX = L_P + 32 * 144;
constexpr int L_ML = L_MX + 512;
constexpr int L_FIN = L_ML + 512;
constexpr int L_SC = 147456;
constexpr int L_QA = 131072;
constexpr int L_END = 163840;

DI rsrc_t mk_rsrc(const void* p, unsigned bytes) { return __builtin_amdgcn_make_buffer_rsrc((void*)p, 0, bytes, 0x00020000); }
template <int AUX> DI u32x4 ld128(rsrc_t r, int voff, int soff) { return __builtin_amdgcn_raw_buffer_load_b128(r, voff, soff, AUX); }
template <int AUX> DI u32x2 ld64(rsrc_t r, int voff, int soff) { return __builtin_amdgcn_raw_buffer_load_b64(r, voff, soff, AUX); }
template <int AUX> DI unsigned ld32(rsrc_t r, int voff, int soff) { return __builtin_amdgcn_raw_buffer_load_b32(r, voff, soff, AUX); }
DI float u2f(unsigned u) { return __builtin_bit_cast(float, u); }
DI unsigned f2u(float f) { return __builtin_bit_cast(unsigned, f); }
DI unsigned pk_bf16(float lo, float hi) { bf16x2 r; r[0] = (__bf16)lo; r[1] = (__bf16)hi; return __builtin_bit_cast(unsigned, r); }
DI float bfr(float x) { return u2f(pk_bf16(x, x) << 16); }
DI unsigned hilo(float x) { const float h = bfr(x); return pk_bf16(h, x - h); }
DI float ex2(float x) { return __builtin_amdgcn_exp2f(x); }
DI float dot2(unsigned a, unsigned b, float c) { return __builtin_amdgcn_fdot2_f32_bf16(__builtin_bit_cast(bf16x2, a), __builtin_bit_cast(bf16x2, b), c, false); }
DI float dot8(const u32x4& a, const u32x4& b) { return dot2(a[3], b[3], dot2(a[2], b[2], dot2(a[1], b[1], dot2(a[0], b[0], 0.f)))); }
template <int CTRL> DI float dppf(float x) { return u2f(__builtin_amdgcn_mov_dpp(f2u(x), CTRL, 0xf, 0xf, true)); }
template <int T> DI unsigned bcast16(unsigned v) { return __builtin_amdgcn_mov_dpp(v, 0x150 + T, 0xf, 0xf, true); }
constexpr int XOR1 = 0xB1, XOR2 = 0x4E, XOR7 = 0x141, ROR8 = 0x128, ROR4 = 0x124;
DI float xrow_max(float x) {
  auto s = __builtin_amdgcn_permlane16_swap(f2u(x), f2u(x), false, false); x = fmaxf(u2f(s[0]), u2f(s[1]));
  auto t = __builtin_amdgcn_permlane32_swap(f2u(x), f2u(x), false, false); return fmaxf(u2f(t[0]), u2f(t[1]));
}
DI float xrow_sum(float x) {
  auto s = __builtin_amdgcn_permlane16_swap(f2u(x), f2u(x), false, false); x = u2f(s[0]) + u2f(s[1]);
  auto t = __builtin_amdgcn_permlane32_swap(f2u(x), f2u(x), false, false); return u2f(t[0]) + u2f(t[1]);
}
DI float row_max(float x) { x = fmaxf(x, dppf<ROR8>(x)); x = fmaxf(x, dppf<ROR4>(x)); x = fmaxf(x, dppf<XOR2>(x)); return fmaxf(x, dppf<XOR1>(x)); }
DI float row_sum(float x) { x += dppf<ROR8>(x); x += dppf<ROR4>(x); x += dppf<XOR2>(x); return x + dppf<XOR1>(x); }
DI f32x4 mfma16(u32x4 a, u32x4 b, f32x4 c) { return __builtin_amdgcn_mfma_f32_16x16x32_bf16(__builtin_bit_cast(bf16x8, a), __builtin_bit_cast(bf16x8, b), c, 0, 0, 0); }
DI f32x16 mfma32(u32x4 a, u32x4 b, f32x16 c) { return __builtin_amdgcn_mfma_f32_32x32x16_bf16(__builtin_bit_cast(bf16x8, a), __builtin_bit_cast(bf16x8, b), c, 0, 0, 0); }
DI u64 uniform64(u64 v) { return ((u64)(unsigned)__builtin_amdgcn_readfirstlane((unsigned)(v >> 32)) << 32) | (unsigned)__builtin_amdgcn_readfirstlane((unsigned)v); }
DI u64 next_tag(u64 e) { return e * 0x9E3779B97F4A7C15ull + 0x632BE59BD9B4E019ull; }
DI void bar() { __syncthreads(); }
#define DRAIN() asm volatile("s_waitcnt vmcnt(0)" ::: "memory")
#define PIN() asm volatile("" ::: "memory")
#define SB() __builtin_amdgcn_sched_barrier(0)
constexpr unsigned SPIN_CAP = 1u << 22;

template <int N, int STEP, int T = 0> DI void rowsum(const u32x4 (&W)[N], unsigned pair, const u32x4& sel, f32x4& D) {
  if constexpr (T < N) {
    const unsigned s = bcast16<T * STEP>(pair);
    const u32x4 B = {__builtin_amdgcn_perm(s, s, sel[0]), __builtin_amdgcn_perm(s, s, sel[1]), __builtin_amdgcn_perm(s, s, sel[2]), __builtin_amdgcn_perm(s, s, sel[3])};
    D = mfma16(W[T], B, D);
    rowsum<N, STEP, T + 1>(W, pair, sel, D);
  }
}
template <int N, int I = 0, class F> DI void static_for(F&& f) { if constexpr (I < N) { f(std::integral_constant<int, I>{}); static_for<N, I + 1>(f); } }
template <int T, class F> DI void rowsum_refill(const u32x4 (&W)[16], unsigned pair, const u32x4& sel, f32x4& D, F&& issue) {
  if constexpr (T < 16) {
    const unsigned s = bcast16<T>(pair);
    const u32x4 B = {__builtin_amdgcn_perm(s, s, sel[0]), __builtin_amdgcn_perm(s, s, sel[1]), __builtin_amdgcn_perm(s, s, sel[2]), __builtin_amdgcn_perm(s, s, sel[3])};
    D = mfma16(W[T], B, D);
    asm volatile("" : "+v"(D));
    __builtin_amdgcn_sched_barrier(0);
    issue(std::integral_constant<int, T>{});
    __builtin_amdgcn_sched_barrier(0);
    rowsum_refill<T + 1>(W, pair, sel, D, issue);
  }
}
DI int swz16(int row) { const int u = (row >> 2) & 3; return ((row & 3) << 2) | (((u ^ (u >> 1)) & 1) << 1) | (u >> 1); }
DI u32x4 make_sel(int c) {
  const int j = c & 7; const unsigned b = (c & 8) ? 0x0302u : 0x0100u;
  const unsigned v = (j & 1) ? ((b << 16) | 0x0c0cu) : (0x0c0c0000u | b);
  u32x4 s;
#pragma unroll
  for (int w = 0; w < 4; ++w) s[w] = (w == (j >> 1)) ? v : 0x0c0c0c0cu;
  return s;
}
template <int N> DI float treduce(float (&x)[N], int c) {
#define TR_STEP(CNT, CTRL, BIT) _Pragma("unroll") for (int i = 0; i < (CNT); ++i) { const float a = x[i] + dppf<CTRL>(x[i]), b = x[i + (CNT)] + dppf<CTRL>(x[i + (CNT)]); x[i] = (c & (BIT)) ? b : a; }
  TR_STEP(N / 2, ROR8, 8)
  TR_STEP(N / 4, XOR7, 4)
  if constexpr (N >= 8) { TR_STEP(N / 8, XOR2, 2) } else { x[0] += dppf<XOR2>(x[0]); }
  if constexpr (N >= 16) { TR_STEP(N / 16, XOR1, 1) } else { x[0] += dppf<XOR1>(x[0]); }
#undef TR_STEP
  return x[0];
}
DI void wait_flags32(const GAS u64* f, u64 tag, int lane) {
#pragma unroll 1
  for (unsigned spins = 0; spins < SPIN_CAP; ++spins) {
    if (__all(__hip_atomic_load(f + (lane & 31), RLX_AGENT) == tag)) break;
    __builtin_amdgcn_s_sleep(WSLEEP);
  }
  __builtin_amdgcn_fence(__ATOMIC_ACQUIRE, "wavefront");
}
DI void transpose_w(const u32x2 (&R)[32], u32x4 (&F)[16]) {
#pragma unroll
  for (int s = 0; s < 4; ++s)
#pragma unroll
    for (int t = 0; t < 4; ++t)
#pragma unroll
      for (int jj = 0; jj < 4; ++jj) F[4 * s + t][jj] = __builtin_amdgcn_perm(R[8 * s + 2 * jj + 1][t >> 1], R[8 * s + 2 * jj][t >> 1], (t & 1) ? 0x07060302u : 0x05040100u);
}
DI void project(const u32x4 (&F)[16], const char* lds, int g, int c, f32x4 (&D)[4]) {
  u32x4 A[4];
#pragma unroll
  for (int s = 0; s < 4; ++s) A[s] = *(const u32x4*)(lds + L_P + c * 272 + 64 * s + 16 * g);
#pragma unroll
  for (int t = 0; t < 4; ++t) D[t] = f32x4{0.f, 0.f, 0.f, 0.f};
#pragma unroll
  for (int s = 0; s < 4; ++s)
#pragma unroll
    for (int t = 0; t < 4; ++t) D[t] = mfma16(A[s], F[4 * s + t], D[t]);
}
DI unsigned lsb4(const u32x4& v) { return (v[0] & 1u) | ((v[1] & 1u) << 1) | ((v[2] & 1u) << 2) | ((v[3] & 1u) << 3); }
DI unsigned mark(float v, unsigned old, int k) { return (f2u(v) & ~1u) | (((old >> k) & 1u) ^ 1u); }

#define STAMP(k) do {} while (0)

__global__ void __launch_bounds__(512, 2)
fused_decode(const u16* __restrict__ x, const u16* __restrict__ Kc, const u16* __restrict__ Vc, const u16* __restrict__ wq, const u16* __restrict__ wo,
             const u16* __restrict__ Cc, const u16* __restrict__ w_q, const u16* __restrict__ w_uk, const u16* __restrict__ w_uv, const u16* __restrict__ w_o,
             const u16* __restrict__ gw, u16* __restrict__ out, char* ws) {
  __shared__ __attribute__((aligned(16))) char lds[L_END];
  const int bid = blockIdx.x, tid = threadIdx.x, lane = tid & 63, w = __builtin_amdgcn_readfirstlane(tid >> 6), g = lane >> 4, c = lane & 15;
  const int x8 = bid & 7, idx = bid >> 3;
  STAMP(0);
  const u64 t_start = (OFFM & (1024 | 2048)) ? __builtin_amdgcn_s_memrealtime() : 0;
  asm volatile("" :: "s"(x), "s"(Kc), "s"(Vc), "s"(wq), "s"(wo), "s"(ws));
  const rsrc_t rws = mk_rsrc(ws, WS_END);
  float* red = (float*)(lds + L_RED);
#define LD_EPOCH() u64 e0 = __hip_atomic_load((GAS u64*)(ws + WS_EPOCH), RLX_AGENT), e1 = __hip_atomic_load((GAS u64*)(ws + WS_EPOCH + 128), RLX_AGENT);
#define USE_EPOCH() asm volatile("" : "+v"(e0), "+v"(e1)); const u64 tag0 = next_tag(uniform64(e0)), tag1 = next_tag(uniform64(e1));
#define LD_ROWS(W, ptr, OFF) { const rsrc_t rq = mk_rsrc(ptr, (OFF) ? 0 : 512 * 4096 * 2); \
    _Pragma("unroll") for (int t = 0; t < 16; ++t) W[t] = ld128<AUX_NT>(rq, g * (16 * 8192) + c * 16, (64 * w + t) * 8192 + h * 256); }
#define LD_OUTW(R, ptr, OFF) { const rsrc_t ro = mk_rsrc(ptr, (OFF) ? 0 : 4096 * 512 * 2); \
    _Pragma("unroll") for (int i = 0; i < 32; ++i) R[i] = ld64<AUX_NT>(ro, g * 8192 + c * 8 + (i & 3) * 1024, (h * 128 + 32 * (i >> 3) + (i & 4)) * 1024 + w * 128); }
#define QPROJ() { asm volatile("" : "+v"(x0), "+v"(x1)); f32x4 D = {0.f, 0.f, 0.f, 0.f}; rowsum<16, 1>(W, x0 | (x1 << 16), sel, D); \
    _Pragma("unroll") for (int r = 0; r < 4; ++r) red[(w * 2 + (c >> 3)) * 128 + 32 * g + 8 * r + (c & 7)] = D[r]; }
#define QREAD(q, bb) { f32x4 a0 = {0.f, 0.f, 0.f, 0.f}, a1 = a0; \
    _Pragma("unroll") for (int ww = 0; ww < 8; ++ww) { const f32x4* p = (const f32x4*)(red + (ww * 2 + (bb)) * 128 + 8 * c); a0 += p[0]; a1 += p[1]; } \
    q = u32x4{pk_bf16(a0[0], a0[1]), pk_bf16(a0[2], a0[3]), pk_bf16(a1[0], a1[1]), pk_bf16(a1[2], a1[3])}; }

  if (idx >= 24) {
  if (OFFM & 256) { STAMP(1); STAMP(2); STAMP(3); STAMP(4); STAMP(5); STAMP(6); STAMP(7); return; }
  const int rb = idx >= 28, rs = __builtin_amdgcn_readfirstlane(4 * x8 + (idx & 3));
  const int fy_off = (rb * 128 + (tid >> 2)) * 2048 + rs * 64 + (tid & 3) * 16;
  unsigned fold; u64 tagr;
  float gate;
#define LD_GATE() const u32x4 xv = *(const u32x4*)(x + rb * 512 + lane * 8), gv = *(const u32x4*)(gw + lane * 8);
#define USE_GATE() { float s = 0.f; \
    _Pragma("unroll") for (int j = 0; j < 4; ++j) s += bfr(u2f(xv[j] << 16) * u2f(gv[j] << 16)) + bfr(u2f(xv[j] & 0xffff0000u) * u2f(gv[j] & 0xffff0000u)); \
    s = bfr(xrow_sum(row_sum(s))); gate = bfr(1.f / (1.f + __expf(-s))); }

  if (idx < 28) {
    const int h = rs;
    const u32x4 sel = make_sel(c);
    u32x4 W[16], U[16];
    unsigned x0 = x[tid], x1 = x[512 + tid];
    unsigned tch = 0;
    if (TOUCH) tch = ld32<AUX_NT>(mk_rsrc(w_uk, 4 << 20), (lane & 1) << 21, h * 256) ^ ld32<AUX_NT>(mk_rsrc(w_uv, 4 << 20), (lane & 1) << 21, h * 256) ^ ld32<AUX_NT>(mk_rsrc(w_o, 4 << 20), 0, h * 131072);
    LD_ROWS(W, w_q, OFFM & 1);
    SB();
    {
      asm volatile("" : "+v"(x0), "+v"(x1) : "v"(tch));
      const unsigned xp = x0 | (x1 << 16);
      const rsrc_t ru = mk_rsrc(w_uk, (OFFM & 2) ? 0 : 512 * 4096 * 2);
      f32x4 D = {0.f, 0.f, 0.f, 0.f};
      rowsum_refill<0>(W, xp, sel, D, [&](auto T) { U[T] = ld128<AUX_NT>(ru, g * (16 * 8192) + c * 16, (64 * w + T) * 8192 + h * 256); });
#pragma unroll
      for (int r = 0; r < 4; ++r) red[(w * 2 + (c >> 3)) * 128 + 32 * g + 8 * r + (c & 7)] = D[r];
    }
    LD_EPOCH();
    const u32x4 fy = ld128<A_PRE>(rws, fy_off, WS_Y);
    const u32x4 oy0 = ld128<A_PRE>(rws, c * 16, WS_Y + (h * 4 + 3) * 2048 + w * 256), oy1 = ld128<A_PRE>(rws, c * 16, WS_Y + (128 + h * 4 + 3) * 2048 + w * 256);
    LD_GATE();
    SB();
    bar();
#pragma unroll
    for (int bb = 0; bb < 2; ++bb) {
      u32x4 q; QREAD(q, bb);
      float xs[16];
#pragma unroll
      for (int t = 0; t < 16; ++t) xs[t] = dot8(U[t], q);
      const float qa = treduce<16>(xs, c);
      *(u16*)(lds + L_RING + bb * 1024 + tid * 2) = (u16)pk_bf16(qa, qa);
    }
    SB();
    USE_EPOCH(); USE_GATE();
    fold = lsb4(fy);
    unsigned old = lsb4(oy0) | (lsb4(oy1) << 4);
    asm volatile("" : "+v"(fold), "+v"(gate), "+v"(old));
    tagr = tag0;
    DRAIN();
    bar();
    if (w == 0) {
      __builtin_amdgcn_raw_buffer_store_b128(*(const u32x4*)(lds + L_RING + lane * 16), rws, lane * 16, WS_QA + h * 1024, A_ST);
      __builtin_amdgcn_raw_buffer_store_b128(*(const u32x4*)(lds + L_RING + 1024 + lane * 16), rws, lane * 16, WS_QA + (32 + h) * 1024, A_ST);
      DRAIN();
      if (lane == 0) __hip_atomic_store((GAS u64*)(ws + WS_F1) + h, tag0, RLX_AGENT);
    }
    STAMP(1);
    {
      u32x2 R[32];
      LD_OUTW(R, w_o, OFFM & 16);
      LD_ROWS(W, w_uv, OFFM & 8);
      SB();
      transpose_w(R, U);
    }
#pragma unroll
    for (int i = 0; i < 16; ++i) asm volatile("" : "+v"(W[i]), "+v"(U[i]));
    STAMP(2);
    if (w == 0) {
      if (OFFM & 2048) { while (__builtin_amdgcn_s_memrealtime() - t_start < 3900) __builtin_amdgcn_s_sleep(8); }
      wait_flags32((const GAS u64*)(ws + WS_F2), tag0, lane);
      if (lane < 2) __hip_atomic_store((GAS u64*)(ws + WS_GO) + h * 16 + lane, lane ? tag1 : tag0, RLX_AGENT);
    }
    bar();
    STAMP(3);
    u32x4 T[4];
#pragma unroll
    for (int k = 0; k < 4; ++k) T[k] = ld128<PB_LD>(mk_rsrc(ws, (OFFM & 32) ? 0 : WS_END), lane * 16, WS_PB + (((k >> 1) * 32 + h) * 16 + 2 * w + (k & 1)) * 1024);
    const u32x2 mlw = ld64<PB_LD>(rws, (g & 1) * 4096 + c * 8, WS_ML + h * 128);
    const float ms = u2f(mlw[0]), M0 = row_max(ms), M = (M0 == -INFINITY) ? 0.f : M0;
    const float wt = ex2((ms - M) * LOG2E) * u2f(mlw[1]), cf = wt / row_sum(wt);
    float* cr = (float*)(lds + L_RING);
#pragma unroll
    for (int bb = 0; bb < 2; ++bb) {
      const float f0 = u2f(__builtin_amdgcn_readlane(f2u(cf), 16 * bb + 2 * w)), f1 = u2f(__builtin_amdgcn_readlane(f2u(cf), 16 * bb + 2 * w + 1));
      float a[8];
#pragma unroll
      for (int j = 0; j < 4; ++j) {
        a[2 * j] = f0 * u2f(T[2 * bb][j] << 16) + f1 * u2f(T[2 * bb + 1][j] << 16);
        a[2 * j + 1] = f0 * u2f(T[2 * bb][j] & 0xffff0000u) + f1 * u2f(T[2 * bb + 1][j] & 0xffff0000u);
      }
      *(f32x4*)(cr + (w * 2 + bb) * 512 + lane * 8) = f32x4{a[0], a[1], a[2], a[3]};
      *(f32x4*)(cr + (w * 2 + bb) * 512 + lane * 8 + 4) = f32x4{a[4], a[5], a[6], a[7]};
    }
    bar();
    {
      float c0 = 0.f, c1 = 0.f;
#pragma unroll
      for (int ww = 0; ww < 8; ++ww) { c0 += cr[ww * 1024 + tid]; c1 += cr[ww * 1024 + 512 + tid]; }
      const unsigned h0 = hilo(c0), h1 = hilo(c1);
      f32x4 D = {0.f, 0.f, 0.f, 0.f}, D2 = D;
      rowsum<16, 1>(W, __builtin_amdgcn_perm(h1, h0, 0x05040100u), sel, D);
      rowsum<16, 1>(W, __builtin_amdgcn_perm(h1, h0, 0x07060302u), sel, D2);
      D += D2;
#pragma unroll
      for (int r = 0; r < 4; ++r) red[(w * 2 + (c >> 3)) * 128 + 32 * g + 8 * r + (c & 7)] = D[r];
    }
    bar();
    if (tid < 256) {
      float v = 0.f;
#pragma unroll
      for (int ww = 0; ww < 8; ++ww) v += red[ww * 256 + tid];
      *(u16*)(lds + L_P + (tid >> 7) * 272 + (tid & 127) * 2) = (u16)pk_bf16(v, v);
    }
    bar();
    f32x4 D[4];
    project(U, lds, g, c & 1, D);
    if (g == 0) {
      __builtin_amdgcn_raw_buffer_store_b128(u32x4{mark(D[0][0], old, 0), mark(D[1][0], old, 1), mark(D[2][0], old, 2), mark(D[3][0], old, 3)}, rws, c * 16, WS_Y + (h * 4 + 3) * 2048 + w * 256, A_ST);
      __builtin_amdgcn_raw_buffer_store_b128(u32x4{mark(D[0][1], old, 4), mark(D[1][1], old, 5), mark(D[2][1], old, 6), mark(D[3][1], old, 7)}, rws, c * 16, WS_Y + (128 + h * 4 + 3) * 2048 + w * 256, A_ST);
    }
    STAMP(4);
  } else {
    const int b = rs & 1, sp = rs >> 1;
    u32x4 W[16], S0[8], S1[8];
#define LDC(st, i) ld128<AUX_NT>(mk_rsrc(Cc, ((st) < 8 && !(OFFM & 4)) ? (16u << 20) : 0u), lane * 16 + ((i) & 3) * 1024, (b * 8192 + sp * 512 + ((st) & 7) * 64 + 8 * w + ((i) & 4)) * 1024)
#define LDS_(S, st) _Pragma("unroll") for (int i = 0; i < 8; ++i) S[i] = LDC(st, i);
#define STC(slot, S) { int ln = lane; asm volatile("" : "+v"(ln)); char* base = lds + L_RING + (slot) * 65536 + (ln >> 4) * 16384 + 8 * w * 256; \
      _Pragma("unroll") for (int i = 0; i < 8; ++i) *(u32x4*)(base + i * 256 + 16 * ((ln & 15) ^ swz16(8 * w + i))) = S[i]; }
    LD_EPOCH();
    const u32x4 fy = ld128<A_PRE>(rws, fy_off, WS_Y);
    LD_GATE();
    LDS_(S0, 0);
    LDS_(S1, 1);
    SB();
    USE_EPOCH(); USE_GATE();
    fold = lsb4(fy);
    asm volatile("" : "+v"(fold), "+v"(gate));
    tagr = tag1;
    STC(0, S0);
    SB();
    LDS_(S0, 2);
    SB();
    STC(1, S1);
    SB();
    LDS_(S1, 3);
    PIN();
    STAMP(1);
    if (w == 0) wait_flags32((const GAS u64*)(ws + WS_F1), tag0, lane);
    bar();
#pragma unroll
    for (int k = 0; k < 4; ++k) *(u32x4*)(lds + L_QA + (4 * w + k) * 1024 + 16 * (lane ^ swz16(4 * w + k))) = ld128<A_QA>(rws, lane * 16, WS_QA + (b * 32 + 4 * w + k) * 1024);
    bar();
    const int kt = w & 3, hh = w >> 2;
#pragma unroll
    for (int s = 0; s < 16; ++s) W[s] = *(const u32x4*)(lds + L_QA + (16 * hh + c) * 1024 + 16 * ((4 * s + g) ^ swz16(c)));
#pragma unroll
    for (int s = 0; s < 16; ++s) asm volatile("" : "+v"(W[s]));
    bar();
    STAMP(2);
    float mq = -INFINITY, lq = 0.f, mp = -INFINITY;
    f32x16 A0, A1;
#pragma unroll
    for (int i = 0; i < 16; ++i) { A0[i] = 0.f; A1[i] = 0.f; }
    float* mx = (float*)(lds + L_MX);
#define STAGE(slot, REFILL) { const char* tile = lds + L_RING + (slot) * 65536; \
      int ln = lane; asm volatile("" : "+v"(ln)); \
      const int g = ln >> 4, c = ln & 15, swz = swz16(c), h5 = ln >> 5, hq = 16 * hh + c, hp = ln & 31; \
      const char* rowp = tile + (16 * kt + c) * 256; \
      f32x4 X0 = {0.f, 0.f, 0.f, 0.f}, X1 = X0; \
      { u32x4 fa[4], fb[4]; \
        _Pragma("unroll") for (int j = 0; j < 4; ++j) fa[j] = *(const u32x4*)(rowp + 16 * ((4 * j + g) ^ swz)); \
        _Pragma("unroll") for (int j = 0; j < 4; ++j) fb[j] = *(const u32x4*)(rowp + 16384 + 16 * ((4 * j + g) ^ swz)); \
        SB(); \
        X0 = mfma16(fa[0], W[0], X0); X1 = mfma16(fa[1], W[1], X1); X0 = mfma16(fa[2], W[2], X0); X1 = mfma16(fa[3], W[3], X1); \
        SB(); \
        _Pragma("unroll") for (int j = 0; j < 4; ++j) fa[j] = *(const u32x4*)(rowp + 32768 + 16 * ((4 * j + g) ^ swz)); \
        SB(); \
        X0 = mfma16(fb[0], W[4], X0); X1 = mfma16(fb[1], W[5], X1); X0 = mfma16(fb[2], W[6], X0); X1 = mfma16(fb[3], W[7], X1); \
        SB(); \
        _Pragma("unroll") for (int j = 0; j < 4; ++j) fb[j] = *(const u32x4*)(rowp + 49152 + 16 * ((4 * j + g) ^ swz)); \
        SB(); \
        X0 = mfma16(fa[0], W[8], X0); X1 = mfma16(fa[1], W[9], X1); X0 = mfma16(fa[2], W[10], X0); X1 = mfma16(fa[3], W[11], X1); \
        X0 = mfma16(fb[0], W[12], X0); X1 = mfma16(fb[1], W[13], X1); X0 = mfma16(fb[2], W[14], X0); X1 = mfma16(fb[3], W[15], X1); } \
      float sc[4]; \
      _Pragma("unroll") for (int r = 0; r < 4; ++r) sc[r] = bfr(bfr(X0[r] + X1[r]) * SCALE); \
      const float tm = xrow_max(fmaxf(fmaxf(sc[0], sc[1]), fmaxf(sc[2], sc[3]))); \
      if (g == 0) mx[kt * 32 + hq] = tm; \
      bar(); \
      { const float q0 = mx[hq], q1 = mx[32 + hq], q2 = mx[64 + hq], q3 = mx[96 + hq], p0 = mx[hp], p1 = mx[32 + hp], p2 = mx[64 + hp], p3 = mx[96 + hp]; \
        const float mn = fmaxf(fmaxf(mq, fmaxf(q0, q1)), fmaxf(q2, q3)), ms = (mn == -INFINITY) ? 0.f : mn; \
        float p[4]; \
        _Pragma("unroll") for (int r = 0; r < 4; ++r) p[r] = ex2((sc[r] - ms) * LOG2E); \
        *(u32x2*)(lds + L_P + hq * 144 + (16 * kt + 4 * g) * 2) = u32x2{pk_bf16(p[0], p[1]), pk_bf16(p[2], p[3])}; \
        lq = lq * ex2((mq - ms) * LOG2E) + ((p[0] + p[1]) + (p[2] + p[3])); \
        mq = mn; \
        const float mn2 = fmaxf(fmaxf(mp, fmaxf(p0, p1)), fmaxf(p2, p3)), ms2 = (mn2 == -INFINITY) ? 0.f : mn2; \
        const float al = ex2((mp - ms2) * LOG2E); mp = mn2; A0 *= al; A1 *= al; } \
      SB(); REFILL; SB(); \
      { const int blk = (ln >> 4) & 1, q4 = (ln & 15) >> 2, p4 = ln & 3; \
        const char* trp = tile + (w >> 1) * 16384 + (8 * h5 + q4) * 256 + 8 * (p4 & 1); \
        const int ch0 = 8 * (w & 1) + 2 * blk + (p4 >> 1), z0 = swz16(8 * h5 + q4), z1 = swz16(8 * h5 + 4 + q4); \
        s16x4 f[4][2][2]; \
        _Pragma("unroll") for (int ks = 0; ks < 4; ++ks) _Pragma("unroll") for (int ct = 0; ct < 2; ++ct) { \
          f[ks][ct][0] = __builtin_amdgcn_ds_read_tr16_b64_v4i16((LAS s16x4*)(trp + ks * 4096 + 16 * ((ch0 + 4 * ct) ^ z0))); \
          f[ks][ct][1] = __builtin_amdgcn_ds_read_tr16_b64_v4i16((LAS s16x4*)(trp + ks * 4096 + 1024 + 16 * ((ch0 + 4 * ct) ^ z1))); } \
        SB(); \
        bar(); \
        u32x4 P[4]; \
        _Pragma("unroll") for (int ks = 0; ks < 4; ++ks) P[ks] = *(const u32x4*)(lds + L_P + hp * 144 + (16 * ks + 8 * h5) * 2); \
        SB(); \
        _Pragma("unroll") for (int ks = 0; ks < 4; ++ks) { \
          A0 = mfma32(__builtin_bit_cast(u32x4, __builtin_shufflevector(f[ks][0][0], f[ks][0][1], 0, 1, 2, 3, 4, 5, 6, 7)), P[ks], A0); \
          A1 = mfma32(__builtin_bit_cast(u32x4, __builtin_shufflevector(f[ks][1][0], f[ks][1][1], 0, 1, 2, 3, 4, 5, 6, 7)), P[ks], A1); } \
        SB(); } }
    STAGE(0, ;);
    STAMP(8);
#pragma unroll 1
    for (int it = 0; it < 3; ++it) {
      STAGE(1, { STC(0, S0); SB(); LDS_(S0, 2 * it + 4); });
      STAGE(0, { STC(1, S1); SB(); LDS_(S1, 2 * it + 5); });
      STAMP(10 + it);
    }
    STAGE(1, ;);
    STAMP(13);
    const int h5 = lane >> 5, hq = 16 * hh + c, hp = lane & 31;
    float* lx = (float*)(lds + L_ML);
    lq = xrow_sum(lq);
    if (g == 0) lx[kt * 32 + hq] = lq;
    bar();
    const float lt = (lx[hp] + lx[32 + hp]) + (lx[64 + hp] + lx[96 + hp]);
    const float inv = 1.f / lt;
    char* img = lds + L_RING + hp * 1040 + (64 * w + 4 * h5) * 2;
#pragma unroll
    for (int k = 0; k < 4; ++k) {
      *(u32x2*)(img + 16 * k) = u32x2{pk_bf16(A0[4 * k] * inv, A0[4 * k + 1] * inv), pk_bf16(A0[4 * k + 2] * inv, A0[4 * k + 3] * inv)};
      *(u32x2*)(img + 64 + 16 * k) = u32x2{pk_bf16(A1[4 * k] * inv, A1[4 * k + 1] * inv), pk_bf16(A1[4 * k + 2] * inv, A1[4 * k + 3] * inv)};
    }
    if (tid < 32) __builtin_amdgcn_raw_buffer_store_b64(u32x2{f2u(mp), f2u(lt)}, mk_rsrc(ws, (OFFM & 512) ? 0 : WS_END), hp * 128, WS_ML + b * 4096 + sp * 8, AUX_SC1);
    bar();
#pragma unroll
    for (int k = 0; k < 4; ++k)
      __builtin_amdgcn_raw_buffer_store_b128(*(const u32x4*)(lds + L_RING + (4 * w + k) * 1040 + lane * 16), mk_rsrc(ws, (OFFM & 32) ? 0 : WS_END), lane * 16, WS_PB + ((b * 32 + 4 * w + k) * 16 + sp) * 1024, PB_ST);
    DRAIN();
    bar();
    if (tid == 0) __hip_atomic_store((GAS u64*)(ws + WS_F2) + rs, tag0, RLX_AGENT);
    STAMP(3); STAMP(4);
  }
  STAMP(5);
  {
    const bool isb = (lane & 12) == 12;
    float* fr = (float*)(lds + L_RING);
    u32x4 y = {0u, 0u, 0u, 0u};
#define POLL_Y(SLP) _Pragma("unroll 1") for (unsigned spins = 0; spins < SPIN_CAP; ++spins) { \
      y = ld128<A_POLL>(rws, fy_off, WS_Y); if ((lsb4(y) ^ fold) == 15u) break; __builtin_amdgcn_s_sleep(SLP); }
#define SUM_ROWS(v) { const float a0 = v[0] + dppf<ROR8>(v[0]), a1 = v[1] + dppf<ROR8>(v[1]), a2 = v[2] + dppf<ROR8>(v[2]), a3 = v[3] + dppf<ROR8>(v[3]); \
      const float b0 = (c & 8) ? a2 : a0, b1 = (c & 8) ? a3 : a1; \
      const float c0 = b0 + dppf<0x124>(b0), c1 = b1 + dppf<0x12C>(b1); \
      v[0] = xrow_sum((c & 4) ? c0 : c1); }
    if (isb) { POLL_Y(16); }
    {
      float v[4];
#pragma unroll
      for (int j = 0; j < 4; ++j) v[j] = u2f(y[j] & ~0xffu);
      SUM_ROWS(v);
      if (lane < 16) fr[128 + w * 16 + lane] = v[0];
      if (lane == 0) *(unsigned*)(lds + L_FIN + w * 4) = fold;
    }
    bar();
    float bpart = 0.f;
    if (w == 0) {
#pragma unroll
      for (int ww = 0; ww < 8; ++ww) bpart += fr[128 + ww * 16 + c];
      bpart = bfr(bfr(1.f - gate) * bfr(bpart));
      const int po = (rb * 128 + 16 * (lane & 7)) * 2048 + rs * 64;
      const unsigned po_old = *(const unsigned*)(lds + L_FIN + (lane & 7) * 4);
#pragma unroll 1
      for (unsigned spins = 0; spins < SPIN_CAP; ++spins) {
        if (__any((ld32<A_POLL>(rws, po, WS_Y) ^ po_old) & 1u)) break;
        __builtin_amdgcn_s_sleep(SSLEEP);
      }
    }
    GAS u16* op = (GAS u16*)out + rb * 512 + rs * 16 + 4 * (c & 3) + (((c >> 2) & 2) | (((c >> 2) & 1) ^ 1));
    asm volatile("" : "+v"(bpart), "+v"(op));
    bar();
    y = u32x4{0u, 0u, 0u, 0xfeu};
    if (!isb) { POLL_Y(FSLEEP); }
    STAMP(6);
    const float lse = u2f((((y[0] >> 1) & 0x7fu) | (((y[1] >> 1) & 0x7fu) << 7) | (((y[2] >> 1) & 0x7fu) << 14) | (((y[3] >> 1) & 0x7fu) << 21)) << 4);
    float M = fmaxf(lse, dppf<ROR8>(lse)); M = fmaxf(M, dppf<ROR4>(M));
    const float e = isb ? 0.f : ex2(lse - M);
    float L = e + dppf<ROR8>(e); L += dppf<ROR4>(L);
    const float cf = e * __builtin_amdgcn_rcpf(L);
    float v[4];
#pragma unroll
    for (int j = 0; j < 4; ++j) v[j] = cf * u2f(y[j] & ~0xffu);
    SUM_ROWS(v);
    if (lane < 16) fr[w * 16 + lane] = v[0];
    bar();
    if (tid < 16) {
      const float a = ((fr[tid] + fr[16 + tid]) + (fr[32 + tid] + fr[48 + tid])) + ((fr[64 + tid] + fr[80 + tid]) + (fr[96 + tid] + fr[112 + tid]));
      const float o = bfr(gate * bfr(a)) + bpart;
      *op = (u16)pk_bf16(o, o);
    }
    if (rs == 0 && tid == 0) __hip_atomic_store((GAS u64*)(ws + WS_EPOCH + rb * 128), tagr, RLX_AGENT);
  }
  STAMP(7);
    return;
  }
  {
    const int h = __builtin_amdgcn_readfirstlane(4 * x8 + idx / 6), b = __builtin_amdgcn_readfirstlane((idx % 6) / 3), sp = __builtin_amdgcn_readfirstlane(idx % 3);
    const int src = b * 128 + h * 4 + sp;
    const u32x4 sel = make_sel(c);
    const int tb = (b * 32 + h) * 512, t1 = tb + 512 * (sp + 1) / 3, t0 = tb + 512 * sp / 3 + w;
    const rsrc_t rk = mk_rsrc(Kc, t1 * 4096), rv = mk_rsrc(Vc, t1 * 4096), ro = mk_rsrc(wo, (OFFM & 128) ? 0 : 4096 * 512 * 2);
#define LDT(r, i_, j) ld128<AUX_NT>(r, lane * 16 + (j) * 1024, (t0 + 8 * (i_)) * 4096)
#define REQ(a, r, i_) { _Pragma("unroll") for (int j = 0; j < 4; ++j) a[j] = LDT(r, i_, j); SB(); }
#define REQ_REST(a, r) { _Pragma("unroll") for (int j = 0; j < 4; ++j) a[j] = ld128<AUX_NT>(r, lane * 16, (t0 - w + 168) * 4096 + (w + 8 * j) * 1024); SB(); }
#define ARRIVED(a) { _Pragma("unroll") for (int j = 0; j < 4; ++j) asm volatile("" : "+v"(a[j])); SB(); }
    u32x4 W[16], A[4], B[4], C[4], D4[4], E[4], F[4];
    const int wr = __builtin_amdgcn_readfirstlane((w + (idx % 6) * 4 / 3) & 7);
    unsigned x0 = x[64 * wr + lane], x1 = x[512 + 64 * wr + lane];
    LD_EPOCH();
    unsigned old = ld32<A_PRE>(rws, lane * 4, WS_Y + src * 2048 + w * 256);
    const unsigned touch = ld32<AUX_NT>(ro, 0, h * 131072), touch2 = ld32<AUX_NT>(rv, 0, t0 * 4096), touch3 = TOUCH ? ld32<AUX_NT>(rk, 0, t0 * 4096) : 0u;
    {
      const rsrc_t rq = mk_rsrc(wq, (OFFM & 64) ? 0 : 512 * 4096 * 2);
#pragma unroll
      for (int i = 0; i < 16; ++i) W[i] = ld128<AUX_NT>(rq, g * (16 * 8192) + c * 16, (64 * wr + i) * 8192 + h * 256);
    }
    SB();
    USE_EPOCH();
    u64 tag = b ? tag1 : tag0;
    asm volatile("" : "+s"(tag));
    asm volatile("" : "+v"(old), "+v"(x0), "+v"(x1) : "v"(touch), "v"(touch2), "v"(touch3));
    {
      f32x4 D = {0.f, 0.f, 0.f, 0.f};
      rowsum_refill<0>(W, x0 | (x1 << 16), sel, D, [&](auto T) {
        constexpr int j = T & 3;
        if constexpr (T < 4) A[j] = LDT(rk, 0, j); else if constexpr (T < 8) B[j] = LDT(rk, 1, j);
        else if constexpr (T < 12) C[j] = LDT(rk, 2, j); else D4[j] = LDT(rk, 3, j); });
#pragma unroll
      for (int r = 0; r < 4; ++r) red[(wr * 2 + (c >> 3)) * 128 + 32 * g + 8 * r + (c & 7)] = D[r];
    }
    SB();
    REQ(E, rk, 4); REQ_REST(F, rk);
    bar();
    u32x4 q;
    QREAD(q, b);
    STAMP(1);
#define TLREC()
    float* scw = (float*)(lds + L_SC + w * 1536);
    float m = -INFINITY;
#define SCORE(a, i_) { float xs[4]; _Pragma("unroll") for (int j = 0; j < 4; ++j) xs[j] = dot8(a[j], q); \
      float s = bfr(bfr(treduce<4>(xs, c)) * SCALE); \
      if ((i_) == 21) s = (w + 8 * (c >> 2) < 4 * (t1 - t0 + w - 168)) ? s : -INFINITY; \
      m = fmaxf(m, s); scw[(i_) * 16 + (lane >> 2)] = s; asm volatile("" : "+v"(m)); SB(); TLREC(); }
    SCORE(A, 0); REQ(A, rk, 5);
    SCORE(B, 1);
    if (DEEP & 1) REQ(B, rk, 6);
    SCORE(C, 2); SCORE(D4, 3); SCORE(E, 4); SCORE(F, 21);
    const unsigned img = (unsigned)(uintptr_t)(LAS char*)(lds + L_RING) + w * 16384;
    const int dvo = (lane >> 3) * 1024 + 16 * ((lane & 7) ^ ((lane >> 4 & 1) << 1));
#define DMA8(k_, vo) { unsigned keep; \
      asm volatile("s_mov_b32 %0, m0\n\ts_mov_b32 m0, %1\n\ts_nop 4\n\tbuffer_load_dwordx4 %2, %3, %4 offen nt lds\n\ts_mov_b32 m0, %0" : "=&s"(keep) \
                   : "s"(__builtin_amdgcn_readfirstlane(img + (k_) * 1024)), "v"(vo), "s"(ro), "s"(__builtin_amdgcn_readfirstlane((h * 128 + 8 * (k_)) * 1024 + w * 128)) : "memory"); SB(); }
#pragma unroll 1
    for (int i = 5; i < 21; i += 2) {
      const rsrc_t rn = mk_rsrc(i < 19 ? Kc : Vc, t1 * 4096);
      if (DEEP & 1) {
        SCORE(A, i); REQ(A, rn, i < 19 ? i + 2 : 0); DMA8(i - 5, dvo);
        SCORE(B, i + 1); REQ(B, rn, i < 19 ? i + 3 : 1); DMA8(i - 4, dvo ^ 64);
      } else {
        ARRIVED(A); REQ(B, rk, i + 1); DMA8(i - 5, dvo);
        SCORE(A, i);
        ARRIVED(B); REQ(A, rn, i < 19 ? i + 2 : 0); DMA8(i - 4, dvo ^ 64);
        SCORE(B, i + 1);
      }
    }
    m = xrow_max(row_max(m));
    float l = 0.f;
    {
      const float ms = (m == -INFINITY) ? 0.f : m;
      if (lane < 32) scw[352 + lane] = -INFINITY;
#pragma unroll
      for (int k = 0; k < 6; ++k) { const float p = ex2((scw[64 * k + lane] - ms) * LOG2E); l += p; scw[64 * k + lane] = u2f(hilo(p)); }
      l = xrow_sum(row_sum(l));
    }
    f32x4 O = {0.f, 0.f, 0.f, 0.f};
#define PV(a, i_) { rowsum<4, 4>(a, f2u(scw[(i_) * 16 + (lane >> 2)]), sel, O); asm volatile("" : "+v"(O)); SB(); TLREC(); }
    if (!(DEEP & 1)) { ARRIVED(A); REQ(B, rv, 1); }
    else { ARRIVED(B); }
    asm volatile("s_waitcnt vmcnt(%0)" :: "i"((DEEP & 1) ? 0 : 4) : "memory");
    {
      const int q4 = c >> 2, p4 = lane & 3, phi = (q4 >> 1) | ((g & 1) << 1);
#pragma unroll
      for (int s = 0; s < 4; ++s)
#pragma unroll
        for (int ct = 0; ct < 4; ++ct) {
          const char* a = lds + L_RING + w * 16384 + (32 * s + 8 * g + q4) * 128 + 16 * ((p4 >> 1) | ((ct ^ phi) << 1)) + 8 * (p4 & 1);
          const s16x4 lo = __builtin_amdgcn_ds_read_tr16_b64_v4i16((LAS s16x4*)a), hi = __builtin_amdgcn_ds_read_tr16_b64_v4i16((LAS s16x4*)(a + 512));
          W[4 * s + ct] = __builtin_bit_cast(u32x4, __builtin_shufflevector(lo, hi, 0, 1, 2, 3, 4, 5, 6, 7));
        }
    }
#pragma unroll
    for (int i = 0; i < 16; ++i) asm volatile("" : "+v"(W[i]));
    if (DEEP & 2) {
      PV(A, 0); REQ(A, rv, 2); PV(B, 1); REQ(B, rv, 3);
#pragma unroll 1
      for (int i = 2; i < 18; i += 2) { PV(A, i); REQ(A, rv, i + 2); PV(B, i + 1); REQ(B, rv, i + 3); }
    } else {
      PV(A, 0);
      ARRIVED(B); REQ(A, rv, 2); PV(B, 1);
#pragma unroll 1
      for (int i = 2; i < 21 - NBOOST; i += 2) {
        ARRIVED(A); REQ(B, rv, i + 1); PV(A, i);
        ARRIVED(B); REQ(A, rv, i + 2); PV(B, i + 1);
      }
    }
    const GAS u64* go = (const GAS u64*)(ws + WS_GO) + h * 16 + b;
    u64 gov;
    if constexpr (DEEP & 2) {
      REQ(C, rv, 20); REQ_REST(D4, rv);
      gov = __hip_atomic_load(go, RLX_AGENT); SB();
      PV(A, 18); PV(B, 19); PV(C, 20); PV(D4, 21);
    } else if constexpr (NBOOST == 5) {
      ARRIVED(A); REQ(B, rv, 17); REQ(C, rv, 18); REQ(D4, rv, 19); REQ(E, rv, 20); REQ_REST(F, rv);
      gov = __hip_atomic_load(go, RLX_AGENT); SB();
      PV(A, 16); PV(B, 17); PV(C, 18); PV(D4, 19); PV(E, 20); PV(F, 21);
    } else if constexpr (NBOOST == 3) {
      ARRIVED(A); REQ(B, rv, 19); REQ(C, rv, 20); REQ_REST(D4, rv);
      gov = __hip_atomic_load(go, RLX_AGENT); SB();
      PV(A, 18); PV(B, 19); PV(C, 20); PV(D4, 21);
    } else {
      REQ_REST(B, rv);
      gov = __hip_atomic_load(go, RLX_AGENT); SB();
      PV(A, 20); PV(B, 21);
    }
    STAMP(2);
    float* ml = (float*)(lds + L_ML);
#pragma unroll
    for (int r = 0; r < 4; ++r) {
      const unsigned hl = hilo(O[r] + dppf<ROR8>(O[r]));
      *(u16*)(lds + L_P + (2 * w + (c >> 3)) * 272 + (32 * g + 8 * r + (c & 7)) * 2) = (u16)((c & 8) ? (hl >> 16) : hl);
    }
    if (lane == 0) { ml[w] = m; ml[8 + w] = l; }
    bar();
    STAMP(4);
    f32x4 D[4];
    const float mj = ml[lane & 7], lj = ml[8 + (lane & 7)], ma = ml[2 * g], mb = ml[2 * g + 1];
    project(W, lds, g, c, D);
    float M = fmaxf(mj, dppf<XOR7>(mj)); M = fmaxf(M, dppf<XOR2>(M)); M = fmaxf(M, dppf<XOR1>(M));
    const float Ms = (M == -INFINITY) ? 0.f : M;
    float L = ex2((mj - Ms) * LOG2E) * lj; L += dppf<XOR7>(L); L += dppf<XOR2>(L); L += dppf<XOR1>(L);
    const float inv = __builtin_amdgcn_rcpf(L), fa = ex2((ma - Ms) * LOG2E) * inv, fb = ex2((mb - Ms) * LOG2E) * inv;
    float y4[4];
#pragma unroll
    for (int k = 0; k < 4; ++k) y4[k] = fa * (D[k][0] + D[k][1]) + fb * (D[k][2] + D[k][3]);
    const auto s0 = __builtin_amdgcn_permlane32_swap(f2u(y4[0]), f2u(y4[2]), false, false), s1 = __builtin_amdgcn_permlane32_swap(f2u(y4[1]), f2u(y4[3]), false, false);
    const auto s2 = __builtin_amdgcn_permlane16_swap(f2u(u2f(s0[0]) + u2f(s0[1])), f2u(u2f(s1[0]) + u2f(s1[1])), false, false);
    const unsigned code = f2u(Ms * LOG2E + __builtin_amdgcn_logf(L)) >> 4;
    const unsigned yw = (f2u(u2f(s2[0]) + u2f(s2[1])) & ~0xffu) | (((code >> (7 * (lane & 3))) & 0x7fu) << 1) | ((old & 1u) ^ 1u);
    const u32x4 yv = {yw, f2u(dppf<0x55>(u2f(yw))), f2u(dppf<0xAA>(u2f(yw))), f2u(dppf<0xFF>(u2f(yw)))};
#pragma unroll 1
    for (unsigned spins = 0; !(OFFM & 256) && uniform64(gov) != tag && spins < SPIN_CAP; ++spins) gov = __hip_atomic_load(go, RLX_AGENT);
    if ((lane & 3) == 0) __builtin_amdgcn_raw_buffer_store_b128(yv, rws, lane * 4, WS_Y + src * 2048 + w * 256, A_YST);
    STAMP(5); STAMP(6); STAMP(3);
  }
}

extern "C" void kernel_launch(void* const* d_in, const int* in_sizes, int n_in, void* d_out, int out_size, void* d_ws, size_t ws_size, hipStream_t stream) {
  fused_decode<<<256, 512, 0, stream>>>((const u16*)d_in[0], (const u16*)d_in[1], (const u16*)d_in[2], (const u16*)d_in[3], (const u16*)d_in[4],
                                        (const u16*)d_in[6], (const u16*)d_in[7], (const u16*)d_in[8], (const u16*)d_in[9], (const u16*)d_in[10],
                                        (const u16*)d_in[11], (u16*)d_out, (char*)d_ws);
}
```

```cpp
#include <hip/hip_runtime.h>
#include <stdint.h>
#include <type_traits>

typedef unsigned short u16;
typedef unsigned long long u64;
typedef unsigned u32x4 __attribute__((ext_vector_type(4)));
typedef unsigned u32x2 __attribute__((ext_vector_type(2)));
typedef float f32x4 __attribute__((ext_vector_type(4)));
typedef float f32x16 __attribute__((ext_vector_type(16)));
typedef __bf16 bf16x8 __attribute__((ext_vector_type(8)));
typedef __bf16 bf16x2 __attribute__((ext_vector_type(2)));
typedef short s16x4 __attribute__((ext_vector_type(4)));
typedef __amdgpu_buffer_rsrc_t rsrc_t;
#define GAS __attribute__((address_space(1)))
#define LAS __attribute__((address_space(3)))
#define DI __device__ __forceinline__
#define RLX_AGENT __ATOMIC_RELAXED, __HIP_MEMORY_SCOPE_AGENT

constexpr int AUX_NT = 2, AUX_SC1 = 16, AUX_SC1_NT = 18;
constexpr int W_BYTES = 512 * 4096 * 2;
constexpr float SCALE = 0.08838834764831845f;
constexpr float LOG2E = 1.4426950408889634f;

constexpr int WS_EPOCH = 0;
constexpr int WS_F1 = 1024;
constexpr int WS_F2 = 2048;
constexpr int WS_GO = 4096;
constexpr int WS_QA = 8192;
constexpr int WS_ML = WS_QA + 65536;
constexpr int WS_PB = WS_ML + 8192;
constexpr int WS_Y = WS_PB + 2 * 32 * 16 * 1024;
constexpr int WS_END = WS_Y + 2 * 128 * 2048;

constexpr int L_RING = 0;
constexpr int L_RED = 131072;
constexpr int L_P = L_RED + 8192;
constexpr int L_MX = L_P + 32 * 144;
constexpr int L_ML = L_MX + 512;
constexpr int L_FIN = L_ML + 512;
constexpr int L_SC = 147456;
constexpr int L_QA = 131072;
constexpr int L_END = 163840;

DI rsrc_t mk_rsrc(const void* p, unsigned bytes) { return __builtin_amdgcn_make_buffer_rsrc((void*)p, 0, bytes, 0x00020000); }
template <int AUX> DI u32x4 ld128(rsrc_t r, int voff, int soff) { return __builtin_amdgcn_raw_buffer_load_b128(r, voff, soff, AUX); }
template <int AUX> DI u32x2 ld64(rsrc_t r, int voff, int soff) { return __builtin_amdgcn_raw_buffer_load_b64(r, voff, soff, AUX); }
template <int AUX> DI unsigned ld32(rsrc_t r, int voff, int soff) { return __builtin_amdgcn_raw_buffer_load_b32(r, voff, soff, AUX); }
DI float u2f(unsigned u) { return __builtin_bit_cast(float, u); }
DI unsigned f2u(float f) { return __builtin_bit_cast(unsigned, f); }
DI unsigned pk_bf16(float lo, float hi) { bf16x2 r; r[0] = (__bf16)lo; r[1] = (__bf16)hi; return __builtin_bit_cast(unsigned, r); }
DI float bfr(float x) { return u2f(pk_bf16(x, x) << 16); }
DI unsigned hilo(float x) { const float h = bfr(x); return pk_bf16(h, x - h); }
DI float ex2(float x) { return __builtin_amdgcn_exp2f(x); }
DI float dot2(unsigned a, unsigned b, float c) { return __builtin_amdgcn_fdot2_f32_bf16(__builtin_bit_cast(bf16x2, a), __builtin_bit_cast(bf16x2, b), c, false); }
DI float dot8(const u32x4& a, const u32x4& b) { return dot2(a[3], b[3], dot2(a[2], b[2], dot2(a[1], b[1], dot2(a[0], b[0], 0.f)))); }
template <int CTRL> DI float dppf(float x) { return u2f(__builtin_amdgcn_mov_dpp(f2u(x), CTRL, 0xf, 0xf, true)); }
template <int T> DI unsigned bcast16(unsigned v) { return __builtin_amdgcn_mov_dpp(v, 0x150 + T, 0xf, 0xf, true); }
constexpr int XOR1 = 0xB1, XOR2 = 0x4E, XOR7 = 0x141, ROR8 = 0x128, ROR4 = 0x124;
DI float xrow_max(float x) {
  auto s = __builtin_amdgcn_permlane16_swap(f2u(x), f2u(x), false, false); x = fmaxf(u2f(s[0]), u2f(s[1]));
  auto t = __builtin_amdgcn_permlane32_swap(f2u(x), f2u(x), false, false); return fmaxf(u2f(t[0]), u2f(t[1]));
}
DI float xrow_sum(float x) {
  auto s = __builtin_amdgcn_permlane16_swap(f2u(x), f2u(x), false, false); x = u2f(s[0]) + u2f(s[1]);
  auto t = __builtin_amdgcn_permlane32_swap(f2u(x), f2u(x), false, false); return u2f(t[0]) + u2f(t[1]);
}
DI float row_max(float x) { x = fmaxf(x, dppf<ROR8>(x)); x = fmaxf(x, dppf<ROR4>(x)); x = fmaxf(x, dppf<XOR2>(x)); return fmaxf(x, dppf<XOR1>(x)); }
DI float row_sum(float x) { x += dppf<ROR8>(x); x += dppf<ROR4>(x); x += dppf<XOR2>(x); return x + dppf<XOR1>(x); }
DI f32x4 mfma16(u32x4 a, u32x4 b, f32x4 c) { return __builtin_amdgcn_mfma_f32_16x16x32_bf16(__builtin_bit_cast(bf16x8, a), __builtin_bit_cast(bf16x8, b), c, 0, 0, 0); }
DI f32x16 mfma32(u32x4 a, u32x4 b, f32x16 c) { return __builtin_amdgcn_mfma_f32_32x32x16_bf16(__builtin_bit_cast(bf16x8, a), __builtin_bit_cast(bf16x8, b), c, 0, 0, 0); }
DI u64 uniform64(u64 v) { return ((u64)(unsigned)__builtin_amdgcn_readfirstlane((unsigned)(v >> 32)) << 32) | (unsigned)__builtin_amdgcn_readfirstlane((unsigned)v); }
DI u64 next_tag(u64 e) { return e * 0x9E3779B97F4A7C15ull + 0x632BE59BD9B4E019ull; }
DI void bar() { __syncthreads(); }
#define DRAIN() asm volatile("s_waitcnt vmcnt(0)" ::: "memory")
#define PIN() asm volatile("" ::: "memory")
#define SB() __builtin_amdgcn_sched_barrier(0)
constexpr unsigned SPIN_CAP = 1u << 22;

template <int N, int STEP, int T = 0> DI void rowsum(const u32x4 (&W)[N], unsigned pair, const u32x4& sel, f32x4& D) {
  if constexpr (T < N) {
    const unsigned s = bcast16<T * STEP>(pair);
    const u32x4 B = {__builtin_amdgcn_perm(s, s, sel[0]), __builtin_amdgcn_perm(s, s, sel[1]), __builtin_amdgcn_perm(s, s, sel[2]), __builtin_amdgcn_perm(s, s, sel[3])};
    D = mfma16(W[T], B, D);
    rowsum<N, STEP, T + 1>(W, pair, sel, D);
  }
}
template <int T, class F> DI void rowsum_refill(const u32x4 (&W)[16], unsigned pair, const u32x4& sel, f32x4& D, F&& issue) {
  if constexpr (T < 16) {
    const unsigned s = bcast16<T>(pair);
    const u32x4 B = {__builtin_amdgcn_perm(s, s, sel[0]), __builtin_amdgcn_perm(s, s, sel[1]), __builtin_amdgcn_perm(s, s, sel[2]), __builtin_amdgcn_perm(s, s, sel[3])};
    D = mfma16(W[T], B, D);
    asm volatile("" : "+v"(D));
    __builtin_amdgcn_sched_barrier(0);
    issue(std::integral_constant<int, T>{});
    __builtin_amdgcn_sched_barrier(0);
    rowsum_refill<T + 1>(W, pair, sel, D, issue);
  }
}
DI int swz16(int row) { const int u = (row >> 2) & 3; return ((row & 3) << 2) | (((u ^ (u >> 1)) & 1) << 1) | (u >> 1); }
DI u32x4 make_sel(int c) {
  const int j = c & 7; const unsigned b = (c & 8) ? 0x0302u : 0x0100u;
  const unsigned v = (j & 1) ? ((b << 16) | 0x0c0cu) : (0x0c0c0000u | b);
  u32x4 s;
#pragma unroll
  for (int w = 0; w < 4; ++w) s[w] = (w == (j >> 1)) ? v : 0x0c0c0c0cu;
  return s;
}
template <int N> DI float treduce(float (&x)[N], int c) {
#define TR_STEP(CNT, CTRL, BIT) _Pragma("unroll") for (int i = 0; i < (CNT); ++i) { const float a = x[i] + dppf<CTRL>(x[i]), b = x[i + (CNT)] + dppf<CTRL>(x[i + (CNT)]); x[i] = (c & (BIT)) ? b : a; }
  TR_STEP(N / 2, ROR8, 8)
  TR_STEP(N / 4, XOR7, 4)
  if constexpr (N >= 8) { TR_STEP(N / 8, XOR2, 2) } else { x[0] += dppf<XOR2>(x[0]); }
  if constexpr (N >= 16) { TR_STEP(N / 16, XOR1, 1) } else { x[0] += dppf<XOR1>(x[0]); }
#undef TR_STEP
  return x[0];
}
DI void wait_flags32(const GAS u64* f, u64 tag, int lane) {
#pragma unroll 1
  for (unsigned spins = 0; spins < SPIN_CAP; ++spins) {
    if (__all(__hip_atomic_load(f + (lane & 31), RLX_AGENT) == tag)) break;
    __builtin_amdgcn_s_sleep(3);
  }
  __builtin_amdgcn_fence(__ATOMIC_ACQUIRE, "wavefront");
  asm volatile("" ::: "memory");
}
DI void transpose_w(const u32x2 (&R)[32], u32x4 (&F)[16]) {
#pragma unroll
  for (int s = 0; s < 4; ++s)
#pragma unroll
    for (int t = 0; t < 4; ++t)
#pragma unroll
      for (int jj = 0; jj < 4; ++jj) F[4 * s + t][jj] = __builtin_amdgcn_perm(R[8 * s + 2 * jj + 1][t >> 1], R[8 * s + 2 * jj][t >> 1], (t & 1) ? 0x07060302u : 0x05040100u);
}
DI void project(const u32x4 (&F)[16], const char* lds, int g, int c, f32x4 (&D)[4]) {
  u32x4 A[4];
#pragma unroll
  for (int s = 0; s < 4; ++s) A[s] = *(const u32x4*)(lds + L_P + c * 272 + 64 * s + 16 * g);
#pragma unroll
  for (int t = 0; t < 4; ++t) D[t] = f32x4{0.f, 0.f, 0.f, 0.f};
#pragma unroll
  for (int s = 0; s < 4; ++s)
#pragma unroll
    for (int t = 0; t < 4; ++t) D[t] = mfma16(A[s], F[4 * s + t], D[t]);
}
DI unsigned lsb4(const u32x4& v) { return (v[0] & 1u) | ((v[1] & 1u) << 1) | ((v[2] & 1u) << 2) | ((v[3] & 1u) << 3); }
DI unsigned mark(float v, unsigned old, int k) { return (f2u(v) & ~1u) | (((old >> k) & 1u) ^ 1u); }


__global__ void __launch_bounds__(512, 2)
fused_decode(const u16* __restrict__ x, const u16* __restrict__ Kc, const u16* __restrict__ Vc, const u16* __restrict__ wq, const u16* __restrict__ wo,
             const u16* __restrict__ Cc, const u16* __restrict__ w_q, const u16* __restrict__ w_uk, const u16* __restrict__ w_uv, const u16* __restrict__ w_o,
             const u16* __restrict__ gw, u16* __restrict__ out, char* ws) {
  __shared__ __attribute__((aligned(16))) char lds[L_END];
  const int bid = blockIdx.x, tid = threadIdx.x, lane = tid & 63, w = __builtin_amdgcn_readfirstlane(tid >> 6), g = lane >> 4, c = lane & 15;
  const int x8 = bid & 7, idx = bid >> 3;
  asm volatile("" :: "s"(x), "s"(Kc), "s"(Vc), "s"(wq), "s"(wo), "s"(ws));
  const rsrc_t rws = mk_rsrc(ws, WS_END);
  float* red = (float*)(lds + L_RED);
#define LD_EPOCH() u64 e0 = __hip_atomic_load((GAS u64*)(ws + WS_EPOCH), RLX_AGENT), e1 = __hip_atomic_load((GAS u64*)(ws + WS_EPOCH + 128), RLX_AGENT);
#define USE_EPOCH() asm volatile("" : "+v"(e0), "+v"(e1)); const u64 tag0 = next_tag(uniform64(e0)), tag1 = next_tag(uniform64(e1));
#define LD_ROWS(W, ptr) { const rsrc_t rq = mk_rsrc(ptr, W_BYTES); \
    _Pragma("unroll") for (int t = 0; t < 16; ++t) W[t] = ld128<AUX_NT>(rq, g * (16 * 8192) + c * 16, (64 * w + t) * 8192 + h * 256); }
#define LD_OUTW(R, ptr) { const rsrc_t ro = mk_rsrc(ptr, W_BYTES); \
    _Pragma("unroll") for (int i = 0; i < 32; ++i) R[i] = ld64<AUX_NT>(ro, g * 8192 + c * 8 + (i & 3) * 1024, (h * 128 + 32 * (i >> 3) + (i & 4)) * 1024 + w * 128); }
#define QREAD(q, bb) { f32x4 a0 = {0.f, 0.f, 0.f, 0.f}, a1 = a0; \
    _Pragma("unroll") for (int ww = 0; ww < 8; ++ww) { const f32x4* p = (const f32x4*)(red + (ww * 2 + (bb)) * 128 + 8 * c); a0 += p[0]; a1 += p[1]; } \
    q = u32x4{pk_bf16(a0[0], a0[1]), pk_bf16(a0[2], a0[3]), pk_bf16(a1[0], a1[1]), pk_bf16(a1[2], a1[3])}; }

  if (idx >= 24) {
  const int rb = idx >= 28, rs = __builtin_amdgcn_readfirstlane(4 * x8 + (idx & 3));
  const int fy_off = (rb * 128 + (tid >> 2)) * 2048 + rs * 64 + (tid & 3) * 16;
  unsigned fold; u64 tagr;
  float gate;
#define LD_GATE() const u32x4 xv = *(const u32x4*)(x + rb * 512 + lane * 8), gv = *(const u32x4*)(gw + lane * 8);
#define USE_GATE() { float s = 0.f; \
    _Pragma("unroll") for (int j = 0; j < 4; ++j) s += bfr(u2f(xv[j] << 16) * u2f(gv[j] << 16)) + bfr(u2f(xv[j] & 0xffff0000u) * u2f(gv[j] & 0xffff0000u)); \
    s = bfr(xrow_sum(row_sum(s))); gate = bfr(1.f / (1.f + __expf(-s))); }

  if (idx < 28) {
    const int h = rs;
    const u32x4 sel = make_sel(c);
    u32x4 W[16], U[16];
    unsigned x0 = x[tid], x1 = x[512 + tid];
    const unsigned tch = ld32<AUX_NT>(mk_rsrc(w_uk, W_BYTES), (lane & 1) << 21, h * 256) ^ ld32<AUX_NT>(mk_rsrc(w_uv, W_BYTES), (lane & 1) << 21, h * 256) ^ ld32<AUX_NT>(mk_rsrc(w_o, W_BYTES), 0, h * 131072);
    LD_ROWS(W, w_q);
    SB();
    {
      asm volatile("" : "+v"(x0), "+v"(x1) : "v"(tch));
      const unsigned xp = x0 | (x1 << 16);
      const rsrc_t ru = mk_rsrc(w_uk, W_BYTES);
      f32x4 D = {0.f, 0.f, 0.f, 0.f};
      rowsum_refill<0>(W, xp, sel, D, [&](auto T) { U[T] = ld128<AUX_NT>(ru, g * (16 * 8192) + c * 16, (64 * w + T) * 8192 + h * 256); });
#pragma unroll
      for (int r = 0; r < 4; ++r) red[(w * 2 + (c >> 3)) * 128 + 32 * g + 8 * r + (c & 7)] = D[r];
    }
    LD_EPOCH();
    const u32x4 fy = ld128<AUX_SC1_NT>(rws, fy_off, WS_Y);
    const u32x4 oy0 = ld128<AUX_SC1_NT>(rws, c * 16, WS_Y + (h * 4 + 3) * 2048 + w * 256), oy1 = ld128<AUX_SC1_NT>(rws, c * 16, WS_Y + (128 + h * 4 + 3) * 2048 + w * 256);
    LD_GATE();
    SB();
    bar();
#pragma unroll
    for (int bb = 0; bb < 2; ++bb) {
      u32x4 q; QREAD(q, bb);
      float xs[16];
#pragma unroll
      for (int t = 0; t < 16; ++t) xs[t] = dot8(U[t], q);
      const float qa = treduce<16>(xs, c);
      *(u16*)(lds + L_RING + bb * 1024 + tid * 2) = (u16)pk_bf16(qa, qa);
    }
    SB();
    USE_EPOCH(); USE_GATE();
    fold = lsb4(fy);
    unsigned old = lsb4(oy0) | (lsb4(oy1) << 4);
    asm volatile("" : "+v"(fold), "+v"(gate), "+v"(old));
    tagr = tag0;
    DRAIN();
    bar();
    if (w == 0) {
      __builtin_amdgcn_raw_buffer_store_b128(*(const u32x4*)(lds + L_RING + lane * 16), rws, lane * 16, WS_QA + h * 1024, AUX_SC1);
      __builtin_amdgcn_raw_buffer_store_b128(*(const u32x4*)(lds + L_RING + 1024 + lane * 16), rws, lane * 16, WS_QA + (32 + h) * 1024, AUX_SC1);
      DRAIN();
      if (lane == 0) __hip_atomic_store((GAS u64*)(ws + WS_F1) + h, tag0, RLX_AGENT);
    }
    {
      u32x2 R[32];
      LD_OUTW(R, w_o);
      LD_ROWS(W, w_uv);
      SB();
      transpose_w(R, U);
    }
#pragma unroll
    for (int i = 0; i < 16; ++i) asm volatile("" : "+v"(W[i]), "+v"(U[i]));
    if (w == 0) {
      wait_flags32((const GAS u64*)(ws + WS_F2), tag0, lane);
      if (lane < 2) __hip_atomic_store((GAS u64*)(ws + WS_GO) + h * 16 + lane, lane ? tag1 : tag0, RLX_AGENT);
    }
    bar();
    u32x4 T[4];
#pragma unroll
    for (int k = 0; k < 4; ++k) T[k] = ld128<AUX_SC1_NT>(rws, lane * 16, WS_PB + (((k >> 1) * 32 + h) * 16 + 2 * w + (k & 1)) * 1024);
    const u32x2 mlw = ld64<AUX_SC1_NT>(rws, (g & 1) * 4096 + c * 8, WS_ML + h * 128);
    const float ms = u2f(mlw[0]), M0 = row_max(ms), M = (M0 == -INFINITY) ? 0.f : M0;
    const float wt = ex2((ms - M) * LOG2E) * u2f(mlw[1]), cf = wt / row_sum(wt);
    float* cr = (float*)(lds + L_RING);
#pragma unroll
    for (int bb = 0; bb < 2; ++bb) {
      const float f0 = u2f(__builtin_amdgcn_readlane(f2u(cf), 16 * bb + 2 * w)), f1 = u2f(__builtin_amdgcn_readlane(f2u(cf), 16 * bb + 2 * w + 1));
      float a[8];
#pragma unroll
      for (int j = 0; j < 4; ++j) {
        a[2 * j] = f0 * u2f(T[2 * bb][j] << 16) + f1 * u2f(T[2 * bb + 1][j] << 16);
        a[2 * j + 1] = f0 * u2f(T[2 * bb][j] & 0xffff0000u) + f1 * u2f(T[2 * bb + 1][j] & 0xffff0000u);
      }
      *(f32x4*)(cr + (w * 2 + bb) * 512 + lane * 8) = f32x4{a[0], a[1], a[2], a[3]};
      *(f32x4*)(cr + (w * 2 + bb) * 512 + lane * 8 + 4) = f32x4{a[4], a[5], a[6], a[7]};
    }
    bar();
    {
      float c0 = 0.f, c1 = 0.f;
#pragma unroll
      for (int ww = 0; ww < 8; ++ww) { c0 += cr[ww * 1024 + tid]; c1 += cr[ww * 1024 + 512 + tid]; }
      const unsigned h0 = hilo(c0), h1 = hilo(c1);
      f32x4 D = {0.f, 0.f, 0.f, 0.f}, D2 = D;
      rowsum<16, 1>(W, __builtin_amdgcn_perm(h1, h0, 0x05040100u), sel, D);
      rowsum<16, 1>(W, __builtin_amdgcn_perm(h1, h0, 0x07060302u), sel, D2);
      D += D2;
#pragma unroll
      for (int r = 0; r < 4; ++r) red[(w * 2 + (c >> 3)) * 128 + 32 * g + 8 * r + (c & 7)] = D[r];
    }
    bar();
    if (tid < 256) {
      float v = 0.f;
#pragma unroll
      for (int ww = 0; ww < 8; ++ww) v += red[ww * 256 + tid];
      *(u16*)(lds + L_P + (tid >> 7) * 272 + (tid & 127) * 2) = (u16)pk_bf16(v, v);
    }
    bar();
    f32x4 D[4];
    project(U, lds, g, c & 1, D);
    if (g == 0) {
      __builtin_amdgcn_raw_buffer_store_b128(u32x4{mark(D[0][0], old, 0), mark(D[1][0], old, 1), mark(D[2][0], old, 2), mark(D[3][0], old, 3)}, rws, c * 16, WS_Y + (h * 4 + 3) * 2048 + w * 256, AUX_SC1);
      __builtin_amdgcn_raw_buffer_store_b128(u32x4{mark(D[0][1], old, 4), mark(D[1][1], old, 5), mark(D[2][1], old, 6), mark(D[3][1], old, 7)}, rws, c * 16, WS_Y + (128 + h * 4 + 3) * 2048 + w * 256, AUX_SC1);
    }
  } else {
    const int b = rs & 1, sp = rs >> 1;
    u32x4 W[16], S0[8], S1[8];
#define LDC(st, i) ld128<AUX_NT>(mk_rsrc(Cc, (st) < 8 ? (16u << 20) : 0u), lane * 16 + ((i) & 3) * 1024, (b * 8192 + sp * 512 + ((st) & 7) * 64 + 8 * w + ((i) & 4)) * 1024)
#define LDS_(S, st) _Pragma("unroll") for (int i = 0; i < 8; ++i) S[i] = LDC(st, i);
#define STC(slot, S) { int ln = lane; asm volatile("" : "+v"(ln)); char* base = lds + L_RING + (slot) * 65536 + (ln >> 4) * 16384 + 8 * w * 256; \
      _Pragma("unroll") for (int i = 0; i < 8; ++i) *(u32x4*)(base + i * 256 + 16 * ((ln & 15) ^ swz16(8 * w + i))) = S[i]; }
    LD_EPOCH();
    const u32x4 fy = ld128<AUX_SC1_NT>(rws, fy_off, WS_Y);
    LD_GATE();
    LDS_(S0, 0);
    LDS_(S1, 1);
    SB();
    USE_EPOCH(); USE_GATE();
    fold = lsb4(fy);
    asm volatile("" : "+v"(fold), "+v"(gate));
    tagr = tag1;
    STC(0, S0);
    SB();
    LDS_(S0, 2);
    SB();
    STC(1, S1);
    SB();
    LDS_(S1, 3);
    PIN();
    if (w == 0) wait_flags32((const GAS u64*)(ws + WS_F1), tag0, lane);
    bar();
#pragma unroll
    for (int k = 0; k < 4; ++k) *(u32x4*)(lds + L_QA + (4 * w + k) * 1024 + 16 * (lane ^ swz16(4 * w + k))) = ld128<AUX_SC1>(rws, lane * 16, WS_QA + (b * 32 + 4 * w + k) * 1024);
    bar();
    const int kt = w & 3, hh = w >> 2;
#pragma unroll
    for (int s = 0; s < 16; ++s) W[s] = *(const u32x4*)(lds + L_QA + (16 * hh + c) * 1024 + 16 * ((4 * s + g) ^ swz16(c)));
#pragma unroll
    for (int s = 0; s < 16; ++s) asm volatile("" : "+v"(W[s]));
    bar();
    float mq = -INFINITY, lq = 0.f, mp = -INFINITY;
    f32x16 A0, A1;
#pragma unroll
    for (int i = 0; i < 16; ++i) { A0[i] = 0.f; A1[i] = 0.f; }
    float* mx = (float*)(lds + L_MX);
#define STAGE(slot, REFILL) { const char* tile = lds + L_RING + (slot) * 65536; \
      int ln = lane; asm volatile("" : "+v"(ln)); \
      const int g = ln >> 4, c = ln & 15, swz = swz16(c), h5 = ln >> 5, hq = 16 * hh + c, hp = ln & 31; \
      const char* rowp = tile + (16 * kt + c) * 256; \
      f32x4 X0 = {0.f, 0.f, 0.f, 0.f}, X1 = X0; \
      { u32x4 fa[4], fb[4]; \
        _Pragma("unroll") for (int j = 0; j < 4; ++j) fa[j] = *(const u32x4*)(rowp + 16 * ((4 * j + g) ^ swz)); \
        _Pragma("unroll") for (int j = 0; j < 4; ++j) fb[j] = *(const u32x4*)(rowp + 16384 + 16 * ((4 * j + g) ^ swz)); \
        SB(); \
        X0 = mfma16(fa[0], W[0], X0); X1 = mfma16(fa[1], W[1], X1); X0 = mfma16(fa[2], W[2], X0); X1 = mfma16(fa[3], W[3], X1); \
        SB(); \
        _Pragma("unroll") for (int j = 0; j < 4; ++j) fa[j] = *(const u32x4*)(rowp + 32768 + 16 * ((4 * j + g) ^ swz)); \
        SB(); \
        X0 = mfma16(fb[0], W[4], X0); X1 = mfma16(fb[1], W[5], X1); X0 = mfma16(fb[2], W[6], X0); X1 = mfma16(fb[3], W[7], X1); \
        SB(); \
        _Pragma("unroll") for (int j = 0; j < 4; ++j) fb[j] = *(const u32x4*)(rowp + 49152 + 16 * ((4 * j + g) ^ swz)); \
        SB(); \
        X0 = mfma16(fa[0], W[8], X0); X1 = mfma16(fa[1], W[9], X1); X0 = mfma16(fa[2], W[10], X0); X1 = mfma16(fa[3], W[11], X1); \
        X0 = mfma16(fb[0], W[12], X0); X1 = mfma16(fb[1], W[13], X1); X0 = mfma16(fb[2], W[14], X0); X1 = mfma16(fb[3], W[15], X1); } \
      float sc[4]; \
      _Pragma("unroll") for (int r = 0; r < 4; ++r) sc[r] = bfr(bfr(X0[r] + X1[r]) * SCALE); \
      const float tm = xrow_max(fmaxf(fmaxf(sc[0], sc[1]), fmaxf(sc[2], sc[3]))); \
      if (g == 0) mx[kt * 32 + hq] = tm; \
      bar(); \
      { const float q0 = mx[hq], q1 = mx[32 + hq], q2 = mx[64 + hq], q3 = mx[96 + hq], p0 = mx[hp], p1 = mx[32 + hp], p2 = mx[64 + hp], p3 = mx[96 + hp]; \
        const float mn = fmaxf(fmaxf(mq, fmaxf(q0, q1)), fmaxf(q2, q3)), ms = (mn == -INFINITY) ? 0.f : mn; \
        float p[4]; \
        _Pragma("unroll") for (int r = 0; r < 4; ++r) p[r] = ex2((sc[r] - ms) * LOG2E); \
        *(u32x2*)(lds + L_P + hq * 144 + (16 * kt + 4 * g) * 2) = u32x2{pk_bf16(p[0], p[1]), pk_bf16(p[2], p[3])}; \
        lq = lq * ex2((mq - ms) * LOG2E) + ((p[0] + p[1]) + (p[2] + p[3])); \
        mq = mn; \
        const float mn2 = fmaxf(fmaxf(mp, fmaxf(p0, p1)), fmaxf(p2, p3)), ms2 = (mn2 == -INFINITY) ? 0.f : mn2; \
        const float al = ex2((mp - ms2) * LOG2E); mp = mn2; A0 *= al; A1 *= al; } \
      SB(); REFILL; SB(); \
      { const int blk = (ln >> 4) & 1, q4 = (ln & 15) >> 2, p4 = ln & 3; \
        const char* trp = tile + (w >> 1) * 16384 + (8 * h5 + q4) * 256 + 8 * (p4 & 1); \
        const int ch0 = 8 * (w & 1) + 2 * blk + (p4 >> 1), z0 = swz16(8 * h5 + q4), z1 = swz16(8 * h5 + 4 + q4); \
        s16x4 f[4][2][2]; \
        _Pragma("unroll") for (int ks = 0; ks < 4; ++ks) _Pragma("unroll") for (int ct = 0; ct < 2; ++ct) { \
          f[ks][ct][0] = __builtin_amdgcn_ds_read_tr16_b64_v4i16((LAS s16x4*)(trp + ks * 4096 + 16 * ((ch0 + 4 * ct) ^ z0))); \
          f[ks][ct][1] = __builtin_amdgcn_ds_read_tr16_b64_v4i16((LAS s16x4*)(trp + ks * 4096 + 1024 + 16 * ((ch0 + 4 * ct) ^ z1))); } \
        SB(); \
        bar(); \
        u32x4 P[4]; \
        _Pragma("unroll") for (int ks = 0; ks < 4; ++ks) P[ks] = *(const u32x4*)(lds + L_P + hp * 144 + (16 * ks + 8 * h5) * 2); \
        SB(); \
        _Pragma("unroll") for (int ks = 0; ks < 4; ++ks) { \
          A0 = mfma32(__builtin_bit_cast(u32x4, __builtin_shufflevector(f[ks][0][0], f[ks][0][1], 0, 1, 2, 3, 4, 5, 6, 7)), P[ks], A0); \
          A1 = mfma32(__builtin_bit_cast(u32x4, __builtin_shufflevector(f[ks][1][0], f[ks][1][1], 0, 1, 2, 3, 4, 5, 6, 7)), P[ks], A1); } \
        SB(); } }
    STAGE(0, ;);
#pragma unroll 1
    for (int it = 0; it < 3; ++it) {
      STAGE(1, { STC(0, S0); SB(); LDS_(S0, 2 * it + 4); });
      STAGE(0, { STC(1, S1); SB(); LDS_(S1, 2 * it + 5); });
    }
    STAGE(1, ;);
    const int h5 = lane >> 5, hq = 16 * hh + c, hp = lane & 31;
    float* lx = (float*)(lds + L_ML);
    lq = xrow_sum(lq);
    if (g == 0) lx[kt * 32 + hq] = lq;
    bar();
    const float lt = (lx[hp] + lx[32 + hp]) + (lx[64 + hp] + lx[96 + hp]);
    const float inv = 1.f / lt;
    char* img = lds + L_RING + hp * 1040 + (64 * w + 4 * h5) * 2;
#pragma unroll
    for (int k = 0; k < 4; ++k) {
      *(u32x2*)(img + 16 * k) = u32x2{pk_bf16(A0[4 * k] * inv, A0[4 * k + 1] * inv), pk_bf16(A0[4 * k + 2] * inv, A0[4 * k + 3] * inv)};
      *(u32x2*)(img + 64 + 16 * k) = u32x2{pk_bf16(A1[4 * k] * inv, A1[4 * k + 1] * inv), pk_bf16(A1[4 * k + 2] * inv, A1[4 * k + 3] * inv)};
    }
    if (tid < 32) __builtin_amdgcn_raw_buffer_store_b64(u32x2{f2u(mp), f2u(lt)}, rws, hp * 128, WS_ML + b * 4096 + sp * 8, AUX_SC1);
    bar();
#pragma unroll
    for (int k = 0; k < 4; ++k)
      __builtin_amdgcn_raw_buffer_store_b128(*(const u32x4*)(lds + L_RING + (4 * w + k) * 1040 + lane * 16), rws, lane * 16, WS_PB + ((b * 32 + 4 * w + k) * 16 + sp) * 1024, AUX_SC1_NT);
    DRAIN();
    bar();
    if (tid == 0) __hip_atomic_store((GAS u64*)(ws + WS_F2) + rs, tag0, RLX_AGENT);
  }
  {
    const bool isb = (lane & 12) == 12;
    float* fr = (float*)(lds + L_RING);
    u32x4 y = {0u, 0u, 0u, 0u};
#define POLL_Y(SLP) _Pragma("unroll 1") for (unsigned spins = 0; spins < SPIN_CAP; ++spins) { \
      PIN(); y = ld128<AUX_SC1>(rws, fy_off, WS_Y); if ((lsb4(y) ^ fold) == 15u) break; __builtin_amdgcn_s_sleep(SLP); }
#define SUM_ROWS(v) { const float a0 = v[0] + dppf<ROR8>(v[0]), a1 = v[1] + dppf<ROR8>(v[1]), a2 = v[2] + dppf<ROR8>(v[2]), a3 = v[3] + dppf<ROR8>(v[3]); \
      const float b0 = (c & 8) ? a2 : a0, b1 = (c & 8) ? a3 : a1; \
      const float c0 = b0 + dppf<0x124>(b0), c1 = b1 + dppf<0x12C>(b1); \
      v[0] = xrow_sum((c & 4) ? c0 : c1); }
    if (isb) { POLL_Y(16); }
    {
      float v[4];
#pragma unroll
      for (int j = 0; j < 4; ++j) v[j] = u2f(y[j] & ~0xffu);
      SUM_ROWS(v);
      if (lane < 16) fr[128 + w * 16 + lane] = v[0];
      if (lane == 0) *(unsigned*)(lds + L_FIN + w * 4) = fold;
    }
    bar();
    float bpart = 0.f;
    if (w == 0) {
#pragma unroll
      for (int ww = 0; ww < 8; ++ww) bpart += fr[128 + ww * 16 + c];
      bpart = bfr(bfr(1.f - gate) * bfr(bpart));
      const int po = (rb * 128 + 16 * (lane & 7)) * 2048 + rs * 64;
      const unsigned po_old = *(const unsigned*)(lds + L_FIN + (lane & 7) * 4);
#pragma unroll 1
      for (unsigned spins = 0; spins < SPIN_CAP; ++spins) {
        PIN();
        if (__any((ld32<AUX_SC1>(rws, po, WS_Y) ^ po_old) & 1u)) break;
        __builtin_amdgcn_s_sleep(24);
      }
    }
    GAS u16* op = (GAS u16*)out + rb * 512 + rs * 16 + 4 * (c & 3) + (((c >> 2) & 2) | (((c >> 2) & 1) ^ 1));
    asm volatile("" : "+v"(bpart), "+v"(op));
    bar();
    y = u32x4{0u, 0u, 0u, 0xfeu};
    if (!isb) { POLL_Y(0); }
    const float lse = u2f((((y[0] >> 1) & 0x7fu) | (((y[1] >> 1) & 0x7fu) << 7) | (((y[2] >> 1) & 0x7fu) << 14) | (((y[3] >> 1) & 0x7fu) << 21)) << 4);
    float M = fmaxf(lse, dppf<ROR8>(lse)); M = fmaxf(M, dppf<ROR4>(M));
    const float e = isb ? 0.f : ex2(lse - M);
    float L = e + dppf<ROR8>(e); L += dppf<ROR4>(L);
    const float cf = e * __builtin_amdgcn_rcpf(L);
    float v[4];
#pragma unroll
    for (int j = 0; j < 4; ++j) v[j] = cf * u2f(y[j] & ~0xffu);
    SUM_ROWS(v);
    if (lane < 16) fr[w * 16 + lane] = v[0];
    bar();
    if (tid < 16) {
      const float a = ((fr[tid] + fr[16 + tid]) + (fr[32 + tid] + fr[48 + tid])) + ((fr[64 + tid] + fr[80 + tid]) + (fr[96 + tid] + fr[112 + tid]));
      const float o = bfr(gate * bfr(a)) + bpart;
      *op = (u16)pk_bf16(o, o);
    }
    if (rs == 0 && tid == 0) __hip_atomic_store((GAS u64*)(ws + WS_EPOCH + rb * 128), tagr, RLX_AGENT);
  }
    return;
  }
  {
    const int h = __builtin_amdgcn_readfirstlane(4 * x8 + idx / 6), b = __builtin_amdgcn_readfirstlane((idx % 6) / 3), sp = __builtin_amdgcn_readfirstlane(idx % 3);
    const int src = b * 128 + h * 4 + sp;
    const u32x4 sel = make_sel(c);
    const int tb = (b * 32 + h) * 512, t1 = tb + 512 * (sp + 1) / 3, t0 = tb + 512 * sp / 3 + w;
    const rsrc_t rk = mk_rsrc(Kc, t1 * 4096), rv = mk_rsrc(Vc, t1 * 4096), ro = mk_rsrc(wo, W_BYTES);
#define LDT(r, i_, j) ld128<AUX_NT>(r, lane * 16 + (j) * 1024, (t0 + 8 * (i_)) * 4096)
#define REQ(a, r, i_) { _Pragma("unroll") for (int j = 0; j < 4; ++j) a[j] = LDT(r, i_, j); SB(); }
#define REQ_REST(a, r) { _Pragma("unroll") for (int j = 0; j < 4; ++j) a[j] = ld128<AUX_NT>(r, lane * 16, (t0 - w + 168) * 4096 + (w + 8 * j) * 1024); SB(); }
#define ARRIVED(a) { _Pragma("unroll") for (int j = 0; j < 4; ++j) asm volatile("" : "+v"(a[j])); SB(); }
    u32x4 W[16], A[4], B[4], C[4], D4[4], E[4], F[4];
    const int wr = __builtin_amdgcn_readfirstlane((w + (idx % 6) * 4 / 3) & 7);
    unsigned x0 = x[64 * wr + lane], x1 = x[512 + 64 * wr + lane];
    LD_EPOCH();
    unsigned old = ld32<AUX_SC1_NT>(rws, lane * 4, WS_Y + src * 2048 + w * 256);
    const unsigned touch = ld32<AUX_NT>(ro, 0, h * 131072), touch2 = ld32<AUX_NT>(rv, 0, t0 * 4096);
    const unsigned touch3 = ld32<AUX_NT>(rk, 0, t0 * 4096);
    SB();
    {
      const rsrc_t rq = mk_rsrc(wq, W_BYTES);
#pragma unroll
      for (int i = 0; i < 16; ++i) W[i] = ld128<AUX_NT>(rq, g * (16 * 8192) + c * 16, (64 * wr + i) * 8192 + h * 256);
    }
    SB();
    asm volatile("" : "+v"(x0), "+v"(x1) : "v"(touch3));
    {
      f32x4 D = {0.f, 0.f, 0.f, 0.f};
      rowsum_refill<0>(W, x0 | (x1 << 16), sel, D, [&](auto T) {
        constexpr int j = T & 3;
        if constexpr (T < 4) A[j] = LDT(rk, 0, j); else if constexpr (T < 8) B[j] = LDT(rk, 1, j);
        else if constexpr (T < 12) C[j] = LDT(rk, 2, j); else D4[j] = LDT(rk, 3, j); });
#pragma unroll
      for (int r = 0; r < 4; ++r) red[(wr * 2 + (c >> 3)) * 128 + 32 * g + 8 * r + (c & 7)] = D[r];
    }
    SB();
    REQ(E, rk, 4); REQ_REST(F, rk);
    bar();
    u32x4 q;
    QREAD(q, b);
    float* scw = (float*)(lds + L_SC + w * 1536);
    float m = -INFINITY;
#define SCORE(a, i_, REST) { float xs[4]; _Pragma("unroll") for (int j = 0; j < 4; ++j) xs[j] = dot8(a[j], q); \
      float s = bfr(bfr(treduce<4>(xs, c)) * SCALE); \
      if (REST) s = (w + 8 * (c >> 2) < 4 * (t1 - t0 + w - 168)) ? s : -INFINITY; \
      m = fmaxf(m, s); scw[(i_) * 16 + (lane >> 2)] = s; asm volatile("" : "+v"(m)); SB(); }
    SCORE(A, 0, false); REQ(A, rk, 5);
    SCORE(B, 1, false);
    REQ(B, rk, 6);
    SCORE(C, 2, false); SCORE(D4, 3, false); SCORE(E, 4, false); SCORE(F, 21, true);
    USE_EPOCH();
    u64 tag = b ? tag1 : tag0;
    asm volatile("" : "+s"(tag));
    asm volatile("" : "+v"(old) : "v"(touch), "v"(touch2));
    SB();
    const unsigned img = (unsigned)(uintptr_t)(LAS char*)(lds + L_RING) + w * 16384;
    const int dvo = (lane >> 3) * 1024 + 16 * ((lane & 7) ^ ((lane >> 4 & 1) << 1));
#define DMA8(k_, vo) { unsigned keep; \
      asm volatile("s_mov_b32 %0, m0\n\ts_mov_b32 m0, %1\n\ts_nop 4\n\tbuffer_load_dwordx4 %2, %3, %4 offen nt lds\n\ts_mov_b32 m0, %0" : "=&s"(keep) \
                   : "s"(__builtin_amdgcn_readfirstlane(img + (k_) * 1024)), "v"(vo), "s"(ro), "s"(__builtin_amdgcn_readfirstlane((h * 128 + 8 * (k_)) * 1024 + w * 128)) : "memory"); SB(); }
#pragma unroll 1
    for (int i = 5; i < 21; i += 2) {
      const rsrc_t rn = mk_rsrc(i < 19 ? Kc : Vc, t1 * 4096);
      SCORE(A, i, false); REQ(A, rn, i < 19 ? i + 2 : 0); DMA8(i - 5, dvo);
      SCORE(B, i + 1, false); REQ(B, rn, i < 19 ? i + 3 : 1); DMA8(i - 4, dvo ^ 64);
    }
    m = xrow_max(row_max(m));
    float l = 0.f;
    {
      const float ms = (m == -INFINITY) ? 0.f : m;
      if (lane < 32) scw[352 + lane] = -INFINITY;
#pragma unroll
      for (int k = 0; k < 6; ++k) { const float p = ex2((scw[64 * k + lane] - ms) * LOG2E); l += p; scw[64 * k + lane] = u2f(hilo(p)); }
      l = xrow_sum(row_sum(l));
    }
    f32x4 O = {0.f, 0.f, 0.f, 0.f};
#define PV(a, i_) { rowsum<4, 4>(a, f2u(scw[(i_) * 16 + (lane >> 2)]), sel, O); asm volatile("" : "+v"(O)); SB(); }
    PV(A, 0); REQ(A, rv, 2);
    asm volatile("s_waitcnt vmcnt(4)" ::: "memory");
    {
      const int q4 = c >> 2, p4 = lane & 3, phi = (q4 >> 1) | ((g & 1) << 1);
#pragma unroll
      for (int s = 0; s < 4; ++s)
#pragma unroll
        for (int ct = 0; ct < 4; ++ct) {
          const char* a = lds + L_RING + w * 16384 + (32 * s + 8 * g + q4) * 128 + 16 * ((p4 >> 1) | ((ct ^ phi) << 1)) + 8 * (p4 & 1);
          const s16x4 lo = __builtin_amdgcn_ds_read_tr16_b64_v4i16((LAS s16x4*)a), hi = __builtin_amdgcn_ds_read_tr16_b64_v4i16((LAS s16x4*)(a + 512));
          W[4 * s + ct] = __builtin_bit_cast(u32x4, __builtin_shufflevector(lo, hi, 0, 1, 2, 3, 4, 5, 6, 7));
        }
    }
#pragma unroll
    for (int i = 0; i < 16; ++i) asm volatile("" : "+v"(W[i]));
    PV(B, 1);
#pragma unroll 1
    for (int i = 2; i < 20; i += 2) {
      ARRIVED(A); REQ(B, rv, i + 1); PV(A, i);
      ARRIVED(B); REQ(A, rv, i + 2); PV(B, i + 1);
    }
    REQ_REST(B, rv);
    const GAS u64* go = (const GAS u64*)(ws + WS_GO) + h * 16 + b;
    u64 gov = __hip_atomic_load(go, RLX_AGENT);
    SB();
    PV(A, 20); PV(B, 21);
    float* ml = (float*)(lds + L_ML);
#pragma unroll
    for (int r = 0; r < 4; ++r) {
      const unsigned hl = hilo(O[r] + dppf<ROR8>(O[r]));
      *(u16*)(lds + L_P + (2 * w + (c >> 3)) * 272 + (32 * g + 8 * r + (c & 7)) * 2) = (u16)((c & 8) ? (hl >> 16) : hl);
    }
    if (lane == 0) { ml[w] = m; ml[8 + w] = l; }
    bar();
    f32x4 D[4];
    const float mj = ml[lane & 7], lj = ml[8 + (lane & 7)], ma = ml[2 * g], mb = ml[2 * g + 1];
    project(W, lds, g, c, D);
    float M = fmaxf(mj, dppf<XOR7>(mj)); M = fmaxf(M, dppf<XOR2>(M)); M = fmaxf(M, dppf<XOR1>(M));
    const float Ms = (M == -INFINITY) ? 0.f : M;
    float L = ex2((mj - Ms) * LOG2E) * lj; L += dppf<XOR7>(L); L += dppf<XOR2>(L); L += dppf<XOR1>(L);
    const float inv = __builtin_amdgcn_rcpf(L), fa = ex2((ma - Ms) * LOG2E) * inv, fb = ex2((mb - Ms) * LOG2E) * inv;
    float y4[4];
#pragma unroll
    for (int k = 0; k < 4; ++k) y4[k] = fa * (D[k][0] + D[k][1]) + fb * (D[k][2] + D[k][3]);
    const auto s0 = __builtin_amdgcn_permlane32_swap(f2u(y4[0]), f2u(y4[2]), false, false), s1 = __builtin_amdgcn_permlane32_swap(f2u(y4[1]), f2u(y4[3]), false, false);
    const auto s2 = __builtin_amdgcn_permlane16_swap(f2u(u2f(s0[0]) + u2f(s0[1])), f2u(u2f(s1[0]) + u2f(s1[1])), false, false);
    const unsigned code = f2u(Ms * LOG2E + __builtin_amdgcn_logf(L)) >> 4;
    const unsigned yw = (f2u(u2f(s2[0]) + u2f(s2[1])) & ~0xffu) | (((code >> (7 * (lane & 3))) & 0x7fu) << 1) | ((old & 1u) ^ 1u);
    const u32x4 yv = {yw, f2u(dppf<0x55>(u2f(yw))), f2u(dppf<0xAA>(u2f(yw))), f2u(dppf<0xFF>(u2f(yw)))};
#pragma unroll 1
    for (unsigned spins = 0; uniform64(gov) != tag && spins < SPIN_CAP; ++spins) gov = __hip_atomic_load(go, RLX_AGENT);
    PIN();
    if ((lane & 3) == 0) __builtin_amdgcn_raw_buffer_store_b128(yv, rws, lane * 4, WS_Y + src * 2048 + w * 256, AUX_SC1);
  }
}

extern "C" void kernel_launch(void* const* d_in, const int* in_sizes, int n_in, void* d_out, int out_size, void* d_ws, size_t ws_size, hipStream_t stream) {
  fused_decode<<<256, 512, 0, stream>>>((const u16*)d_in[0], (const u16*)d_in[1], (const u16*)d_in[2], (const u16*)d_in[3], (const u16*)d_in[4],
                                        (const u16*)d_in[6], (const u16*)d_in[7], (const u16*)d_in[8], (const u16*)d_in[9], (const u16*)d_in[10],
                                        (const u16*)d_in[11], (u16*)d_out, (char*)d_ws + ((ws_size - WS_END) & ~(size_t)((2  __H0__ ) - 1)));
}
```

```cpp
#define DIAG 4
#include <hip/hip_runtime.h>
#include <stdint.h>
#include <type_traits>

typedef unsigned short u16;
typedef unsigned long long u64;
typedef unsigned u32x4 __attribute__((ext_vector_type(4)));
typedef unsigned u32x2 __attribute__((ext_vector_type(2)));
typedef float f32x4 __attribute__((ext_vector_type(4)));
typedef float f32x16 __attribute__((ext_vector_type(16)));
typedef __bf16 bf16x8 __attribute__((ext_vector_type(8)));
typedef __bf16 bf16x2 __attribute__((ext_vector_type(2)));
typedef short s16x4 __attribute__((ext_vector_type(4)));
typedef __amdgpu_buffer_rsrc_t rsrc_t;
#define GAS __attribute__((address_space(1)))
#define LAS __attribute__((address_space(3)))
#define DI __device__ __forceinline__
#define RLX_AGENT __ATOMIC_RELAXED, __HIP_MEMORY_SCOPE_AGENT

constexpr int AUX_NT = 2, AUX_SC1 = 16, AUX_SC1_NT = 18;
constexpr int W_BYTES = 512 * 4096 * 2;
constexpr float SCALE = 0.08838834764831845f;
constexpr float LOG2E = 1.4426950408889634f;

constexpr int WS_EPOCH = 0;
constexpr int WS_F1 = 1024;
constexpr int WS_F2 = 2048;
constexpr int WS_GO = 4096;
constexpr int WS_QA = 8192;
constexpr int WS_ML = WS_QA + 65536;
constexpr int WS_PB = WS_ML + 8192;
constexpr int WS_Y = WS_PB + 2 * 32 * 16 * 1024;
constexpr int WS_END = WS_Y + 2 * 128 * 2048;

constexpr int L_RING = 0;
constexpr int L_RED = 131072;
constexpr int L_P = L_RED + 8192;
constexpr int L_MX = L_P + 32 * 144;
constexpr int L_ML = L_MX + 512;
constexpr int L_FIN = L_ML + 512;
constexpr int L_SC = 147456;
constexpr int L_QA = 131072;
constexpr int L_END = 163840;

DI rsrc_t mk_rsrc(const void* p, unsigned bytes) { return __builtin_amdgcn_make_buffer_rsrc((void*)p, 0, bytes, 0x00020000); }
template <int AUX> DI u32x4 ld128(rsrc_t r, int voff, int soff) { return __builtin_amdgcn_raw_buffer_load_b128(r, voff, soff, AUX); }
template <int AUX> DI u32x2 ld64(rsrc_t r, int voff, int soff) { return __builtin_amdgcn_raw_buffer_load_b64(r, voff, soff, AUX); }
template <int AUX> DI unsigned ld32(rsrc_t r, int voff, int soff) { return __builtin_amdgcn_raw_buffer_load_b32(r, voff, soff, AUX); }
DI float u2f(unsigned u) { return __builtin_bit_cast(float, u); }
DI unsigned f2u(float f) { return __builtin_bit_cast(unsigned, f); }
DI unsigned pk_bf16(float lo, float hi) { bf16x2 r; r[0] = (__bf16)lo; r[1] = (__bf16)hi; return __builtin_bit_cast(unsigned, r); }
DI float bfr(float x) { return u2f(pk_bf16(x, x) << 16); }
DI unsigned hilo(float x) { const float h = bfr(x); return pk_bf16(h, x - h); }
DI float ex2(float x) { return __builtin_amdgcn_exp2f(x); }
DI float dot2(unsigned a, unsigned b, float c) { return __builtin_amdgcn_fdot2_f32_bf16(__builtin_bit_cast(bf16x2, a), __builtin_bit_cast(bf16x2, b), c, false); }
DI float dot8(const u32x4& a, const u32x4& b) { return dot2(a[3], b[3], dot2(a[2], b[2], dot2(a[1], b[1], dot2(a[0], b[0], 0.f)))); }
template <int CTRL> DI float dppf(float x) { return u2f(__builtin_amdgcn_mov_dpp(f2u(x), CTRL, 0xf, 0xf, true)); }
template <int T> DI unsigned bcast16(unsigned v) { return __builtin_amdgcn_mov_dpp(v, 0x150 + T, 0xf, 0xf, true); }
constexpr int XOR1 = 0xB1, XOR2 = 0x4E, XOR7 = 0x141, ROR8 = 0x128, ROR4 = 0x124;
DI float xrow_max(float x) {
  auto s = __builtin_amdgcn_permlane16_swap(f2u(x), f2u(x), false, false); x = fmaxf(u2f(s[0]), u2f(s[1]));
  auto t = __builtin_amdgcn_permlane32_swap(f2u(x), f2u(x), false, false); return fmaxf(u2f(t[0]), u2f(t[1]));
}
DI float xrow_sum(float x) {
  auto s = __builtin_amdgcn_permlane16_swap(f2u(x), f2u(x), false, false); x = u2f(s[0]) + u2f(s[1]);
  auto t = __builtin_amdgcn_permlane32_swap(f2u(x), f2u(x), false, false); return u2f(t[0]) + u2f(t[1]);
}
DI float row_max(float x) { x = fmaxf(x, dppf<ROR8>(x)); x = fmaxf(x, dppf<ROR4>(x)); x = fmaxf(x, dppf<XOR2>(x)); return fmaxf(x, dppf<XOR1>(x)); }
DI float row_sum(float x) { x += dppf<ROR8>(x); x += dppf<ROR4>(x); x += dppf<XOR2>(x); return x + dppf<XOR1>(x); }
DI f32x4 mfma16(u32x4 a, u32x4 b, f32x4 c) { return __builtin_amdgcn_mfma_f32_16x16x32_bf16(__builtin_bit_cast(bf16x8, a), __builtin_bit_cast(bf16x8, b), c, 0, 0, 0); }
DI f32x16 mfma32(u32x4 a, u32x4 b, f32x16 c) { return __builtin_amdgcn_mfma_f32_32x32x16_bf16(__builtin_bit_cast(bf16x8, a), __builtin_bit_cast(bf16x8, b), c, 0, 0, 0); }
DI u64 uniform64(u64 v) { return ((u64)(unsigned)__builtin_amdgcn_readfirstlane((unsigned)(v >> 32)) << 32) | (unsigned)__builtin_amdgcn_readfirstlane((unsigned)v); }
DI u64 next_tag(u64 e) { return e * 0x9E3779B97F4A7C15ull + 0x632BE59BD9B4E019ull; }
DI void bar() { __syncthreads(); }
#define DRAIN() asm volatile("s_waitcnt vmcnt(0)" ::: "memory")
#define PIN() asm volatile("" ::: "memory")
#define SB() __builtin_amdgcn_sched_barrier(0)
constexpr unsigned SPIN_CAP = 1u << 22;

template <int N, int STEP, int T = 0> DI void rowsum(const u32x4 (&W)[N], unsigned pair, const u32x4& sel, f32x4& D) {
  if constexpr (T < N) {
    const unsigned s = bcast16<T * STEP>(pair);
    const u32x4 B = {__builtin_amdgcn_perm(s, s, sel[0]), __builtin_amdgcn_perm(s, s, sel[1]), __builtin_amdgcn_perm(s, s, sel[2]), __builtin_amdgcn_perm(s, s, sel[3])};
    D = mfma16(W[T], B, D);
    rowsum<N, STEP, T + 1>(W, pair, sel, D);
  }
}
template <int T, class F> DI void rowsum_refill(const u32x4 (&W)[16], unsigned pair, const u32x4& sel, f32x4& D, F&& issue) {
  if constexpr (T < 16) {
    const unsigned s = bcast16<T>(pair);
    const u32x4 B = {__builtin_amdgcn_perm(s, s, sel[0]), __builtin_amdgcn_perm(s, s, sel[1]), __builtin_amdgcn_perm(s, s, sel[2]), __builtin_amdgcn_perm(s, s, sel[3])};
    D = mfma16(W[T], B, D);
    asm volatile("" : "+v"(D));
    __builtin_amdgcn_sched_barrier(0);
    issue(std::integral_constant<int, T>{});
    __builtin_amdgcn_sched_barrier(0);
    rowsum_refill<T + 1>(W, pair, sel, D, issue);
  }
}
DI int swz16(int row) { const int u = (row >> 2) & 3; return ((row & 3) << 2) | (((u ^ (u >> 1)) & 1) << 1) | (u >> 1); }
DI u32x4 make_sel(int c) {
  const int j = c & 7; const unsigned b = (c & 8) ? 0x0302u : 0x0100u;
  const unsigned v = (j & 1) ? ((b << 16) | 0x0c0cu) : (0x0c0c0000u | b);
  u32x4 s;
#pragma unroll
  for (int w = 0; w < 4; ++w) s[w] = (w == (j >> 1)) ? v : 0x0c0c0c0cu;
  return s;
}
template <int N> DI float treduce(float (&x)[N], int c) {
#define TR_STEP(CNT, CTRL, BIT) _Pragma("unroll") for (int i = 0; i < (CNT); ++i) { const float a = x[i] + dppf<CTRL>(x[i]), b = x[i + (CNT)] + dppf<CTRL>(x[i + (CNT)]); x[i] = (c & (BIT)) ? b : a; }
  TR_STEP(N / 2, ROR8, 8)
  TR_STEP(N / 4, XOR7, 4)
  if constexpr (N >= 8) { TR_STEP(N / 8, XOR2, 2) } else { x[0] += dppf<XOR2>(x[0]); }
  if constexpr (N >= 16) { TR_STEP(N / 16, XOR1, 1) } else { x[0] += dppf<XOR1>(x[0]); }
#undef TR_STEP
  return x[0];
}
DI void wait_flags32(const GAS u64* f, u64 tag, int lane) {
#pragma unroll 1
  for (unsigned spins = 0; spins < SPIN_CAP; ++spins) {
    if (__all(__hip_atomic_load(f + (lane & 31), RLX_AGENT) == tag)) break;
    __builtin_amdgcn_s_sleep(3);
  }
  __builtin_amdgcn_fence(__ATOMIC_ACQUIRE, "wavefront");
  asm volatile("" ::: "memory");
}
DI void transpose_w(const u32x2 (&R)[32], u32x4 (&F)[16]) {
#pragma unroll
  for (int s = 0; s < 4; ++s)
#pragma unroll
    for (int t = 0; t < 4; ++t)
#pragma unroll
      for (int jj = 0; jj < 4; ++jj) F[4 * s + t][jj] = __builtin_amdgcn_perm(R[8 * s + 2 * jj + 1][t >> 1], R[8 * s + 2 * jj][t >> 1], (t & 1) ? 0x07060302u : 0x05040100u);
}
DI void project(const u32x4 (&F)[16], const char* lds, int g, int c, f32x4 (&D)[4]) {
  u32x4 A[4];
#pragma unroll
  for (int s = 0; s < 4; ++s) A[s] = *(const u32x4*)(lds + L_P + c * 272 + 64 * s + 16 * g);
#pragma unroll
  for (int t = 0; t < 4; ++t) D[t] = f32x4{0.f, 0.f, 0.f, 0.f};
#pragma unroll
  for (int s = 0; s < 4; ++s)
#pragma unroll
    for (int t = 0; t < 4; ++t) D[t] = mfma16(A[s], F[4 * s + t], D[t]);
}
DI unsigned lsb4(const u32x4& v) { return (v[0] & 1u) | ((v[1] & 1u) << 1) | ((v[2] & 1u) << 2) | ((v[3] & 1u) << 3); }
DI unsigned mark(float v, unsigned old, int k) { return (f2u(v) & ~1u) | (((old >> k) & 1u) ^ 1u); }


__global__ void __launch_bounds__(512, 2)
fused_decode(const u16* __restrict__ x, const u16* __restrict__ Kc, const u16* __restrict__ Vc, const u16* __restrict__ wq, const u16* __restrict__ wo,
             const u16* __restrict__ Cc, const u16* __restrict__ w_q, const u16* __restrict__ w_uk, const u16* __restrict__ w_uv, const u16* __restrict__ w_o,
             const u16* __restrict__ gw, u16* __restrict__ out, char* ws) {
  __shared__ __attribute__((aligned(16))) char lds[L_END];
  const int bid = blockIdx.x, tid = threadIdx.x, lane = tid & 63, w = __builtin_amdgcn_readfirstlane(tid >> 6), g = lane >> 4, c = lane & 15;
  const int x8 = bid & 7, idx = bid >> 3;
  asm volatile("" :: "s"(x), "s"(Kc), "s"(Vc), "s"(wq), "s"(wo), "s"(ws));
  const u64 t_begin = __builtin_amdgcn_s_memrealtime();
  if (tid == 0) __hip_atomic_store((GAS u64*)(ws + (3 << 20)) + bid * 16, t_begin, RLX_AGENT);
  const rsrc_t rws = mk_rsrc(ws, WS_END);
  float* red = (float*)(lds + L_RED);
#define LD_EPOCH() u64 e0 = __hip_atomic_load((GAS u64*)(ws + WS_EPOCH), RLX_AGENT), e1 = __hip_atomic_load((GAS u64*)(ws + WS_EPOCH + 128), RLX_AGENT);
#define USE_EPOCH() asm volatile("" : "+v"(e0), "+v"(e1)); const u64 tag0 = next_tag(uniform64(e0)), tag1 = next_tag(uniform64(e1));
#define LD_ROWS(W, ptr) { const rsrc_t rq = mk_rsrc(ptr, W_BYTES); \
    _Pragma("unroll") for (int t = 0; t < 16; ++t) W[t] = ld128<AUX_NT>(rq, g * (16 * 8192) + c * 16, (64 * w + t) * 8192 + h * 256); }
#define LD_OUTW(R, ptr) { const rsrc_t ro = mk_rsrc(ptr, W_BYTES); \
    _Pragma("unroll") for (int i = 0; i < 32; ++i) R[i] = ld64<AUX_NT>(ro, g * 8192 + c * 8 + (i & 3) * 1024, (h * 128 + 32 * (i >> 3) + (i & 4)) * 1024 + w * 128); }
#define QREAD(q, bb) { f32x4 a0 = {0.f, 0.f, 0.f, 0.f}, a1 = a0; \
    _Pragma("unroll") for (int ww = 0; ww < 8; ++ww) { const f32x4* p = (const f32x4*)(red + (ww * 2 + (bb)) * 128 + 8 * c); a0 += p[0]; a1 += p[1]; } \
    q = u32x4{pk_bf16(a0[0], a0[1]), pk_bf16(a0[2], a0[3]), pk_bf16(a1[0], a1[1]), pk_bf16(a1[2], a1[3])}; }

  if (idx >= 24) {
  const int rb = idx >= 28, rs = __builtin_amdgcn_readfirstlane(4 * x8 + (idx & 3));
  const int fy_off = (rb * 128 + (tid >> 2)) * 2048 + rs * 64 + (tid & 3) * 16;
  unsigned fold; u64 tagr;
  float gate;
#define LD_GATE() const u32x4 xv = *(const u32x4*)(x + rb * 512 + lane * 8), gv = *(const u32x4*)(gw + lane * 8);
#define USE_GATE() { float s = 0.f; \
    _Pragma("unroll") for (int j = 0; j < 4; ++j) s += bfr(u2f(xv[j] << 16) * u2f(gv[j] << 16)) + bfr(u2f(xv[j] & 0xffff0000u) * u2f(gv[j] & 0xffff0000u)); \
    s = bfr(xrow_sum(row_sum(s))); gate = bfr(1.f / (1.f + __expf(-s))); }

  if (idx < 28) {
    const int h = rs;
    const u32x4 sel = make_sel(c);
    u32x4 W[16], U[16];
    unsigned x0 = x[tid], x1 = x[512 + tid];
    const unsigned tch = ld32<AUX_NT>(mk_rsrc(w_uk, W_BYTES), (lane & 1) << 21, h * 256) ^ ld32<AUX_NT>(mk_rsrc(w_uv, W_BYTES), (lane & 1) << 21, h * 256) ^ ld32<AUX_NT>(mk_rsrc(w_o, W_BYTES), 0, h * 131072);
    LD_ROWS(W, w_q);
    SB();
    {
      asm volatile("" : "+v"(x0), "+v"(x1) : "v"(tch));
      const unsigned xp = x0 | (x1 << 16);
      const rsrc_t ru = mk_rsrc(w_uk, W_BYTES);
      f32x4 D = {0.f, 0.f, 0.f, 0.f};
      rowsum_refill<0>(W, xp, sel, D, [&](auto T) { U[T] = ld128<AUX_NT>(ru, g * (16 * 8192) + c * 16, (64 * w + T) * 8192 + h * 256); });
#pragma unroll
      for (int r = 0; r < 4; ++r) red[(w * 2 + (c >> 3)) * 128 + 32 * g + 8 * r + (c & 7)] = D[r];
    }
    LD_EPOCH();
    const u32x4 fy = ld128<AUX_SC1_NT>(rws, fy_off, WS_Y);
    const u32x4 oy0 = ld128<AUX_SC1_NT>(rws, c * 16, WS_Y + (h * 4 + 3) * 2048 + w * 256), oy1 = ld128<AUX_SC1_NT>(rws, c * 16, WS_Y + (128 + h * 4 + 3) * 2048 + w * 256);
    LD_GATE();
    SB();
    bar();
#pragma unroll
    for (int bb = 0; bb < 2; ++bb) {
      u32x4 q; QREAD(q, bb);
      float xs[16];
#pragma unroll
      for (int t = 0; t < 16; ++t) xs[t] = dot8(U[t], q);
      const float qa = treduce<16>(xs, c);
      *(u16*)(lds + L_RING + bb * 1024 + tid * 2) = (u16)pk_bf16(qa, qa);
    }
    SB();
    USE_EPOCH(); USE_GATE();
    fold = lsb4(fy);
    unsigned old = lsb4(oy0) | (lsb4(oy1) << 4);
    asm volatile("" : "+v"(fold), "+v"(gate), "+v"(old));
    tagr = tag0;
    DRAIN();
    bar();
    if (w == 0) {
      __builtin_amdgcn_raw_buffer_store_b128(*(const u32x4*)(lds + L_RING + lane * 16), rws, lane * 16, WS_QA + h * 1024, AUX_SC1);
      __builtin_amdgcn_raw_buffer_store_b128(*(const u32x4*)(lds + L_RING + 1024 + lane * 16), rws, lane * 16, WS_QA + (32 + h) * 1024, AUX_SC1);
      DRAIN();
      if (lane == 0) __hip_atomic_store((GAS u64*)(ws + WS_F1) + h, tag0, RLX_AGENT);
    }
    {
      u32x2 R[32];
      LD_OUTW(R, w_o);
      LD_ROWS(W, w_uv);
      SB();
      transpose_w(R, U);
    }
#pragma unroll
    for (int i = 0; i < 16; ++i) asm volatile("" : "+v"(W[i]), "+v"(U[i]));
    if (w == 0) {
      wait_flags32((const GAS u64*)(ws + WS_F2), tag0, lane);
      if (lane < 2) __hip_atomic_store((GAS u64*)(ws + WS_GO) + h * 16 + lane, lane ? tag1 : tag0, RLX_AGENT);
    }
    bar();
    u32x4 T[4];
#pragma unroll
    for (int k = 0; k < 4; ++k) T[k] = ld128<AUX_SC1_NT>(rws, lane * 16, WS_PB + (((k >> 1) * 32 + h) * 16 + 2 * w + (k & 1)) * 1024);
    const u32x2 mlw = ld64<AUX_SC1_NT>(rws, (g & 1) * 4096 + c * 8, WS_ML + h * 128);
    const float ms = u2f(mlw[0]), M0 = row_max(ms), M = (M0 == -INFINITY) ? 0.f : M0;
    const float wt = ex2((ms - M) * LOG2E) * u2f(mlw[1]), cf = wt / row_sum(wt);
    float* cr = (float*)(lds + L_RING);
#pragma unroll
    for (int bb = 0; bb < 2; ++bb) {
      const float f0 = u2f(__builtin_amdgcn_readlane(f2u(cf), 16 * bb + 2 * w)), f1 = u2f(__builtin_amdgcn_readlane(f2u(cf), 16 * bb + 2 * w + 1));
      float a[8];
#pragma unroll
      for (int j = 0; j < 4; ++j) {
        a[2 * j] = f0 * u2f(T[2 * bb][j] << 16) + f1 * u2f(T[2 * bb + 1][j] << 16);
        a[2 * j + 1] = f0 * u2f(T[2 * bb][j] & 0xffff0000u) + f1 * u2f(T[2 * bb + 1][j] & 0xffff0000u);
      }
      *(f32x4*)(cr + (w * 2 + bb) * 512 + lane * 8) = f32x4{a[0], a[1], a[2], a[3]};
      *(f32x4*)(cr + (w * 2 + bb) * 512 + lane * 8 + 4) = f32x4{a[4], a[5], a[6], a[7]};
    }
    bar();
    {
      float c0 = 0.f, c1 = 0.f;
#pragma unroll
      for (int ww = 0; ww < 8; ++ww) { c0 += cr[ww * 1024 + tid]; c1 += cr[ww * 1024 + 512 + tid]; }
      const unsigned h0 = hilo(c0), h1 = hilo(c1);
      f32x4 D = {0.f, 0.f, 0.f, 0.f}, D2 = D;
      rowsum<16, 1>(W, __builtin_amdgcn_perm(h1, h0, 0x05040100u), sel, D);
      rowsum<16, 1>(W, __builtin_amdgcn_perm(h1, h0, 0x07060302u), sel, D2);
      D += D2;
#pragma unroll
      for (int r = 0; r < 4; ++r) red[(w * 2 + (c >> 3)) * 128 + 32 * g + 8 * r + (c & 7)] = D[r];
    }
    bar();
    if (tid < 256) {
      float v = 0.f;
#pragma unroll
      for (int ww = 0; ww < 8; ++ww) v += red[ww * 256 + tid];
      *(u16*)(lds + L_P + (tid >> 7) * 272 + (tid & 127) * 2) = (u16)pk_bf16(v, v);
    }
    bar();
    f32x4 D[4];
    project(U, lds, g, c & 1, D);
    if (g == 0) {
      __builtin_amdgcn_raw_buffer_store_b128(u32x4{mark(D[0][0], old, 0), mark(D[1][0], old, 1), mark(D[2][0], old, 2), mark(D[3][0], old, 3)}, rws, c * 16, WS_Y + (h * 4 + 3) * 2048 + w * 256, AUX_SC1);
      __builtin_amdgcn_raw_buffer_store_b128(u32x4{mark(D[0][1], old, 4), mark(D[1][1], old, 5), mark(D[2][1], old, 6), mark(D[3][1], old, 7)}, rws, c * 16, WS_Y + (128 + h * 4 + 3) * 2048 + w * 256, AUX_SC1);
    }
  } else {
    const int b = rs & 1, sp = rs >> 1;
    u32x4 W[16], S0[8], S1[8];
#define LDC(st, i) ld128<AUX_NT>(mk_rsrc(Cc, (st) < 8 ? (16u << 20) : 0u), lane * 16 + ((i) & 3) * 1024, (b * 8192 + sp * 512 + ((st) & 7) * 64 + 8 * w + ((i) & 4)) * 1024)
#define LDS_(S, st) _Pragma("unroll") for (int i = 0; i < 8; ++i) S[i] = LDC(st, i);
#define STC(slot, S) { int ln = lane; asm volatile("" : "+v"(ln)); char* base = lds + L_RING + (slot) * 65536 + (ln >> 4) * 16384 + 8 * w * 256; \
      _Pragma("unroll") for (int i = 0; i < 8; ++i) *(u32x4*)(base + i * 256 + 16 * ((ln & 15) ^ swz16(8 * w + i))) = S[i]; }
    LD_EPOCH();
    const u32x4 fy = ld128<AUX_SC1_NT>(rws, fy_off, WS_Y);
    LD_GATE();
    LDS_(S0, 0);
    LDS_(S1, 1);
    SB();
    USE_EPOCH(); USE_GATE();
    fold = lsb4(fy);
    asm volatile("" : "+v"(fold), "+v"(gate));
    tagr = tag1;
    STC(0, S0);
    SB();
    LDS_(S0, 2);
    SB();
    STC(1, S1);
    SB();
    LDS_(S1, 3);
    PIN();
    if (w == 0) wait_flags32((const GAS u64*)(ws + WS_F1), tag0, lane);
    bar();
#pragma unroll
    for (int k = 0; k < 4; ++k) *(u32x4*)(lds + L_QA + (4 * w + k) * 1024 + 16 * (lane ^ swz16(4 * w + k))) = ld128<AUX_SC1>(rws, lane * 16, WS_QA + (b * 32 + 4 * w + k) * 1024);
    bar();
    const int kt = w & 3, hh = w >> 2;
#pragma unroll
    for (int s = 0; s < 16; ++s) W[s] = *(const u32x4*)(lds + L_QA + (16 * hh + c) * 1024 + 16 * ((4 * s + g) ^ swz16(c)));
#pragma unroll
    for (int s = 0; s < 16; ++s) asm volatile("" : "+v"(W[s]));
    bar();
    float mq = -INFINITY, lq = 0.f, mp = -INFINITY;
    f32x16 A0, A1;
#pragma unroll
    for (int i = 0; i < 16; ++i) { A0[i] = 0.f; A1[i] = 0.f; }
    float* mx = (float*)(lds + L_MX);
#define STAGE(slot, REFILL) { const char* tile = lds + L_RING + (slot) * 65536; \
      int ln = lane; asm volatile("" : "+v"(ln)); \
      const int g = ln >> 4, c = ln & 15, swz = swz16(c), h5 = ln >> 5, hq = 16 * hh + c, hp = ln & 31; \
      const char* rowp = tile + (16 * kt + c) * 256; \
      f32x4 X0 = {0.f, 0.f, 0.f, 0.f}, X1 = X0; \
      { u32x4 fa[4], fb[4]; \
        _Pragma("unroll") for (int j = 0; j < 4; ++j) fa[j] = *(const u32x4*)(rowp + 16 * ((4 * j + g) ^ swz)); \
        _Pragma("unroll") for (int j = 0; j < 4; ++j) fb[j] = *(const u32x4*)(rowp + 16384 + 16 * ((4 * j + g) ^ swz)); \
        SB(); \
        X0 = mfma16(fa[0], W[0], X0); X1 = mfma16(fa[1], W[1], X1); X0 = mfma16(fa[2], W[2], X0); X1 = mfma16(fa[3], W[3], X1); \
        SB(); \
        _Pragma("unroll") for (int j = 0; j < 4; ++j) fa[j] = *(const u32x4*)(rowp + 32768 + 16 * ((4 * j + g) ^ swz)); \
        SB(); \
        X0 = mfma16(fb[0], W[4], X0); X1 = mfma16(fb[1], W[5], X1); X0 = mfma16(fb[2], W[6], X0); X1 = mfma16(fb[3], W[7], X1); \
        SB(); \
        _Pragma("unroll") for (int j = 0; j < 4; ++j) fb[j] = *(const u32x4*)(rowp + 49152 + 16 * ((4 * j + g) ^ swz)); \
        SB(); \
        X0 = mfma16(fa[0], W[8], X0); X1 = mfma16(fa[1], W[9], X1); X0 = mfma16(fa[2], W[10], X0); X1 = mfma16(fa[3], W[11], X1); \
        X0 = mfma16(fb[0], W[12], X0); X1 = mfma16(fb[1], W[13], X1); X0 = mfma16(fb[2], W[14], X0); X1 = mfma16(fb[3], W[15], X1); } \
      float sc[4]; \
      _Pragma("unroll") for (int r = 0; r < 4; ++r) sc[r] = bfr(bfr(X0[r] + X1[r]) * SCALE); \
      const float tm = xrow_max(fmaxf(fmaxf(sc[0], sc[1]), fmaxf(sc[2], sc[3]))); \
      if (g == 0) mx[kt * 32 + hq] = tm; \
      bar(); \
      { const float q0 = mx[hq], q1 = mx[32 + hq], q2 = mx[64 + hq], q3 = mx[96 + hq], p0 = mx[hp], p1 = mx[32 + hp], p2 = mx[64 + hp], p3 = mx[96 + hp]; \
        const float mn = fmaxf(fmaxf(mq, fmaxf(q0, q1)), fmaxf(q2, q3)), ms = (mn == -INFINITY) ? 0.f : mn; \
        float p[4]; \
        _Pragma("unroll") for (int r = 0; r < 4; ++r) p[r] = ex2((sc[r] - ms) * LOG2E); \
        *(u32x2*)(lds + L_P + hq * 144 + (16 * kt + 4 * g) * 2) = u32x2{pk_bf16(p[0], p[1]), pk_bf16(p[2], p[3])}; \
        lq = lq * ex2((mq - ms) * LOG2E) + ((p[0] + p[1]) + (p[2] + p[3])); \
        mq = mn; \
        const float mn2 = fmaxf(fmaxf(mp, fmaxf(p0, p1)), fmaxf(p2, p3)), ms2 = (mn2 == -INFINITY) ? 0.f : mn2; \
        const float al = ex2((mp - ms2) * LOG2E); mp = mn2; A0 *= al; A1 *= al; } \
      SB(); REFILL; SB(); \
      { const int blk = (ln >> 4) & 1, q4 = (ln & 15) >> 2, p4 = ln & 3; \
        const char* trp = tile + (w >> 1) * 16384 + (8 * h5 + q4) * 256 + 8 * (p4 & 1); \
        const int ch0 = 8 * (w & 1) + 2 * blk + (p4 >> 1), z0 = swz16(8 * h5 + q4), z1 = swz16(8 * h5 + 4 + q4); \
        s16x4 f[4][2][2]; \
        _Pragma("unroll") for (int ks = 0; ks < 4; ++ks) _Pragma("unroll") for (int ct = 0; ct < 2; ++ct) { \
          f[ks][ct][0] = __builtin_amdgcn_ds_read_tr16_b64_v4i16((LAS s16x4*)(trp + ks * 4096 + 16 * ((ch0 + 4 * ct) ^ z0))); \
          f[ks][ct][1] = __builtin_amdgcn_ds_read_tr16_b64_v4i16((LAS s16x4*)(trp + ks * 4096 + 1024 + 16 * ((ch0 + 4 * ct) ^ z1))); } \
        SB(); \
        bar(); \
        u32x4 P[4]; \
        _Pragma("unroll") for (int ks = 0; ks < 4; ++ks) P[ks] = *(const u32x4*)(lds + L_P + hp * 144 + (16 * ks + 8 * h5) * 2); \
        SB(); \
        _Pragma("unroll") for (int ks = 0; ks < 4; ++ks) { \
          A0 = mfma32(__builtin_bit_cast(u32x4, __builtin_shufflevector(f[ks][0][0], f[ks][0][1], 0, 1, 2, 3, 4, 5, 6, 7)), P[ks], A0); \
          A1 = mfma32(__builtin_bit_cast(u32x4, __builtin_shufflevector(f[ks][1][0], f[ks][1][1], 0, 1, 2, 3, 4, 5, 6, 7)), P[ks], A1); } \
        SB(); } }
    STAGE(0, ;);
#pragma unroll 1
    for (int it = 0; it < 3; ++it) {
      STAGE(1, { STC(0, S0); SB(); LDS_(S0, 2 * it + 4); });
      STAGE(0, { STC(1, S1); SB(); LDS_(S1, 2 * it + 5); });
    }
    STAGE(1, ;);
    const int h5 = lane >> 5, hq = 16 * hh + c, hp = lane & 31;
    float* lx = (float*)(lds + L_ML);
    lq = xrow_sum(lq);
    if (g == 0) lx[kt * 32 + hq] = lq;
    bar();
    const float lt = (lx[hp] + lx[32 + hp]) + (lx[64 + hp] + lx[96 + hp]);
    const float inv = 1.f / lt;
    char* img = lds + L_RING + hp * 1040 + (64 * w + 4 * h5) * 2;
#pragma unroll
    for (int k = 0; k < 4; ++k) {
      *(u32x2*)(img + 16 * k) = u32x2{pk_bf16(A0[4 * k] * inv, A0[4 * k + 1] * inv), pk_bf16(A0[4 * k + 2] * inv, A0[4 * k + 3] * inv)};
      *(u32x2*)(img + 64 + 16 * k) = u32x2{pk_bf16(A1[4 * k] * inv, A1[4 * k + 1] * inv), pk_bf16(A1[4 * k + 2] * inv, A1[4 * k + 3] * inv)};
    }
    if (tid < 32) __builtin_amdgcn_raw_buffer_store_b64(u32x2{f2u(mp), f2u(lt)}, rws, hp * 128, WS_ML + b * 4096 + sp * 8, AUX_SC1);
    bar();
#pragma unroll
    for (int k = 0; k < 4; ++k)
      __builtin_amdgcn_raw_buffer_store_b128(*(const u32x4*)(lds + L_RING + (4 * w + k) * 1040 + lane * 16), rws, lane * 16, WS_PB + ((b * 32 + 4 * w + k) * 16 + sp) * 1024, AUX_SC1_NT);
    DRAIN();
    bar();
    if (tid == 0) __hip_atomic_store((GAS u64*)(ws + WS_F2) + rs, tag0, RLX_AGENT);
  }
  {
    const bool isb = (lane & 12) == 12;
    float* fr = (float*)(lds + L_RING);
    u32x4 y = {0u, 0u, 0u, 0u};
#define POLL_Y(SLP) _Pragma("unroll 1") for (unsigned spins = 0; spins < SPIN_CAP; ++spins) { \
      PIN(); y = ld128<AUX_SC1>(rws, fy_off, WS_Y); if ((lsb4(y) ^ fold) == 15u) break; __builtin_amdgcn_s_sleep(SLP); }
#define SUM_ROWS(v) { const float a0 = v[0] + dppf<ROR8>(v[0]), a1 = v[1] + dppf<ROR8>(v[1]), a2 = v[2] + dppf<ROR8>(v[2]), a3 = v[3] + dppf<ROR8>(v[3]); \
      const float b0 = (c & 8) ? a2 : a0, b1 = (c & 8) ? a3 : a1; \
      const float c0 = b0 + dppf<0x124>(b0), c1 = b1 + dppf<0x12C>(b1); \
      v[0] = xrow_sum((c & 4) ? c0 : c1); }
    if (isb) { POLL_Y(16); }
    {
      float v[4];
#pragma unroll
      for (int j = 0; j < 4; ++j) v[j] = u2f(y[j] & ~0xffu);
      SUM_ROWS(v);
      if (lane < 16) fr[128 + w * 16 + lane] = v[0];
      if (lane == 0) *(unsigned*)(lds + L_FIN + w * 4) = fold;
    }
    bar();
    float bpart = 0.f;
    if (w == 0) {
#pragma unroll
      for (int ww = 0; ww < 8; ++ww) bpart += fr[128 + ww * 16 + c];
      bpart = bfr(bfr(1.f - gate) * bfr(bpart));
      const int po = (rb * 128 + 16 * (lane & 7)) * 2048 + rs * 64;
      const unsigned po_old = *(const unsigned*)(lds + L_FIN + (lane & 7) * 4);
#pragma unroll 1
      for (unsigned spins = 0; spins < SPIN_CAP; ++spins) {
        PIN();
        if (__any((ld32<AUX_SC1>(rws, po, WS_Y) ^ po_old) & 1u)) break;
        __builtin_amdgcn_s_sleep(24);
      }
    }
    GAS u16* op = (GAS u16*)out + rb * 512 + rs * 16 + 4 * (c & 3) + (((c >> 2) & 2) | (((c >> 2) & 1) ^ 1));
    asm volatile("" : "+v"(bpart), "+v"(op));
    bar();
    y = u32x4{0u, 0u, 0u, 0xfeu};
    if (!isb) { POLL_Y(0); }
    const float lse = u2f((((y[0] >> 1) & 0x7fu) | (((y[1] >> 1) & 0x7fu) << 7) | (((y[2] >> 1) & 0x7fu) << 14) | (((y[3] >> 1) & 0x7fu) << 21)) << 4);
    float M = fmaxf(lse, dppf<ROR8>(lse)); M = fmaxf(M, dppf<ROR4>(M));
    const float e = isb ? 0.f : ex2(lse - M);
    float L = e + dppf<ROR8>(e); L += dppf<ROR4>(L);
    const float cf = e * __builtin_amdgcn_rcpf(L);
    float v[4];
#pragma unroll
    for (int j = 0; j < 4; ++j) v[j] = cf * u2f(y[j] & ~0xffu);
    SUM_ROWS(v);
    if (lane < 16) fr[w * 16 + lane] = v[0];
    bar();
    if (tid < 16) {
      const float a = ((fr[tid] + fr[16 + tid]) + (fr[32 + tid] + fr[48 + tid])) + ((fr[64 + tid] + fr[80 + tid]) + (fr[96 + tid] + fr[112 + tid]));
      const float o = bfr(gate * bfr(a)) + bpart;
      *op = (u16)pk_bf16(o, o);
    }
    if (rs == 0 && rb == 0 && w == 0) {
      const u64 now = __builtin_amdgcn_s_memrealtime();
      float mn = 1e9f, mx = 0.f, sm = 0.f, e_mx = 0.f, e_sm = 0.f;
      for (int k = 0; k < 4; ++k) { const float d = (float)(long long)(now - __hip_atomic_load((GAS u64*)(ws + (3 << 20)) + (k * 64 + lane) * 16, RLX_AGENT)); mn = fminf(mn, d); mx = fmaxf(mx, d); sm += d; }
      for (int k = 0; k < 3; ++k) { const float d = (float)(long long)(now - __hip_atomic_load((GAS u64*)(ws + (3 << 20)) + (k * 64 + lane) * 16 + 1, RLX_AGENT)); if (d >= 0.f && d < 2000.f) { e_mx += 1.f; e_sm += d; } }
      mn = -xrow_max(row_max(-mn)); mx = xrow_max(row_max(mx)); sm = xrow_sum(row_sum(sm)) / 256.f; e_sm = xrow_sum(row_sum(e_sm)) / xrow_sum(row_sum(e_mx));
      float q = DIAG == 1 ? (mx - mn) * 10.f : DIAG == 2 ? mx * 1.f : DIAG == 3 ? e_sm * 10.f : DIAG == 4 ? (mx - sm) * 10.f : 0.f;
      q = fminf(fmaxf(q, 0.f), 20000.f);
      while ((float)(long long)(__builtin_amdgcn_s_memrealtime() - now) < q) __builtin_amdgcn_s_sleep(1);
    }
    if (rs == 0 && tid == 0) __hip_atomic_store((GAS u64*)(ws + WS_EPOCH + rb * 128), tagr, RLX_AGENT);
  }
    return;
  }
  {
    const int h = __builtin_amdgcn_readfirstlane(4 * x8 + idx / 6), b = __builtin_amdgcn_readfirstlane((idx % 6) / 3), sp = __builtin_amdgcn_readfirstlane(idx % 3);
    const int src = b * 128 + h * 4 + sp;
    const u32x4 sel = make_sel(c);
    const int tb = (b * 32 + h) * 512, t1 = tb + 512 * (sp + 1) / 3, t0 = tb + 512 * sp / 3 + w;
    const rsrc_t rk = mk_rsrc(Kc, t1 * 4096), rv = mk_rsrc(Vc, t1 * 4096), ro = mk_rsrc(wo, W_BYTES);
#define LDT(r, i_, j) ld128<AUX_NT>(r, lane * 16 + (j) * 1024, (t0 + 8 * (i_)) * 4096)
#define REQ(a, r, i_) { _Pragma("unroll") for (int j = 0; j < 4; ++j) a[j] = LDT(r, i_, j); SB(); }
#define REQ_REST(a, r) { _Pragma("unroll") for (int j = 0; j < 4; ++j) a[j] = ld128<AUX_NT>(r, lane * 16, (t0 - w + 168) * 4096 + (w + 8 * j) * 1024); SB(); }
#define ARRIVED(a) { _Pragma("unroll") for (int j = 0; j < 4; ++j) asm volatile("" : "+v"(a[j])); SB(); }
    u32x4 W[16], A[4], B[4], C[4], D4[4], E[4], F[4];
    const int wr = __builtin_amdgcn_readfirstlane((w + (idx % 6) * 4 / 3) & 7);
    unsigned x0 = x[64 * wr + lane], x1 = x[512 + 64 * wr + lane];
    LD_EPOCH();
    unsigned old = ld32<AUX_SC1_NT>(rws, lane * 4, WS_Y + src * 2048 + w * 256);
    const unsigned touch = ld32<AUX_NT>(ro, 0, h * 131072), touch2 = ld32<AUX_NT>(rv, 0, t0 * 4096);
    const unsigned touch3 = ld32<AUX_NT>(rk, 0, t0 * 4096);
    SB();
    {
      const rsrc_t rq = mk_rsrc(wq, W_BYTES);
#pragma unroll
      for (int i = 0; i < 16; ++i) W[i] = ld128<AUX_NT>(rq, g * (16 * 8192) + c * 16, (64 * wr + i) * 8192 + h * 256);
    }
    SB();
    asm volatile("" : "+v"(x0), "+v"(x1) : "v"(touch3));
    {
      f32x4 D = {0.f, 0.f, 0.f, 0.f};
      rowsum_refill<0>(W, x0 | (x1 << 16), sel, D, [&](auto T) {
        constexpr int j = T & 3;
        if constexpr (T < 4) A[j] = LDT(rk, 0, j); else if constexpr (T < 8) B[j] = LDT(rk, 1, j);
        else if constexpr (T < 12) C[j] = LDT(rk, 2, j); else D4[j] = LDT(rk, 3, j); });
#pragma unroll
      for (int r = 0; r < 4; ++r) red[(wr * 2 + (c >> 3)) * 128 + 32 * g + 8 * r + (c & 7)] = D[r];
    }
    SB();
    REQ(E, rk, 4); REQ_REST(F, rk);
    bar();
    u32x4 q;
    QREAD(q, b);
    float* scw = (float*)(lds + L_SC + w * 1536);
    float m = -INFINITY;
#define SCORE(a, i_, REST) { float xs[4]; _Pragma("unroll") for (int j = 0; j < 4; ++j) xs[j] = dot8(a[j], q); \
      float s = bfr(bfr(treduce<4>(xs, c)) * SCALE); \
      if (REST) s = (w + 8 * (c >> 2) < 4 * (t1 - t0 + w - 168)) ? s : -INFINITY; \
      m = fmaxf(m, s); scw[(i_) * 16 + (lane >> 2)] = s; asm volatile("" : "+v"(m)); SB(); }
    SCORE(A, 0, false); REQ(A, rk, 5);
    SCORE(B, 1, false);
    REQ(B, rk, 6);
    SCORE(C, 2, false); SCORE(D4, 3, false); SCORE(E, 4, false); SCORE(F, 21, true);
    USE_EPOCH();
    u64 tag = b ? tag1 : tag0;
    asm volatile("" : "+s"(tag));
    asm volatile("" : "+v"(old) : "v"(touch), "v"(touch2));
    SB();
    const unsigned img = (unsigned)(uintptr_t)(LAS char*)(lds + L_RING) + w * 16384;
    const int dvo = (lane >> 3) * 1024 + 16 * ((lane & 7) ^ ((lane >> 4 & 1) << 1));
#define DMA8(k_, vo) { unsigned keep; \
      asm volatile("s_mov_b32 %0, m0\n\ts_mov_b32 m0, %1\n\ts_nop 4\n\tbuffer_load_dwordx4 %2, %3, %4 offen nt lds\n\ts_mov_b32 m0, %0" : "=&s"(keep) \
                   : "s"(__builtin_amdgcn_readfirstlane(img + (k_) * 1024)), "v"(vo), "s"(ro), "s"(__builtin_amdgcn_readfirstlane((h * 128 + 8 * (k_)) * 1024 + w * 128)) : "memory"); SB(); }
#pragma unroll 1
    for (int i = 5; i < 21; i += 2) {
      const rsrc_t rn = mk_rsrc(i < 19 ? Kc : Vc, t1 * 4096);
      SCORE(A, i, false); REQ(A, rn, i < 19 ? i + 2 : 0); DMA8(i - 5, dvo);
      SCORE(B, i + 1, false); REQ(B, rn, i < 19 ? i + 3 : 1); DMA8(i - 4, dvo ^ 64);
    }
    m = xrow_max(row_max(m));
    float l = 0.f;
    {
      const float ms = (m == -INFINITY) ? 0.f : m;
      if (lane < 32) scw[352 + lane] = -INFINITY;
#pragma unroll
      for (int k = 0; k < 6; ++k) { const float p = ex2((scw[64 * k + lane] - ms) * LOG2E); l += p; scw[64 * k + lane] = u2f(hilo(p)); }
      l = xrow_sum(row_sum(l));
    }
    f32x4 O = {0.f, 0.f, 0.f, 0.f};
#define PV(a, i_) { rowsum<4, 4>(a, f2u(scw[(i_) * 16 + (lane >> 2)]), sel, O); asm volatile("" : "+v"(O)); SB(); }
    PV(A, 0); REQ(A, rv, 2);
    asm volatile("s_waitcnt vmcnt(4)" ::: "memory");
    {
      const int q4 = c >> 2, p4 = lane & 3, phi = (q4 >> 1) | ((g & 1) << 1);
#pragma unroll
      for (int s = 0; s < 4; ++s)
#pragma unroll
        for (int ct = 0; ct < 4; ++ct) {
          const char* a = lds + L_RING + w * 16384 + (32 * s + 8 * g + q4) * 128 + 16 * ((p4 >> 1) | ((ct ^ phi) << 1)) + 8 * (p4 & 1);
          const s16x4 lo = __builtin_amdgcn_ds_read_tr16_b64_v4i16((LAS s16x4*)a), hi = __builtin_amdgcn_ds_read_tr16_b64_v4i16((LAS s16x4*)(a + 512));
          W[4 * s + ct] = __builtin_bit_cast(u32x4, __builtin_shufflevector(lo, hi, 0, 1, 2, 3, 4, 5, 6, 7));
        }
    }
#pragma unroll
    for (int i = 0; i < 16; ++i) asm volatile("" : "+v"(W[i]));
    PV(B, 1);
#pragma unroll 1
    for (int i = 2; i < 20; i += 2) {
      ARRIVED(A); REQ(B, rv, i + 1); PV(A, i);
      ARRIVED(B); REQ(A, rv, i + 2); PV(B, i + 1);
    }
    REQ_REST(B, rv);
    const GAS u64* go = (const GAS u64*)(ws + WS_GO) + h * 16 + b;
    u64 gov = __hip_atomic_load(go, RLX_AGENT);
    SB();
    PV(A, 20); PV(B, 21);
    float* ml = (float*)(lds + L_ML);
#pragma unroll
    for (int r = 0; r < 4; ++r) {
      const unsigned hl = hilo(O[r] + dppf<ROR8>(O[r]));
      *(u16*)(lds + L_P + (2 * w + (c >> 3)) * 272 + (32 * g + 8 * r + (c & 7)) * 2) = (u16)((c & 8) ? (hl >> 16) : hl);
    }
    if (lane == 0) { ml[w] = m; ml[8 + w] = l; }
    bar();
    f32x4 D[4];
    const float mj = ml[lane & 7], lj = ml[8 + (lane & 7)], ma = ml[2 * g], mb = ml[2 * g + 1];
    project(W, lds, g, c, D);
    float M = fmaxf(mj, dppf<XOR7>(mj)); M = fmaxf(M, dppf<XOR2>(M)); M = fmaxf(M, dppf<XOR1>(M));
    const float Ms = (M == -INFINITY) ? 0.f : M;
    float L = ex2((mj - Ms) * LOG2E) * lj; L += dppf<XOR7>(L); L += dppf<XOR2>(L); L += dppf<XOR1>(L);
    const float inv = __builtin_amdgcn_rcpf(L), fa = ex2((ma - Ms) * LOG2E) * inv, fb = ex2((mb - Ms) * LOG2E) * inv;
    float y4[4];
#pragma unroll
    for (int k = 0; k < 4; ++k) y4[k] = fa * (D[k][0] + D[k][1]) + fb * (D[k][2] + D[k][3]);
    const auto s0 = __builtin_amdgcn_permlane32_swap(f2u(y4[0]), f2u(y4[2]), false, false), s1 = __builtin_amdgcn_permlane32_swap(f2u(y4[1]), f2u(y4[3]), false, false);
    const auto s2 = __builtin_amdgcn_permlane16_swap(f2u(u2f(s0[0]) + u2f(s0[1])), f2u(u2f(s1[0]) + u2f(s1[1])), false, false);
    const unsigned code = f2u(Ms * LOG2E + __builtin_amdgcn_logf(L)) >> 4;
    const unsigned yw = (f2u(u2f(s2[0]) + u2f(s2[1])) & ~0xffu) | (((code >> (7 * (lane & 3))) & 0x7fu) << 1) | ((old & 1u) ^ 1u);
    const u32x4 yv = {yw, f2u(dppf<0x55>(u2f(yw))), f2u(dppf<0xAA>(u2f(yw))), f2u(dppf<0xFF>(u2f(yw)))};
#pragma unroll 1
    for (unsigned spins = 0; uniform64(gov) != tag && spins < SPIN_CAP; ++spins) gov = __hip_atomic_load(go, RLX_AGENT);
    if (tid == 0) { __hip_atomic_store((GAS u64*)(ws + (3 << 20)) + bid * 16 + 1, (u64)__builtin_amdgcn_s_memrealtime(), RLX_AGENT); DRAIN(); }
    PIN();
    if ((lane & 3) == 0) __builtin_amdgcn_raw_buffer_store_b128(yv, rws, lane * 4, WS_Y + src * 2048 + w * 256, AUX_SC1);
  }
}

extern "C" void kernel_launch(void* const* d_in, const int* in_sizes, int n_in, void* d_out, int out_size, void* d_ws, size_t ws_size, hipStream_t stream) {
  fused_decode<<<256, 512, 0, stream>>>((const u16*)d_in[0], (const u16*)d_in[1], (const u16*)d_in[2], (const u16*)d_in[3], (const u16*)d_in[4],
                                        (const u16*)d_in[6], (const u16*)d_in[7], (const u16*)d_in[8], (const u16*)d_in[9], (const u16*)d_in[10],
                                        (const u16*)d_in[11], (u16*)d_out, (char*)d_ws);
}
```

```cpp
#include <hip/hip_runtime.h>
#include <stdint.h>
#include <type_traits>

typedef unsigned short u16;
typedef unsigned long long u64;
typedef unsigned u32x4 __attribute__((ext_vector_type(4)));
typedef unsigned u32x2 __attribute__((ext_vector_type(2)));
typedef float f32x4 __attribute__((ext_vector_type(4)));
typedef float f32x16 __attribute__((ext_vector_type(16)));
typedef __bf16 bf16x8 __attribute__((ext_vector_type(8)));
typedef __bf16 bf16x2 __attribute__((ext_vector_type(2)));
typedef short s16x4 __attribute__((ext_vector_type(4)));
typedef __amdgpu_buffer_rsrc_t rsrc_t;
#define GAS __attribute__((address_space(1)))
#define LAS __attribute__((address_space(3)))
#define DI __device__ __forceinline__
#define RLX_AGENT __ATOMIC_RELAXED, __HIP_MEMORY_SCOPE_AGENT

constexpr int AUX_NT = 2, AUX_SC1 = 16, AUX_SC1_NT = 18;
constexpr int W_BYTES = 512 * 4096 * 2;
constexpr float SCALE = 0.08838834764831845f;
constexpr float LOG2E = 1.4426950408889634f;

constexpr int WS_EPOCH = 0;
constexpr int WS_F1 = 1024;
constexpr int WS_F2 = 2048;
constexpr int WS_GO = 4096;
constexpr int WS_QA = 8192;
constexpr int WS_ML = WS_QA + 65536;
constexpr int WS_PB = WS_ML + 8192;
constexpr int WS_Y = WS_PB + 2 * 32 * 16 * 1024;
constexpr int WS_END = WS_Y + 2 * 128 * 2048;

constexpr int L_RING = 0;
constexpr int L_RED = 131072;
constexpr int L_P = L_RED + 8192;
constexpr int L_MX = L_P + 32 * 144;
constexpr int L_ML = L_MX + 512;
constexpr int L_FIN = L_ML + 512;
constexpr int L_SC = 147456;
constexpr int L_QA = 131072;
constexpr int L_END = 163840;

DI rsrc_t mk_rsrc(const void* p, unsigned bytes) { return __builtin_amdgcn_make_buffer_rsrc((void*)p, 0, bytes, 0x00020000); }
template <int AUX> DI u32x4 ld128(rsrc_t r, int voff, int soff) { return __builtin_amdgcn_raw_buffer_load_b128(r, voff, soff, AUX); }
template <int AUX> DI u32x2 ld64(rsrc_t r, int voff, int soff) { return __builtin_amdgcn_raw_buffer_load_b64(r, voff, soff, AUX); }
template <int AUX> DI unsigned ld32(rsrc_t r, int voff, int soff) { return __builtin_amdgcn_raw_buffer_load_b32(r, voff, soff, AUX); }
DI float u2f(unsigned u) { return __builtin_bit_cast(float, u); }
DI unsigned f2u(float f) { return __builtin_bit_cast(unsigned, f); }
DI unsigned pk_bf16(float lo, float hi) { bf16x2 r; r[0] = (__bf16)lo; r[1] = (__bf16)hi; return __builtin_bit_cast(unsigned, r); }
DI float bfr(float x) { return u2f(pk_bf16(x, x) << 16); }
DI unsigned hilo(float x) { const float h = bfr(x); return pk_bf16(h, x - h); }
DI float ex2(float x) { return __builtin_amdgcn_exp2f(x); }
DI float dot2(unsigned a, unsigned b, float c) { return __builtin_amdgcn_fdot2_f32_bf16(__builtin_bit_cast(bf16x2, a), __builtin_bit_cast(bf16x2, b), c, false); }
DI float dot8(const u32x4& a, const u32x4& b) { return dot2(a[3], b[3], dot2(a[2], b[2], dot2(a[1], b[1], dot2(a[0], b[0], 0.f)))); }
template <int CTRL> DI float dppf(float x) { return u2f(__builtin_amdgcn_mov_dpp(f2u(x), CTRL, 0xf, 0xf, true)); }
template <int T> DI unsigned bcast16(unsigned v) { return __builtin_amdgcn_mov_dpp(v, 0x150 + T, 0xf, 0xf, true); }
constexpr int XOR1 = 0xB1, XOR2 = 0x4E, XOR7 = 0x141, ROR8 = 0x128, ROR4 = 0x124;
DI float xrow_max(float x) {
  auto s = __builtin_amdgcn_permlane16_swap(f2u(x), f2u(x), false, false); x = fmaxf(u2f(s[0]), u2f(s[1]));
  auto t = __builtin_amdgcn_permlane32_swap(f2u(x), f2u(x), false, false); return fmaxf(u2f(t[0]), u2f(t[1]));
}
DI float xrow_sum(float x) {
  auto s = __builtin_amdgcn_permlane16_swap(f2u(x), f2u(x), false, false); x = u2f(s[0]) + u2f(s[1]);
  auto t = __builtin_amdgcn_permlane32_swap(f2u(x), f2u(x), false, false); return u2f(t[0]) + u2f(t[1]);
}
DI float row_max(float x) { x = fmaxf(x, dppf<ROR8>(x)); x = fmaxf(x, dppf<ROR4>(x)); x = fmaxf(x, dppf<XOR2>(x)); return fmaxf(x, dppf<XOR1>(x)); }
DI float row_sum(float x) { x += dppf<ROR8>(x); x += dppf<ROR4>(x); x += dppf<XOR2>(x); return x + dppf<XOR1>(x); }
DI f32x4 mfma16(u32x4 a, u32x4 b, f32x4 c) { return __builtin_amdgcn_mfma_f32_16x16x32_bf16(__builtin_bit_cast(bf16x8, a), __builtin_bit_cast(bf16x8, b), c, 0, 0, 0); }
DI f32x16 mfma32(u32x4 a, u32x4 b, f32x16 c) { return __builtin_amdgcn_mfma_f32_32x32x16_bf16(__builtin_bit_cast(bf16x8, a), __builtin_bit_cast(bf16x8, b), c, 0, 0, 0); }
DI u64 uniform64(u64 v) { return ((u64)(unsigned)__builtin_amdgcn_readfirstlane((unsigned)(v >> 32)) << 32) | (unsigned)__builtin_amdgcn_readfirstlane((unsigned)v); }
DI u64 next_tag(u64 e) { return e * 0x9E3779B97F4A7C15ull + 0x632BE59BD9B4E019ull; }
DI void bar() { __syncthreads(); }
#define DRAIN() asm volatile("s_waitcnt vmcnt(0)" ::: "memory")
#define PIN() asm volatile("" ::: "memory")
#define SB() __builtin_amdgcn_sched_barrier(0)
constexpr unsigned SPIN_CAP = 1u << 22;

template <int N, int STEP, int T = 0> DI void rowsum(const u32x4 (&W)[N], unsigned pair, const u32x4& sel, f32x4& D) {
  if constexpr (T < N) {
    const unsigned s = bcast16<T * STEP>(pair);
    const u32x4 B = {__builtin_amdgcn_perm(s, s, sel[0]), __builtin_amdgcn_perm(s, s, sel[1]), __builtin_amdgcn_perm(s, s, sel[2]), __builtin_amdgcn_perm(s, s, sel[3])};
    D = mfma16(W[T], B, D);
    rowsum<N, STEP, T + 1>(W, pair, sel, D);
  }
}
template <int T, class F> DI void rowsum_refill(const u32x4 (&W)[16], unsigned pair, const u32x4& sel, f32x4& D, F&& issue) {
  if constexpr (T < 16) {
    const unsigned s = bcast16<T>(pair);
    const u32x4 B = {__builtin_amdgcn_perm(s, s, sel[0]), __builtin_amdgcn_perm(s, s, sel[1]), __builtin_amdgcn_perm(s, s, sel[2]), __builtin_amdgcn_perm(s, s, sel[3])};
    D = mfma16(W[T], B, D);
    asm volatile("" : "+v"(D));
    __builtin_amdgcn_sched_barrier(0);
    issue(std::integral_constant<int, T>{});
    __builtin_amdgcn_sched_barrier(0);
    rowsum_refill<T + 1>(W, pair, sel, D, issue);
  }
}
DI int swz16(int row) { const int u = (row >> 2) & 3; return ((row & 3) << 2) | (((u ^ (u >> 1)) & 1) << 1) | (u >> 1); }
DI u32x4 make_sel(int c) {
  const int j = c & 7; const unsigned b = (c & 8) ? 0x0302u : 0x0100u;
  const unsigned v = (j & 1) ? ((b << 16) | 0x0c0cu) : (0x0c0c0000u | b);
  u32x4 s;
#pragma unroll
  for (int w = 0; w < 4; ++w) s[w] = (w == (j >> 1)) ? v : 0x0c0c0c0cu;
  return s;
}
template <int N> DI float treduce(float (&x)[N], int c) {
#define TR_STEP(CNT, CTRL, BIT) _Pragma("unroll") for (int i = 0; i < (CNT); ++i) { const float a = x[i] + dppf<CTRL>(x[i]), b = x[i + (CNT)] + dppf<CTRL>(x[i + (CNT)]); x[i] = (c & (BIT)) ? b : a; }
  TR_STEP(N / 2, ROR8, 8)
  TR_STEP(N / 4, XOR7, 4)
  if constexpr (N >= 8) { TR_STEP(N / 8, XOR2, 2) } else { x[0] += dppf<XOR2>(x[0]); }
  if constexpr (N >= 16) { TR_STEP(N / 16, XOR1, 1) } else { x[0] += dppf<XOR1>(x[0]); }
#undef TR_STEP
  return x[0];
}
DI void wait_flags32(const GAS u64* f, u64 tag, int lane) {
#pragma unroll 1
  for (unsigned spins = 0; spins < SPIN_CAP; ++spins) {
    if (__all(__hip_atomic_load(f + (lane & 31), RLX_AGENT) == tag)) break;
    __builtin_amdgcn_s_sleep(3);
  }
  __builtin_amdgcn_fence(__ATOMIC_ACQUIRE, "wavefront");
  asm volatile("" ::: "memory");
}
DI void transpose_w(const u32x2 (&R)[32], u32x4 (&F)[16]) {
#pragma unroll
  for (int s = 0; s < 4; ++s)
#pragma unroll
    for (int t = 0; t < 4; ++t)
#pragma unroll
      for (int jj = 0; jj < 4; ++jj) F[4 * s + t][jj] = __builtin_amdgcn_perm(R[8 * s + 2 * jj + 1][t >> 1], R[8 * s + 2 * jj][t >> 1], (t & 1) ? 0x07060302u : 0x05040100u);
}
DI void project(const u32x4 (&F)[16], const char* lds, int g, int c, f32x4 (&D)[4]) {
  u32x4 A[4];
#pragma unroll
  for (int s = 0; s < 4; ++s) A[s] = *(const u32x4*)(lds + L_P + c * 272 + 64 * s + 16 * g);
  __builtin_amdgcn_sched_barrier(0);
#pragma unroll
  for (int t = 0; t < 4; ++t) D[t] = f32x4{0.f, 0.f, 0.f, 0.f};
#pragma unroll
  for (int s = 0; s < 4; ++s)
#pragma unroll
    for (int t = 0; t < 4; ++t) D[t] = mfma16(A[s], F[4 * s + t], D[t]);
}
DI unsigned lsb4(const u32x4& v) { return (v[0] & 1u) | ((v[1] & 1u) << 1) | ((v[2] & 1u) << 2) | ((v[3] & 1u) << 3); }
DI unsigned mark(float v, unsigned old, int k) { return (f2u(v) & ~1u) | (((old >> k) & 1u) ^ 1u); }


__global__ void __launch_bounds__(512, 2)
fused_decode(const u16* __restrict__ x, const u16* __restrict__ Kc, const u16* __restrict__ Vc, const u16* __restrict__ wq, const u16* __restrict__ wo,
             const u16* __restrict__ Cc, const u16* __restrict__ w_q, const u16* __restrict__ w_uk, const u16* __restrict__ w_uv, const u16* __restrict__ w_o,
             const u16* __restrict__ gw, u16* __restrict__ out, char* ws) {
  __shared__ __attribute__((aligned(16))) char lds[L_END];
  const int bid = blockIdx.x, tid = threadIdx.x, lane = tid & 63, w = __builtin_amdgcn_readfirstlane(tid >> 6), g = lane >> 4, c = lane & 15;
  const int x8 = bid & 7, idx = bid >> 3;
  asm volatile("" :: "s"(x), "s"(Kc), "s"(Vc), "s"(wq), "s"(wo), "s"(ws));
  const rsrc_t rws = mk_rsrc(ws, WS_END);
  float* red = (float*)(lds + L_RED);
#define LD_EPOCH() u64 e0 = __hip_atomic_load((GAS u64*)(ws + WS_EPOCH), RLX_AGENT), e1 = __hip_atomic_load((GAS u64*)(ws + WS_EPOCH + 128), RLX_AGENT);
#define USE_EPOCH() asm volatile("" : "+v"(e0), "+v"(e1)); const u64 tag0 = next_tag(uniform64(e0)), tag1 = next_tag(uniform64(e1));
#define LD_ROWS(W, ptr) { const rsrc_t rq = mk_rsrc(ptr, W_BYTES); \
    _Pragma("unroll") for (int t = 0; t < 16; ++t) W[t] = ld128<AUX_NT>(rq, g * (16 * 8192) + c * 16, (64 * w + t) * 8192 + h * 256); }
#define LD_OUTW(R, ptr) { const rsrc_t ro = mk_rsrc(ptr, W_BYTES); \
    _Pragma("unroll") for (int i = 0; i < 32; ++i) R[i] = ld64<AUX_NT>(ro, g * 8192 + c * 8 + (i & 3) * 1024, (h * 128 + 32 * (i >> 3) + (i & 4)) * 1024 + w * 128); }
#define QREAD(q, bb) { f32x4 a0 = {0.f, 0.f, 0.f, 0.f}, a1 = a0; \
    _Pragma("unroll") for (int ww = 0; ww < 8; ++ww) { const f32x4* p = (const f32x4*)(red + (ww * 2 + (bb)) * 128 + 8 * c); a0 += p[0]; a1 += p[1]; } \
    q = u32x4{pk_bf16(a0[0], a0[1]), pk_bf16(a0[2], a0[3]), pk_bf16(a1[0], a1[1]), pk_bf16(a1[2], a1[3])}; }

  if (idx >= 24) {
  const int rb = idx >= 28, rs = __builtin_amdgcn_readfirstlane(4 * x8 + (idx & 3));
  const int fy_off = (rb * 128 + (tid >> 2)) * 2048 + rs * 64 + (tid & 3) * 16;
  unsigned fold; u64 tagr;
  float gate;
#define LD_GATE() const u32x4 xv = *(const u32x4*)(x + rb * 512 + lane * 8), gv = *(const u32x4*)(gw + lane * 8);
#define USE_GATE() { float s = 0.f; \
    _Pragma("unroll") for (int j = 0; j < 4; ++j) s += bfr(u2f(xv[j] << 16) * u2f(gv[j] << 16)) + bfr(u2f(xv[j] & 0xffff0000u) * u2f(gv[j] & 0xffff0000u)); \
    s = bfr(xrow_sum(row_sum(s))); gate = bfr(1.f / (1.f + __expf(-s))); }

  if (idx < 28) {
    const int h = rs;
    const u32x4 sel = make_sel(c);
    u32x4 W[16], U[16];
    unsigned x0 = x[tid], x1 = x[512 + tid];
    const unsigned tch = ld32<AUX_NT>(mk_rsrc(w_uk, W_BYTES), (lane & 1) << 21, h * 256) ^ ld32<AUX_NT>(mk_rsrc(w_uv, W_BYTES), (lane & 1) << 21, h * 256) ^ ld32<AUX_NT>(mk_rsrc(w_o, W_BYTES), 0, h * 131072);
    LD_ROWS(W, w_q);
    SB();
    {
      asm volatile("" : "+v"(x0), "+v"(x1) : "v"(tch));
      const unsigned xp = x0 | (x1 << 16);
      const rsrc_t ru = mk_rsrc(w_uk, W_BYTES);
      f32x4 D = {0.f, 0.f, 0.f, 0.f};
      rowsum_refill<0>(W, xp, sel, D, [&](auto T) { U[T] = ld128<AUX_NT>(ru, g * (16 * 8192) + c * 16, (64 * w + T) * 8192 + h * 256); });
#pragma unroll
      for (int r = 0; r < 4; ++r) red[(w * 2 + (c >> 3)) * 128 + 32 * g + 8 * r + (c & 7)] = D[r];
    }
    LD_EPOCH();
    const u32x4 fy = ld128<AUX_SC1_NT>(rws, fy_off, WS_Y);
    const u32x4 oy0 = ld128<AUX_SC1_NT>(rws, c * 16, WS_Y + (h * 4 + 3) * 2048 + w * 256), oy1 = ld128<AUX_SC1_NT>(rws, c * 16, WS_Y + (128 + h * 4 + 3) * 2048 + w * 256);
    LD_GATE();
    SB();
    bar();
#pragma unroll
    for (int bb = 0; bb < 2; ++bb) {
      u32x4 q; QREAD(q, bb);
      float xs[16];
#pragma unroll
      for (int t = 0; t < 16; ++t) xs[t] = dot8(U[t], q);
      const float qa = treduce<16>(xs, c);
      *(u16*)(lds + L_RING + bb * 1024 + tid * 2) = (u16)pk_bf16(qa, qa);
    }
    SB();
    USE_EPOCH(); USE_GATE();
    fold = lsb4(fy);
    unsigned old = lsb4(oy0) | (lsb4(oy1) << 4);
    asm volatile("" : "+v"(fold), "+v"(gate), "+v"(old));
    tagr = tag0;
    DRAIN();
    bar();
    if (w == 0) {
      __builtin_amdgcn_raw_buffer_store_b128(*(const u32x4*)(lds + L_RING + lane * 16), rws, lane * 16, WS_QA + h * 1024, AUX_SC1);
      __builtin_amdgcn_raw_buffer_store_b128(*(const u32x4*)(lds + L_RING + 1024 + lane * 16), rws, lane * 16, WS_QA + (32 + h) * 1024, AUX_SC1);
      DRAIN();
      if (lane == 0) __hip_atomic_store((GAS u64*)(ws + WS_F1) + h, tag0, RLX_AGENT);
    }
    {
      u32x2 R[32];
      LD_OUTW(R, w_o);
      LD_ROWS(W, w_uv);
      SB();
      transpose_w(R, U);
    }
#pragma unroll
    for (int i = 0; i < 16; ++i) asm volatile("" : "+v"(W[i]), "+v"(U[i]));
    if (w == 0) {
      wait_flags32((const GAS u64*)(ws + WS_F2), tag0, lane);
      if (lane < 2) __hip_atomic_store((GAS u64*)(ws + WS_GO) + h * 16 + lane, lane ? tag1 : tag0, RLX_AGENT);
    }
    bar();
    u32x4 T[4];
#pragma unroll
    for (int k = 0; k < 4; ++k) T[k] = ld128<AUX_SC1_NT>(rws, lane * 16, WS_PB + (((k >> 1) * 32 + h) * 16 + 2 * w + (k & 1)) * 1024);
    const u32x2 mlw = ld64<AUX_SC1_NT>(rws, (g & 1) * 4096 + c * 8, WS_ML + h * 128);
    const float ms = u2f(mlw[0]), M0 = row_max(ms), M = (M0 == -INFINITY) ? 0.f : M0;
    const float wt = ex2((ms - M) * LOG2E) * u2f(mlw[1]), cf = wt / row_sum(wt);
    float* cr = (float*)(lds + L_RING);
#pragma unroll
    for (int bb = 0; bb < 2; ++bb) {
      const float f0 = u2f(__builtin_amdgcn_readlane(f2u(cf), 16 * bb + 2 * w)), f1 = u2f(__builtin_amdgcn_readlane(f2u(cf), 16 * bb + 2 * w + 1));
      float a[8];
#pragma unroll
      for (int j = 0; j < 4; ++j) {
        a[2 * j] = f0 * u2f(T[2 * bb][j] << 16) + f1 * u2f(T[2 * bb + 1][j] << 16);
        a[2 * j + 1] = f0 * u2f(T[2 * bb][j] & 0xffff0000u) + f1 * u2f(T[2 * bb + 1][j] & 0xffff0000u);
      }
      *(f32x4*)(cr + (w * 2 + bb) * 512 + lane * 8) = f32x4{a[0], a[1], a[2], a[3]};
      *(f32x4*)(cr + (w * 2 + bb) * 512 + lane * 8 + 4) = f32x4{a[4], a[5], a[6], a[7]};
    }
    bar();
    {
      float c0 = 0.f, c1 = 0.f;
#pragma unroll
      for (int ww = 0; ww < 8; ++ww) { c0 += cr[ww * 1024 + tid]; c1 += cr[ww * 1024 + 512 + tid]; }
      const unsigned h0 = hilo(c0), h1 = hilo(c1);
      f32x4 D = {0.f, 0.f, 0.f, 0.f}, D2 = D;
      rowsum<16, 1>(W, __builtin_amdgcn_perm(h1, h0, 0x05040100u), sel, D);
      rowsum<16, 1>(W, __builtin_amdgcn_perm(h1, h0, 0x07060302u), sel, D2);
      D += D2;
#pragma unroll
      for (int r = 0; r < 4; ++r) red[(w * 2 + (c >> 3)) * 128 + 32 * g + 8 * r + (c & 7)] = D[r];
    }
    bar();
    if (tid < 256) {
      float v = 0.f;
#pragma unroll
      for (int ww = 0; ww < 8; ++ww) v += red[ww * 256 + tid];
      *(u16*)(lds + L_P + (tid >> 7) * 272 + (tid & 127) * 2) = (u16)pk_bf16(v, v);
    }
    bar();
    f32x4 D[4];
    project(U, lds, g, c & 1, D);
    if (g == 0) {
      __builtin_amdgcn_raw_buffer_store_b128(u32x4{mark(D[0][0], old, 0), mark(D[1][0], old, 1), mark(D[2][0], old, 2), mark(D[3][0], old, 3)}, rws, c * 16, WS_Y + (h * 4 + 3) * 2048 + w * 256, AUX_SC1);
      __builtin_amdgcn_raw_buffer_store_b128(u32x4{mark(D[0][1], old, 4), mark(D[1][1], old, 5), mark(D[2][1], old, 6), mark(D[3][1], old, 7)}, rws, c * 16, WS_Y + (128 + h * 4 + 3) * 2048 + w * 256, AUX_SC1);
    }
  } else {
    const int b = rs & 1, sp = rs >> 1;
    u32x4 W[16], S0[8], S1[8];
#define LDC(st, i) ld128<AUX_NT>(mk_rsrc(Cc, (st) < 8 ? (16u << 20) : 0u), lane * 16 + ((i) & 3) * 1024, (b * 8192 + sp * 512 + ((st) & 7) * 64 + 8 * w + ((i) & 4)) * 1024)
#define LDS_(S, st) _Pragma("unroll") for (int i = 0; i < 8; ++i) S[i] = LDC(st, i);
#define STC(slot, S) { int ln = lane; asm volatile("" : "+v"(ln)); char* base = lds + L_RING + (slot) * 65536 + (ln >> 4) * 16384 + 8 * w * 256; \
      _Pragma("unroll") for (int i = 0; i < 8; ++i) *(u32x4*)(base + i * 256 + 16 * ((ln & 15) ^ swz16(8 * w + i))) = S[i]; }
    LD_EPOCH();
    const u32x4 fy = ld128<AUX_SC1_NT>(rws, fy_off, WS_Y);
    LD_GATE();
    LDS_(S0, 0);
    LDS_(S1, 1);
    SB();
    USE_EPOCH(); USE_GATE();
    fold = lsb4(fy);
    asm volatile("" : "+v"(fold), "+v"(gate));
    tagr = tag1;
    STC(0, S0);
    SB();
    LDS_(S0, 2);
    SB();
    STC(1, S1);
    SB();
    LDS_(S1, 3);
    PIN();
    if (w == 0) wait_flags32((const GAS u64*)(ws + WS_F1), tag0, lane);
    bar();
#pragma unroll
    for (int k = 0; k < 4; ++k) *(u32x4*)(lds + L_QA + (4 * w + k) * 1024 + 16 * (lane ^ swz16(4 * w + k))) = ld128<AUX_SC1>(rws, lane * 16, WS_QA + (b * 32 + 4 * w + k) * 1024);
    bar();
    const int kt = w & 3, hh = w >> 2;
#pragma unroll
    for (int s = 0; s < 16; ++s) W[s] = *(const u32x4*)(lds + L_QA + (16 * hh + c) * 1024 + 16 * ((4 * s + g) ^ swz16(c)));
#pragma unroll
    for (int s = 0; s < 16; ++s) asm volatile("" : "+v"(W[s]));
    bar();
    float mq = -INFINITY, lq = 0.f, mp = -INFINITY;
    f32x16 A0, A1;
#pragma unroll
    for (int i = 0; i < 16; ++i) { A0[i] = 0.f; A1[i] = 0.f; }
    float* mx = (float*)(lds + L_MX);
#define STAGE(slot, REFILL) { const char* tile = lds + L_RING + (slot) * 65536; \
      int ln = lane; asm volatile("" : "+v"(ln)); \
      const int g = ln >> 4, c = ln & 15, swz = swz16(c), h5 = ln >> 5, hq = 16 * hh + c, hp = ln & 31; \
      const char* rowp = tile + (16 * kt + c) * 256; \
      f32x4 X0 = {0.f, 0.f, 0.f, 0.f}, X1 = X0; \
      { u32x4 fa[4], fb[4]; \
        _Pragma("unroll") for (int j = 0; j < 4; ++j) fa[j] = *(const u32x4*)(rowp + 16 * ((4 * j + g) ^ swz)); \
        _Pragma("unroll") for (int j = 0; j < 4; ++j) fb[j] = *(const u32x4*)(rowp + 16384 + 16 * ((4 * j + g) ^ swz)); \
        SB(); \
        X0 = mfma16(fa[0], W[0], X0); X1 = mfma16(fa[1], W[1], X1); X0 = mfma16(fa[2], W[2], X0); X1 = mfma16(fa[3], W[3], X1); \
        SB(); \
        _Pragma("unroll") for (int j = 0; j < 4; ++j) fa[j] = *(const u32x4*)(rowp + 32768 + 16 * ((4 * j + g) ^ swz)); \
        SB(); \
        X0 = mfma16(fb[0], W[4], X0); X1 = mfma16(fb[1], W[5], X1); X0 = mfma16(fb[2], W[6], X0); X1 = mfma16(fb[3], W[7], X1); \
        SB(); \
        _Pragma("unroll") for (int j = 0; j < 4; ++j) fb[j] = *(const u32x4*)(rowp + 49152 + 16 * ((4 * j + g) ^ swz)); \
        SB(); \
        X0 = mfma16(fa[0], W[8], X0); X1 = mfma16(fa[1], W[9], X1); X0 = mfma16(fa[2], W[10], X0); X1 = mfma16(fa[3], W[11], X1); \
        X0 = mfma16(fb[0], W[12], X0); X1 = mfma16(fb[1], W[13], X1); X0 = mfma16(fb[2], W[14], X0); X1 = mfma16(fb[3], W[15], X1); } \
      float sc[4]; \
      _Pragma("unroll") for (int r = 0; r < 4; ++r) sc[r] = bfr(bfr(X0[r] + X1[r]) * SCALE); \
      const float tm = xrow_max(fmaxf(fmaxf(sc[0], sc[1]), fmaxf(sc[2], sc[3]))); \
      if (g == 0) mx[kt * 32 + hq] = tm; \
      bar(); \
      { const float q0 = mx[hq], q1 = mx[32 + hq], q2 = mx[64 + hq], q3 = mx[96 + hq], p0 = mx[hp], p1 = mx[32 + hp], p2 = mx[64 + hp], p3 = mx[96 + hp]; \
        const float mn = fmaxf(fmaxf(mq, fmaxf(q0, q1)), fmaxf(q2, q3)), ms = (mn == -INFINITY) ? 0.f : mn; \
        float p[4]; \
        _Pragma("unroll") for (int r = 0; r < 4; ++r) p[r] = ex2((sc[r] - ms) * LOG2E); \
        *(u32x2*)(lds + L_P + hq * 144 + (16 * kt + 4 * g) * 2) = u32x2{pk_bf16(p[0], p[1]), pk_bf16(p[2], p[3])}; \
        lq = lq * ex2((mq - ms) * LOG2E) + ((p[0] + p[1]) + (p[2] + p[3])); \
        mq = mn; \
        const float mn2 = fmaxf(fmaxf(mp, fmaxf(p0, p1)), fmaxf(p2, p3)), ms2 = (mn2 == -INFINITY) ? 0.f : mn2; \
        const float al = ex2((mp - ms2) * LOG2E); mp = mn2; A0 *= al; A1 *= al; } \
      SB(); REFILL; SB(); \
      { const int blk = (ln >> 4) & 1, q4 = (ln & 15) >> 2, p4 = ln & 3; \
        const char* trp = tile + (w >> 1) * 16384 + (8 * h5 + q4) * 256 + 8 * (p4 & 1); \
        const int ch0 = 8 * (w & 1) + 2 * blk + (p4 >> 1), z0 = swz16(8 * h5 + q4), z1 = swz16(8 * h5 + 4 + q4); \
        s16x4 f[4][2][2]; \
        _Pragma("unroll") for (int ks = 0; ks < 4; ++ks) _Pragma("unroll") for (int ct = 0; ct < 2; ++ct) { \
          f[ks][ct][0] = __builtin_amdgcn_ds_read_tr16_b64_v4i16((LAS s16x4*)(trp + ks * 4096 + 16 * ((ch0 + 4 * ct) ^ z0))); \
          f[ks][ct][1] = __builtin_amdgcn_ds_read_tr16_b64_v4i16((LAS s16x4*)(trp + ks * 4096 + 1024 + 16 * ((ch0 + 4 * ct) ^ z1))); } \
        SB(); \
        bar(); \
        u32x4 P[4]; \
        _Pragma("unroll") for (int ks = 0; ks < 4; ++ks) P[ks] = *(const u32x4*)(lds + L_P + hp * 144 + (16 * ks + 8 * h5) * 2); \
        SB(); \
        _Pragma("unroll") for (int ks = 0; ks < 4; ++ks) { \
          A0 = mfma32(__builtin_bit_cast(u32x4, __builtin_shufflevector(f[ks][0][0], f[ks][0][1], 0, 1, 2, 3, 4, 5, 6, 7)), P[ks], A0); \
          A1 = mfma32(__builtin_bit_cast(u32x4, __builtin_shufflevector(f[ks][1][0], f[ks][1][1], 0, 1, 2, 3, 4, 5, 6, 7)), P[ks], A1); } \
        SB(); } }
    STAGE(0, ;);
#pragma unroll 1
    for (int it = 0; it < 3; ++it) {
      STAGE(1, { STC(0, S0); SB(); LDS_(S0, 2 * it + 4); });
      STAGE(0, { STC(1, S1); SB(); LDS_(S1, 2 * it + 5); });
    }
    STAGE(1, ;);
    const int h5 = lane >> 5, hq = 16 * hh + c, hp = lane & 31;
    float* lx = (float*)(lds + L_ML);
    lq = xrow_sum(lq);
    if (g == 0) lx[kt * 32 + hq] = lq;
    bar();
    const float lt = (lx[hp] + lx[32 + hp]) + (lx[64 + hp] + lx[96 + hp]);
    const float inv = 1.f / lt;
    char* img = lds + L_RING + hp * 1040 + (64 * w + 4 * h5) * 2;
#pragma unroll
    for (int k = 0; k < 4; ++k) {
      *(u32x2*)(img + 16 * k) = u32x2{pk_bf16(A0[4 * k] * inv, A0[4 * k + 1] * inv), pk_bf16(A0[4 * k + 2] * inv, A0[4 * k + 3] * inv)};
      *(u32x2*)(img + 64 + 16 * k) = u32x2{pk_bf16(A1[4 * k] * inv, A1[4 * k + 1] * inv), pk_bf16(A1[4 * k + 2] * inv, A1[4 * k + 3] * inv)};
    }
    if (tid < 32) __builtin_amdgcn_raw_buffer_store_b64(u32x2{f2u(mp), f2u(lt)}, rws, hp * 128, WS_ML + b * 4096 + sp * 8, AUX_SC1);
    bar();
#pragma unroll
    for (int k = 0; k < 4; ++k)
      __builtin_amdgcn_raw_buffer_store_b128(*(const u32x4*)(lds + L_RING + (4 * w + k) * 1040 + lane * 16), rws, lane * 16, WS_PB + ((b * 32 + 4 * w + k) * 16 + sp) * 1024, AUX_SC1_NT);
    DRAIN();
    bar();
    if (tid == 0) __hip_atomic_store((GAS u64*)(ws + WS_F2) + rs, tag0, RLX_AGENT);
  }
  {
    const bool isb = (lane & 12) == 12;
    float* fr = (float*)(lds + L_RING);
    u32x4 y = {0u, 0u, 0u, 0u};
#define POLL_Y(SLP) _Pragma("unroll 1") for (unsigned spins = 0; spins < SPIN_CAP; ++spins) { \
      PIN(); y = ld128<AUX_SC1>(rws, fy_off, WS_Y); if ((lsb4(y) ^ fold) == 15u) break; __builtin_amdgcn_s_sleep(SLP); }
#define SUM_ROWS(v) { const float a0 = v[0] + dppf<ROR8>(v[0]), a1 = v[1] + dppf<ROR8>(v[1]), a2 = v[2] + dppf<ROR8>(v[2]), a3 = v[3] + dppf<ROR8>(v[3]); \
      const float b0 = (c & 8) ? a2 : a0, b1 = (c & 8) ? a3 : a1; \
      const float c0 = b0 + dppf<0x124>(b0), c1 = b1 + dppf<0x12C>(b1); \
      v[0] = xrow_sum((c & 4) ? c0 : c1); }
    if (isb) { POLL_Y(16); }
    {
      float v[4];
#pragma unroll
      for (int j = 0; j < 4; ++j) v[j] = u2f(y[j] & ~0xffu);
      SUM_ROWS(v);
      if (lane < 16) fr[128 + w * 16 + lane] = v[0];
      if (lane == 0) *(unsigned*)(lds + L_FIN + w * 4) = fold;
    }
    bar();
    float bpart = 0.f;
    if (w == 0) {
#pragma unroll
      for (int ww = 0; ww < 8; ++ww) bpart += fr[128 + ww * 16 + c];
      bpart = bfr(bfr(1.f - gate) * bfr(bpart));
      const int po = (rb * 128 + 16 * (lane & 7)) * 2048 + rs * 64;
      const unsigned po_old = *(const unsigned*)(lds + L_FIN + (lane & 7) * 4);
#pragma unroll 1
      for (unsigned spins = 0; spins < SPIN_CAP; ++spins) {
        PIN();
        if (__any((ld32<AUX_SC1>(rws, po, WS_Y) ^ po_old) & 1u)) break;
        __builtin_amdgcn_s_sleep(24);
      }
    }
    GAS u16* op = (GAS u16*)out + rb * 512 + rs * 16 + 4 * (c & 3) + (((c >> 2) & 2) | (((c >> 2) & 1) ^ 1));
    asm volatile("" : "+v"(bpart), "+v"(op));
    bar();
    y = u32x4{0u, 0u, 0u, 0xfeu};
    if (!isb) { POLL_Y(0); }
    const float lse = u2f((((y[0] >> 1) & 0x7fu) | (((y[1] >> 1) & 0x7fu) << 7) | (((y[2] >> 1) & 0x7fu) << 14) | (((y[3] >> 1) & 0x7fu) << 21)) << 4);
    float M = fmaxf(lse, dppf<ROR8>(lse)); M = fmaxf(M, dppf<ROR4>(M));
    const float e = isb ? 0.f : ex2(lse - M);
    float L = e + dppf<ROR8>(e); L += dppf<ROR4>(L);
    const float cf = e * __builtin_amdgcn_rcpf(L);
    float v[4];
#pragma unroll
    for (int j = 0; j < 4; ++j) v[j] = cf * u2f(y[j] & ~0xffu);
    SUM_ROWS(v);
    if (lane < 16) fr[w * 16 + lane] = v[0];
    bar();
    if (tid < 16) {
      const float a = ((fr[tid] + fr[16 + tid]) + (fr[32 + tid] + fr[48 + tid])) + ((fr[64 + tid] + fr[80 + tid]) + (fr[96 + tid] + fr[112 + tid]));
      const float o = bfr(gate * bfr(a)) + bpart;
      *op = (u16)pk_bf16(o, o);
    }
    if (rs == 0 && tid == 0) __hip_atomic_store((GAS u64*)(ws + WS_EPOCH + rb * 128), tagr, RLX_AGENT);
  }
    return;
  }
  {
    const int h = __builtin_amdgcn_readfirstlane(4 * x8 + idx / 6), b = __builtin_amdgcn_readfirstlane((idx % 6) / 3), sp = __builtin_amdgcn_readfirstlane(idx % 3);
    const int src = b * 128 + h * 4 + sp;
    const u32x4 sel = make_sel(c);
    const int tb = (b * 32 + h) * 512, t1 = tb + 512 * (sp + 1) / 3, t0 = tb + 512 * sp / 3 + w;
    const rsrc_t rk = mk_rsrc(Kc, t1 * 4096), rv = mk_rsrc(Vc, t1 * 4096), ro = mk_rsrc(wo, W_BYTES);
#define LDT(r, i_, j) ld128<AUX_NT>(r, lane * 16 + (j) * 1024, (t0 + 8 * (i_)) * 4096)
#define REQ(a, r, i_) { _Pragma("unroll") for (int j = 0; j < 4; ++j) a[j] = LDT(r, i_, j); SB(); }
#define REQ_REST(a, r) { _Pragma("unroll") for (int j = 0; j < 4; ++j) a[j] = ld128<AUX_NT>(r, lane * 16, (t0 - w + 168) * 4096 + (w + 8 * j) * 1024); SB(); }
#define ARRIVED(a) { _Pragma("unroll") for (int j = 0; j < 4; ++j) asm volatile("" : "+v"(a[j])); SB(); }
    u32x4 W[16], A[4], B[4], C[4], D4[4], E[4], F[4];
    const int wr = __builtin_amdgcn_readfirstlane((w + (idx % 6) * 4 / 3) & 7);
    unsigned x0 = x[64 * wr + lane], x1 = x[512 + 64 * wr + lane];
    LD_EPOCH();
    unsigned old = ld32<AUX_SC1_NT>(rws, lane * 4, WS_Y + src * 2048 + w * 256);
    const unsigned touch = ld32<AUX_NT>(ro, 0, h * 131072), touch2 = ld32<AUX_NT>(rv, 0, t0 * 4096);
    const unsigned touch3 = ld32<AUX_NT>(rk, 0, t0 * 4096);
    SB();
    {
      const rsrc_t rq = mk_rsrc(wq, W_BYTES);
#pragma unroll
      for (int i = 0; i < 16; ++i) W[i] = ld128<AUX_NT>(rq, g * (16 * 8192) + c * 16, (64 * wr + i) * 8192 + h * 256);
    }
    SB();
    asm volatile("" : "+v"(x0), "+v"(x1) : "v"(touch3));
    {
      f32x4 D = {0.f, 0.f, 0.f, 0.f};
      rowsum_refill<0>(W, x0 | (x1 << 16), sel, D, [&](auto T) {
        constexpr int j = T & 3;
        if constexpr (T < 4) A[j] = LDT(rk, 0, j); else if constexpr (T < 8) B[j] = LDT(rk, 1, j);
        else if constexpr (T < 12) C[j] = LDT(rk, 2, j); else D4[j] = LDT(rk, 3, j); });
#pragma unroll
      for (int r = 0; r < 4; ++r) red[(wr * 2 + (c >> 3)) * 128 + 32 * g + 8 * r + (c & 7)] = D[r];
    }
    SB();
    REQ(E, rk, 4); REQ_REST(F, rk);
    bar();
    u32x4 q;
    QREAD(q, b);
    float* scw = (float*)(lds + L_SC + w * 1536);
    float m = -INFINITY;
#define SCORE(a, i_, REST) { float xs[4]; _Pragma("unroll") for (int j = 0; j < 4; ++j) xs[j] = dot8(a[j], q); \
      float s = bfr(bfr(treduce<4>(xs, c)) * SCALE); \
      if (REST) s = (w + 8 * (c >> 2) < 4 * (t1 - t0 + w - 168)) ? s : -INFINITY; \
      m = fmaxf(m, s); scw[(i_) * 16 + (lane >> 2)] = s; asm volatile("" : "+v"(m)); SB(); }
    SCORE(A, 0, false); REQ(A, rk, 5);
    SCORE(B, 1, false);
    REQ(B, rk, 6);
    SCORE(C, 2, false); SCORE(D4, 3, false); SCORE(E, 4, false); SCORE(F, 21, true);
    USE_EPOCH();
    u64 tag = b ? tag1 : tag0;
    asm volatile("" : "+s"(tag));
    asm volatile("" : "+v"(old) : "v"(touch), "v"(touch2));
    SB();
    const unsigned img = (unsigned)(uintptr_t)(LAS char*)(lds + L_RING) + w * 16384;
    const int dvo = (lane >> 3) * 1024 + 16 * ((lane & 7) ^ ((lane >> 4 & 1) << 1));
#define DMA8(k_, vo) { unsigned keep; \
      asm volatile("s_mov_b32 %0, m0\n\ts_mov_b32 m0, %1\n\ts_nop 4\n\tbuffer_load_dwordx4 %2, %3, %4 offen nt lds\n\ts_mov_b32 m0, %0" : "=&s"(keep) \
                   : "s"(__builtin_amdgcn_readfirstlane(img + (k_) * 1024)), "v"(vo), "s"(ro), "s"(__builtin_amdgcn_readfirstlane((h * 128 + 8 * (k_)) * 1024 + w * 128)) : "memory"); SB(); }
#pragma unroll 1
    for (int i = 5; i < 21; i += 2) {
      const rsrc_t rn = mk_rsrc(i < 19 ? Kc : Vc, t1 * 4096);
      SCORE(A, i, false); REQ(A, rn, i < 19 ? i + 2 : 0); DMA8(i - 5, dvo);
      SCORE(B, i + 1, false); REQ(B, rn, i < 19 ? i + 3 : 1); DMA8(i - 4, dvo ^ 64);
    }
    float* ml = (float*)(lds + L_ML);
    m = xrow_max(row_max(m));
    float l = 0.f;
    {
      const float ms = (m == -INFINITY) ? 0.f : m;
      if (lane < 32) scw[352 + lane] = -INFINITY;
#pragma unroll
      for (int k = 0; k < 6; ++k) { const float p = ex2((scw[64 * k + lane] - ms) * LOG2E); l += p; scw[64 * k + lane] = u2f(hilo(p)); }
      l = xrow_sum(row_sum(l));
      if (lane == 0) { ml[w] = m; ml[8 + w] = l; }
    }
    f32x4 O = {0.f, 0.f, 0.f, 0.f};
#define PV(a, i_) { rowsum<4, 4>(a, f2u(scw[(i_) * 16 + (lane >> 2)]), sel, O); asm volatile("" : "+v"(O)); SB(); }
    PV(A, 0); REQ(A, rv, 2);
    asm volatile("s_waitcnt vmcnt(4)" ::: "memory");
    {
      const int q4 = c >> 2, p4 = lane & 3, phi = (q4 >> 1) | ((g & 1) << 1);
#pragma unroll
      for (int s = 0; s < 4; ++s)
#pragma unroll
        for (int ct = 0; ct < 4; ++ct) {
          const char* a = lds + L_RING + w * 16384 + (32 * s + 8 * g + q4) * 128 + 16 * ((p4 >> 1) | ((ct ^ phi) << 1)) + 8 * (p4 & 1);
          const s16x4 lo = __builtin_amdgcn_ds_read_tr16_b64_v4i16((LAS s16x4*)a), hi = __builtin_amdgcn_ds_read_tr16_b64_v4i16((LAS s16x4*)(a + 512));
          W[4 * s + ct] = __builtin_bit_cast(u32x4, __builtin_shufflevector(lo, hi, 0, 1, 2, 3, 4, 5, 6, 7));
        }
    }
#pragma unroll
    for (int i = 0; i < 16; ++i) asm volatile("" : "+v"(W[i]));
    PV(B, 1);
#pragma unroll 1
    for (int i = 2; i < 20; i += 2) {
      ARRIVED(A); REQ(B, rv, i + 1); PV(A, i);
      ARRIVED(B); REQ(A, rv, i + 2); PV(B, i + 1);
    }
    const GAS u64* go = (const GAS u64*)(ws + WS_GO) + h * 16 + b;
    u64 gov = __hip_atomic_load(go, RLX_AGENT);
    SB();
    REQ_REST(B, rv);
    bar();
    const float mj = ml[lane & 7], lj = ml[8 + (lane & 7)];
    float M = fmaxf(mj, dppf<XOR7>(mj)); M = fmaxf(M, dppf<XOR2>(M)); M = fmaxf(M, dppf<XOR1>(M));
    const float Ms = (M == -INFINITY) ? 0.f : M;
    float L = ex2((mj - Ms) * LOG2E) * lj; L += dppf<XOR7>(L); L += dppf<XOR2>(L); L += dppf<XOR1>(L);
    float fw = ex2((m - Ms) * LOG2E) * __builtin_amdgcn_rcpf(L);
    const unsigned code = f2u(Ms * LOG2E + __builtin_amdgcn_logf(L)) >> 4;
    unsigned low = (((code >> (7 * (lane & 3))) & 0x7fu) << 1) | ((old & 1u) ^ 1u);
    unsigned wa = L_P + (2 * w + (c >> 3)) * 272 + (32 * g + (c & 7)) * 2, sh = (c & 8) * 2;
    asm volatile("" : "+v"(fw), "+v"(low), "+v"(wa), "+v"(sh));
    SB();
    PV(A, 20); PV(B, 21);
#pragma unroll
    for (int r = 0; r < 4; ++r) *(u16*)(lds + wa + 16 * r) = (u16)(hilo((O[r] + dppf<ROR8>(O[r])) * fw) >> sh);
#pragma unroll 1
    for (unsigned spins = 0; uniform64(gov) != tag && spins < SPIN_CAP; ++spins) gov = __hip_atomic_load(go, RLX_AGENT);
    bar();
    f32x4 D[4];
    project(W, lds, g, c, D);
    float y4[4];
#pragma unroll
    for (int k = 0; k < 4; ++k) y4[k] = (D[k][0] + D[k][1]) + (D[k][2] + D[k][3]);
    const auto s0 = __builtin_amdgcn_permlane32_swap(f2u(y4[0]), f2u(y4[2]), false, false), s1 = __builtin_amdgcn_permlane32_swap(f2u(y4[1]), f2u(y4[3]), false, false);
    const auto s2 = __builtin_amdgcn_permlane16_swap(f2u(u2f(s0[0]) + u2f(s0[1])), f2u(u2f(s1[0]) + u2f(s1[1])), false, false);
    const unsigned yw = (f2u(u2f(s2[0]) + u2f(s2[1])) & ~0xffu) | low;
    const u32x4 yv = {yw, f2u(dppf<0x55>(u2f(yw))), f2u(dppf<0xAA>(u2f(yw))), f2u(dppf<0xFF>(u2f(yw)))};
    if ((lane & 3) == 0) __builtin_amdgcn_raw_buffer_store_b128(yv, rws, lane * 4, WS_Y + src * 2048 + w * 256, AUX_SC1);
  }
}

extern "C" void kernel_launch(void* const* d_in, const int* in_sizes, int n_in, void* d_out, int out_size, void* d_ws, size_t ws_size, hipStream_t stream) {
  fused_decode<<<256, 512, 0, stream>>>((const u16*)d_in[0], (const u16*)d_in[1], (const u16*)d_in[2], (const u16*)d_in[3], (const u16*)d_in[4],
                                        (const u16*)d_in[6], (const u16*)d_in[7], (const u16*)d_in[8], (const u16*)d_in[9], (const u16*)d_in[10],
                                        (const u16*)d_in[11], (u16*)d_out, (char*)d_ws);
}
```

```cpp
#include <hip/hip_runtime.h>
#include <stdint.h>
#include <type_traits>

typedef unsigned short u16;
typedef unsigned long long u64;
typedef unsigned u32x4 __attribute__((ext_vector_type(4)));
typedef unsigned u32x2 __attribute__((ext_vector_type(2)));
typedef float f32x4 __attribute__((ext_vector_type(4)));
typedef float f32x16 __attribute__((ext_vector_type(16)));
typedef __bf16 bf16x8 __attribute__((ext_vector_type(8)));
typedef __bf16 bf16x2 __attribute__((ext_vector_type(2)));
typedef short s16x4 __attribute__((ext_vector_type(4)));
typedef __amdgpu_buffer_rsrc_t rsrc_t;
#define GAS __attribute__((address_space(1)))
#define LAS __attribute__((address_space(3)))
#define DI __device__ __forceinline__
#define RLX_AGENT __ATOMIC_RELAXED, __HIP_MEMORY_SCOPE_AGENT

#define NL 4
#define OFFM 0
#define TL 0
#define STAG 0
#define NDMA 8
#define SSLEEP 24
#define PB_ST 18
#define PB_LD 18
#define BOOST 1
#define EBAR 0
#define HROT 0
#define HMAP 0
#define DEEP1 0
#define DEEP0 0
#define WROT 1
#define TROT 0
#define ILV 0
#define COOP 0
#define PRE4 2
#define M2 0
#define A_PRE 18
#define A_QA 16
#define A_ST 16
#define A_POLL 16
#define A_YST 16
#define WSLEEP 3
#define OUTSC1 0
#define BDELAY 0
#define NBOOST 1
#define TOUCH 0
#define DEEP 0
#define TORD 0
#define FSLEEP 0
#define STAMPS 0
constexpr int AUX_NT = 2, AUX_SC1 = 16;
constexpr float SCALE = 0.08838834764831845f;
constexpr float LOG2E = 1.4426950408889634f;

constexpr int WS_EPOCH = 0;
constexpr int WS_F1 = 1024;
constexpr int WS_F2 = 2048;
constexpr int WS_GO = 4096;
constexpr int WS_QA = 8192;
constexpr int WS_ML = WS_QA + 65536;
constexpr int WS_PB = WS_ML + 8192;
constexpr int WS_Y = WS_PB + 2 * 32 * 16 * 1024;
constexpr int WS_END = WS_Y + 2 * 128 * 2048;
constexpr int WS_STAMP = 4 << 20;

constexpr int L_RING = 0;
constexpr int L_RED = 131072;
constexpr int L_P = L_RED + 8192;
constexpr int L_MX = L_P + 32 * 144;
constexpr int L_ML = L_MX + 512;
constexpr int L_FIN = L_ML + 512;
constexpr int L_SC = 147456;
constexpr int L_QA = 131072;
constexpr int L_END = 163840;

DI rsrc_t mk_rsrc(const void* p, unsigned bytes) { return __builtin_amdgcn_make_buffer_rsrc((void*)p, 0, bytes, 0x00020000); }
template <int AUX> DI u32x4 ld128(rsrc_t r, int voff, int soff) { return __builtin_amdgcn_raw_buffer_load_b128(r, voff, soff, AUX); }
template <int AUX> DI u32x2 ld64(rsrc_t r, int voff, int soff) { return __builtin_amdgcn_raw_buffer_load_b64(r, voff, soff, AUX); }
template <int AUX> DI unsigned ld32(rsrc_t r, int voff, int soff) { return __builtin_amdgcn_raw_buffer_load_b32(r, voff, soff, AUX); }
DI float u2f(unsigned u) { return __builtin_bit_cast(float, u); }
DI unsigned f2u(float f) { return __builtin_bit_cast(unsigned, f); }
DI unsigned pk_bf16(float lo, float hi) { bf16x2 r; r[0] = (__bf16)lo; r[1] = (__bf16)hi; return __builtin_bit_cast(unsigned, r); }
DI float bfr(float x) { return u2f(pk_bf16(x, x) << 16); }
DI unsigned hilo(float x) { const float h = bfr(x); return pk_bf16(h, x - h); }
DI float ex2(float x) { return __builtin_amdgcn_exp2f(x); }
DI float dot2(unsigned a, unsigned b, float c) { return __builtin_amdgcn_fdot2_f32_bf16(__builtin_bit_cast(bf16x2, a), __builtin_bit_cast(bf16x2, b), c, false); }
DI float dot8(const u32x4& a, const u32x4& b) { return dot2(a[3], b[3], dot2(a[2], b[2], dot2(a[1], b[1], dot2(a[0], b[0], 0.f)))); }
template <int CTRL> DI float dppf(float x) { return u2f(__builtin_amdgcn_mov_dpp(f2u(x), CTRL, 0xf, 0xf, true)); }
template <int T> DI unsigned bcast16(unsigned v) { return __builtin_amdgcn_mov_dpp(v, 0x150 + T, 0xf, 0xf, true); }
constexpr int XOR1 = 0xB1, XOR2 = 0x4E, XOR7 = 0x141, ROR8 = 0x128, ROR4 = 0x124;
DI float xrow_max(float x) {
  auto s = __builtin_amdgcn_permlane16_swap(f2u(x), f2u(x), false, false); x = fmaxf(u2f(s[0]), u2f(s[1]));
  auto t = __builtin_amdgcn_permlane32_swap(f2u(x), f2u(x), false, false); return fmaxf(u2f(t[0]), u2f(t[1]));
}
DI float xrow_sum(float x) {
  auto s = __builtin_amdgcn_permlane16_swap(f2u(x), f2u(x), false, false); x = u2f(s[0]) + u2f(s[1]);
  auto t = __builtin_amdgcn_permlane32_swap(f2u(x), f2u(x), false, false); return u2f(t[0]) + u2f(t[1]);
}
DI float row_max(float x) { x = fmaxf(x, dppf<ROR8>(x)); x = fmaxf(x, dppf<ROR4>(x)); x = fmaxf(x, dppf<XOR2>(x)); return fmaxf(x, dppf<XOR1>(x)); }
DI float row_sum(float x) { x += dppf<ROR8>(x); x += dppf<ROR4>(x); x += dppf<XOR2>(x); return x + dppf<XOR1>(x); }
DI f32x4 mfma16(u32x4 a, u32x4 b, f32x4 c) { return __builtin_amdgcn_mfma_f32_16x16x32_bf16(__builtin_bit_cast(bf16x8, a), __builtin_bit_cast(bf16x8, b), c, 0, 0, 0); }
DI f32x16 mfma32(u32x4 a, u32x4 b, f32x16 c) { return __builtin_amdgcn_mfma_f32_32x32x16_bf16(__builtin_bit_cast(bf16x8, a), __builtin_bit_cast(bf16x8, b), c, 0, 0, 0); }
DI u64 uniform64(u64 v) { return ((u64)(unsigned)__builtin_amdgcn_readfirstlane((unsigned)(v >> 32)) << 32) | (unsigned)__builtin_amdgcn_readfirstlane((unsigned)v); }
DI u64 next_tag(u64 e) { return e * 0x9E3779B97F4A7C15ull + 0x632BE59BD9B4E019ull; }
DI void bar() { __syncthreads(); }
#define DRAIN() asm volatile("s_waitcnt vmcnt(0)" ::: "memory")
#define PIN() asm volatile("" ::: "memory")
#define SB() __builtin_amdgcn_sched_barrier(0)
constexpr unsigned SPIN_CAP = 1u << 22;

template <int N, int STEP, int T = 0> DI void rowsum(const u32x4 (&W)[N], unsigned pair, const u32x4& sel, f32x4& D) {
  if constexpr (T < N) {
    const unsigned s = bcast16<T * STEP>(pair);
    const u32x4 B = {__builtin_amdgcn_perm(s, s, sel[0]), __builtin_amdgcn_perm(s, s, sel[1]), __builtin_amdgcn_perm(s, s, sel[2]), __builtin_amdgcn_perm(s, s, sel[3])};
    D = mfma16(W[T], B, D);
    rowsum<N, STEP, T + 1>(W, pair, sel, D);
  }
}
template <int N, int I = 0, class F> DI void static_for(F&& f) { if constexpr (I < N) { f(std::integral_constant<int, I>{}); static_for<N, I + 1>(f); } }
template <int T, class F> DI void rowsum_refill(const u32x4 (&W)[16], unsigned pair, const u32x4& sel, f32x4& D, F&& issue) {
  if constexpr (T < 16) {
    const unsigned s = bcast16<T>(pair);
    const u32x4 B = {__builtin_amdgcn_perm(s, s, sel[0]), __builtin_amdgcn_perm(s, s, sel[1]), __builtin_amdgcn_perm(s, s, sel[2]), __builtin_amdgcn_perm(s, s, sel[3])};
    D = mfma16(W[T], B, D);
    asm volatile("" : "+v"(D));
    __builtin_amdgcn_sched_barrier(0);
    issue(std::integral_constant<int, T>{});
    __builtin_amdgcn_sched_barrier(0);
    rowsum_refill<T + 1>(W, pair, sel, D, issue);
  }
}
DI int swz16(int row) { const int u = (row >> 2) & 3; return ((row & 3) << 2) | (((u ^ (u >> 1)) & 1) << 1) | (u >> 1); }
DI u32x4 make_sel(int c) {
  const int j = c & 7; const unsigned b = (c & 8) ? 0x0302u : 0x0100u;
  const unsigned v = (j & 1) ? ((b << 16) | 0x0c0cu) : (0x0c0c0000u | b);
  u32x4 s;
#pragma unroll
  for (int w = 0; w < 4; ++w) s[w] = (w == (j >> 1)) ? v : 0x0c0c0c0cu;
  return s;
}
template <int N> DI float treduce(float (&x)[N], int c) {
#define TR_STEP(CNT, CTRL, BIT) _Pragma("unroll") for (int i = 0; i < (CNT); ++i) { const float a = x[i] + dppf<CTRL>(x[i]), b = x[i + (CNT)] + dppf<CTRL>(x[i + (CNT)]); x[i] = (c & (BIT)) ? b : a; }
  TR_STEP(N / 2, ROR8, 8)
  TR_STEP(N / 4, XOR7, 4)
  if constexpr (N >= 8) { TR_STEP(N / 8, XOR2, 2) } else { x[0] += dppf<XOR2>(x[0]); }
  if constexpr (N >= 16) { TR_STEP(N / 16, XOR1, 1) } else { x[0] += dppf<XOR1>(x[0]); }
#undef TR_STEP
  return x[0];
}
DI void wait_flags32(const GAS u64* f, u64 tag, int lane) {
#pragma unroll 1
  for (unsigned spins = 0; spins < SPIN_CAP; ++spins) {
    if (__all(__hip_atomic_load(f + (lane & 31), RLX_AGENT) == tag)) break;
    __builtin_amdgcn_s_sleep(WSLEEP);
  }
  __builtin_amdgcn_fence(__ATOMIC_ACQUIRE, "wavefront");
}
DI void transpose_w(const u32x2 (&R)[32], u32x4 (&F)[16]) {
#pragma unroll
  for (int s = 0; s < 4; ++s)
#pragma unroll
    for (int t = 0; t < 4; ++t)
#pragma unroll
      for (int jj = 0; jj < 4; ++jj) F[4 * s + t][jj] = __builtin_amdgcn_perm(R[8 * s + 2 * jj + 1][t >> 1], R[8 * s + 2 * jj][t >> 1], (t & 1) ? 0x07060302u : 0x05040100u);
}
DI void project(const u32x4 (&F)[16], const char* lds, int g, int c, f32x4 (&D)[4]) {
  u32x4 A[4];
#pragma unroll
  for (int s = 0; s < 4; ++s) A[s] = *(const u32x4*)(lds + L_P + c * 272 + 64 * s + 16 * g);
#pragma unroll
  for (int t = 0; t < 4; ++t) D[t] = f32x4{0.f, 0.f, 0.f, 0.f};
#pragma unroll
  for (int s = 0; s < 4; ++s)
#pragma unroll
    for (int t = 0; t < 4; ++t) D[t] = mfma16(A[s], F[4 * s + t], D[t]);
}
DI unsigned lsb4(const u32x4& v) { return (v[0] & 1u) | ((v[1] & 1u) << 1) | ((v[2] & 1u) << 2) | ((v[3] & 1u) << 3); }
DI unsigned mark(float v, unsigned old, int k) { return (f2u(v) & ~1u) | (((old >> k) & 1u) ^ 1u); }

#define STAMP(k) do {} while (0)

__global__ void __launch_bounds__(512, 2)
fused_decode(const u16* __restrict__ x, const u16* __restrict__ Kc, const u16* __restrict__ Vc, const u16* __restrict__ wq, const u16* __restrict__ wo,
             const u16* __restrict__ Cc, const u16* __restrict__ w_q, const u16* __restrict__ w_uk, const u16* __restrict__ w_uv, const u16* __restrict__ w_o,
             const u16* __restrict__ gw, u16* __restrict__ out, char* ws) {
  __shared__ __attribute__((aligned(16))) char lds[L_END];
  const int bid = blockIdx.x, tid = threadIdx.x, lane = tid & 63, w = __builtin_amdgcn_readfirstlane(tid >> 6), g = lane >> 4, c = lane & 15;
  const int x8 = bid & 7, idx = bid >> 3;
  STAMP(0);
  const u64 t_start = (OFFM & (1024 | 2048)) ? __builtin_amdgcn_s_memrealtime() : 0;
  asm volatile("" :: "s"(x), "s"(Kc), "s"(Vc), "s"(wq), "s"(wo), "s"(ws));
  const rsrc_t rws = mk_rsrc(ws, WS_END);
  float* red = (float*)(lds + L_RED);
#define LD_EPOCH() u64 e0 = __hip_atomic_load((GAS u64*)(ws + WS_EPOCH), RLX_AGENT), e1 = __hip_atomic_load((GAS u64*)(ws + WS_EPOCH + 128), RLX_AGENT);
#define USE_EPOCH() asm volatile("" : "+v"(e0), "+v"(e1)); const u64 tag0 = next_tag(uniform64(e0)), tag1 = next_tag(uniform64(e1));
#define LD_ROWS(W, ptr, OFF) { const rsrc_t rq = mk_rsrc(ptr, (OFF) ? 0 : 512 * 4096 * 2); \
    _Pragma("unroll") for (int t = 0; t < 16; ++t) W[t] = ld128<AUX_NT>(rq, g * (16 * 8192) + c * 16, (64 * w + t) * 8192 + h * 256); }
#define LD_OUTW(R, ptr, OFF) { const rsrc_t ro = mk_rsrc(ptr, (OFF) ? 0 : 4096 * 512 * 2); \
    _Pragma("unroll") for (int i = 0; i < 32; ++i) R[i] = ld64<AUX_NT>(ro, g * 8192 + c * 8 + (i & 3) * 1024, (h * 128 + 32 * (i >> 3) + (i & 4)) * 1024 + w * 128); }
#define QPROJ() { asm volatile("" : "+v"(x0), "+v"(x1)); f32x4 D = {0.f, 0.f, 0.f, 0.f}; rowsum<16, 1>(W, x0 | (x1 << 16), sel, D); \
    _Pragma("unroll") for (int r = 0; r < 4; ++r) red[(w * 2 + (c >> 3)) * 128 + 32 * g + 8 * r + (c & 7)] = D[r]; }
#define QREAD(q, bb) { f32x4 a0 = {0.f, 0.f, 0.f, 0.f}, a1 = a0; \
    _Pragma("unroll") for (int ww = 0; ww < 8; ++ww) { const f32x4* p = (const f32x4*)(red + (ww * 2 + (bb)) * 128 + 8 * c); a0 += p[0]; a1 += p[1]; } \
    q = u32x4{pk_bf16(a0[0], a0[1]), pk_bf16(a0[2], a0[3]), pk_bf16(a1[0], a1[1]), pk_bf16(a1[2], a1[3])}; }

  if (idx >= 24) {
  if (OFFM & 256) { STAMP(1); STAMP(2); STAMP(3); STAMP(4); STAMP(5); STAMP(6); STAMP(7); return; }
  const int rb = idx >= 28, rs = __builtin_amdgcn_readfirstlane(4 * x8 + (idx & 3));
  const int fy_off = (rb * 128 + (tid >> 2)) * 2048 + rs * 64 + (tid & 3) * 16;
  unsigned fold; u64 tagr;
  float gate;
#define LD_GATE() const u32x4 xv = *(const u32x4*)(x + rb * 512 + lane * 8), gv = *(const u32x4*)(gw + lane * 8);
#define USE_GATE() { float s = 0.f; \
    _Pragma("unroll") for (int j = 0; j < 4; ++j) s += bfr(u2f(xv[j] << 16) * u2f(gv[j] << 16)) + bfr(u2f(xv[j] & 0xffff0000u) * u2f(gv[j] & 0xffff0000u)); \
    s = bfr(xrow_sum(row_sum(s))); gate = bfr(1.f / (1.f + __expf(-s))); }

  if (idx < 28) {
    const int h = rs;
    const u32x4 sel = make_sel(c);
    u32x4 W[16], U[16];
    unsigned x0 = x[tid], x1 = x[512 + tid];
    unsigned tch = 0;
    if (TOUCH) tch = ld32<AUX_NT>(mk_rsrc(w_uk, 4 << 20), (lane & 1) << 21, h * 256) ^ ld32<AUX_NT>(mk_rsrc(w_uv, 4 << 20), (lane & 1) << 21, h * 256) ^ ld32<AUX_NT>(mk_rsrc(w_o, 4 << 20), 0, h * 131072);
    LD_ROWS(W, w_q, OFFM & 1);
    SB();
    {
      asm volatile("" : "+v"(x0), "+v"(x1) : "v"(tch));
      const unsigned xp = x0 | (x1 << 16);
      const rsrc_t ru = mk_rsrc(w_uk, (OFFM & 2) ? 0 : 512 * 4096 * 2);
      f32x4 D = {0.f, 0.f, 0.f, 0.f};
      rowsum_refill<0>(W, xp, sel, D, [&](auto T) { U[T] = ld128<AUX_NT>(ru, g * (16 * 8192) + c * 16, (64 * w + T) * 8192 + h * 256); });
#pragma unroll
      for (int r = 0; r < 4; ++r) red[(w * 2 + (c >> 3)) * 128 + 32 * g + 8 * r + (c & 7)] = D[r];
    }
    LD_EPOCH();
    const u32x4 fy = ld128<A_PRE>(rws, fy_off, WS_Y);
    const u32x4 oy0 = ld128<A_PRE>(rws, c * 16, WS_Y + (h * 4 + 3) * 2048 + w * 256), oy1 = ld128<A_PRE>(rws, c * 16, WS_Y + (128 + h * 4 + 3) * 2048 + w * 256);
    LD_GATE();
    SB();
    bar();
#pragma unroll
    for (int bb = 0; bb < 2; ++bb) {
      u32x4 q; QREAD(q, bb);
      float xs[16];
#pragma unroll
      for (int t = 0; t < 16; ++t) xs[t] = dot8(U[t], q);
      const float qa = treduce<16>(xs, c);
      *(u16*)(lds + L_RING + bb * 1024 + tid * 2) = (u16)pk_bf16(qa, qa);
    }
    SB();
    USE_EPOCH(); USE_GATE();
    fold = lsb4(fy);
    unsigned old = lsb4(oy0) | (lsb4(oy1) << 4);
    asm volatile("" : "+v"(fold), "+v"(gate), "+v"(old));
    tagr = tag0;
    DRAIN();
    bar();
    if (w == 0) {
      __builtin_amdgcn_raw_buffer_store_b128(*(const u32x4*)(lds + L_RING + lane * 16), rws, lane * 16, WS_QA + h * 1024, A_ST);
      __builtin_amdgcn_raw_buffer_store_b128(*(const u32x4*)(lds + L_RING + 1024 + lane * 16), rws, lane * 16, WS_QA + (32 + h) * 1024, A_ST);
      DRAIN();
      if (lane == 0) __hip_atomic_store((GAS u64*)(ws + WS_F1) + h, tag0, RLX_AGENT);
    }
    STAMP(1);
    {
      u32x2 R[32];
      LD_OUTW(R, w_o, OFFM & 16);
      LD_ROWS(W, w_uv, OFFM & 8);
      SB();
      transpose_w(R, U);
    }
#pragma unroll
    for (int i = 0; i < 16; ++i) asm volatile("" : "+v"(W[i]), "+v"(U[i]));
    STAMP(2);
    if (w == 0) {
      if (OFFM & 2048) { while (__builtin_amdgcn_s_memrealtime() - t_start < 3900) __builtin_amdgcn_s_sleep(8); }
      wait_flags32((const GAS u64*)(ws + WS_F2), tag0, lane);
      if (lane < 2) __hip_atomic_store((GAS u64*)(ws + WS_GO) + h * 16 + lane, lane ? tag1 : tag0, RLX_AGENT);
    }
    bar();
    STAMP(3);
    u32x4 T[4];
#pragma unroll
    for (int k = 0; k < 4; ++k) T[k] = ld128<PB_LD>(mk_rsrc(ws, (OFFM & 32) ? 0 : WS_END), lane * 16, WS_PB + (((k >> 1) * 32 + h) * 16 + 2 * w + (k & 1)) * 1024);
    const u32x2 mlw = ld64<PB_LD>(rws, (g & 1) * 4096 + c * 8, WS_ML + h * 128);
    const float ms = u2f(mlw[0]), M0 = row_max(ms), M = (M0 == -INFINITY) ? 0.f : M0;
    const float wt = ex2((ms - M) * LOG2E) * u2f(mlw[1]), cf = wt / row_sum(wt);
    float* cr = (float*)(lds + L_RING);
#pragma unroll
    for (int bb = 0; bb < 2; ++bb) {
      const float f0 = u2f(__builtin_amdgcn_readlane(f2u(cf), 16 * bb + 2 * w)), f1 = u2f(__builtin_amdgcn_readlane(f2u(cf), 16 * bb + 2 * w + 1));
      float a[8];
#pragma unroll
      for (int j = 0; j < 4; ++j) {
        a[2 * j] = f0 * u2f(T[2 * bb][j] << 16) + f1 * u2f(T[2 * bb + 1][j] << 16);
        a[2 * j + 1] = f0 * u2f(T[2 * bb][j] & 0xffff0000u) + f1 * u2f(T[2 * bb + 1][j] & 0xffff0000u);
      }
      *(f32x4*)(cr + (w * 2 + bb) * 512 + lane * 8) = f32x4{a[0], a[1], a[2], a[3]};
      *(f32x4*)(cr + (w * 2 + bb) * 512 + lane * 8 + 4) = f32x4{a[4], a[5], a[6], a[7]};
    }
    bar();
    {
      float c0 = 0.f, c1 = 0.f;
#pragma unroll
      for (int ww = 0; ww < 8; ++ww) { c0 += cr[ww * 1024 + tid]; c1 += cr[ww * 1024 + 512 + tid]; }
      const unsigned h0 = hilo(c0), h1 = hilo(c1);
      f32x4 D = {0.f, 0.f, 0.f, 0.f}, D2 = D;
      rowsum<16, 1>(W, __builtin_amdgcn_perm(h1, h0, 0x05040100u), sel, D);
      rowsum<16, 1>(W, __builtin_amdgcn_perm(h1, h0, 0x07060302u), sel, D2);
      D += D2;
#pragma unroll
      for (int r = 0; r < 4; ++r) red[(w * 2 + (c >> 3)) * 128 + 32 * g + 8 * r + (c & 7)] = D[r];
    }
    bar();
    if (tid < 256) {
      float v = 0.f;
#pragma unroll
      for (int ww = 0; ww < 8; ++ww) v += red[ww * 256 + tid];
      *(u16*)(lds + L_P + (tid >> 7) * 272 + (tid & 127) * 2) = (u16)pk_bf16(v, v);
    }
    bar();
    f32x4 D[4];
    project(U, lds, g, c & 1, D);
    if (g == 0) {
      __builtin_amdgcn_raw_buffer_store_b128(u32x4{mark(D[0][0], old, 0), mark(D[1][0], old, 1), mark(D[2][0], old, 2), mark(D[3][0], old, 3)}, rws, c * 16, WS_Y + (h * 4 + 3) * 2048 + w * 256, A_ST);
      __builtin_amdgcn_raw_buffer_store_b128(u32x4{mark(D[0][1], old, 4), mark(D[1][1], old, 5), mark(D[2][1], old, 6), mark(D[3][1], old, 7)}, rws, c * 16, WS_Y + (128 + h * 4 + 3) * 2048 + w * 256, A_ST);
    }
    STAMP(4);
  } else {
    const int b = rs & 1, sp = rs >> 1;
    u32x4 W[16], S0[8], S1[8];
#define LDC(st, i) ld128<AUX_NT>(mk_rsrc(Cc, ((st) < 8 && !(OFFM & 4)) ? (16u << 20) : 0u), lane * 16 + ((i) & 3) * 1024, (b * 8192 + sp * 512 + ((st) & 7) * 64 + 8 * w + ((i) & 4)) * 1024)
#define LDS_(S, st) _Pragma("unroll") for (int i = 0; i < 8; ++i) S[i] = LDC(st, i);
#define STC(slot, S) { int ln = lane; asm volatile("" : "+v"(ln)); char* base = lds + L_RING + (slot) * 65536 + (ln >> 4) * 16384 + 8 * w * 256; \
      _Pragma("unroll") for (int i = 0; i < 8; ++i) *(u32x4*)(base + i * 256 + 16 * ((ln & 15) ^ swz16(8 * w + i))) = S[i]; }
    LD_EPOCH();
    const u32x4 fy = ld128<A_PRE>(rws, fy_off, WS_Y);
    LD_GATE();
    LDS_(S0, 0);
    LDS_(S1, 1);
    SB();
    USE_EPOCH(); USE_GATE();
    fold = lsb4(fy);
    asm volatile("" : "+v"(fold), "+v"(gate));
    tagr = tag1;
    STC(0, S0);
    SB();
    LDS_(S0, 2);
    SB();
    STC(1, S1);
    SB();
    LDS_(S1, 3);
    PIN();
    STAMP(1);
    if (w == 0) wait_flags32((const GAS u64*)(ws + WS_F1), tag0, lane);
    bar();
#pragma unroll
    for (int k = 0; k < 4; ++k) *(u32x4*)(lds + L_QA + (4 * w + k) * 1024 + 16 * (lane ^ swz16(4 * w + k))) = ld128<A_QA>(rws, lane * 16, WS_QA + (b * 32 + 4 * w + k) * 1024);
    bar();
    const int kt = w & 3, hh = w >> 2;
#pragma unroll
    for (int s = 0; s < 16; ++s) W[s] = *(const u32x4*)(lds + L_QA + (16 * hh + c) * 1024 + 16 * ((4 * s + g) ^ swz16(c)));
#pragma unroll
    for (int s = 0; s < 16; ++s) asm volatile("" : "+v"(W[s]));
    bar();
    STAMP(2);
    float mq = -INFINITY, lq = 0.f, mp = -INFINITY;
    f32x16 A0, A1;
#pragma unroll
    for (int i = 0; i < 16; ++i) { A0[i] = 0.f; A1[i] = 0.f; }
    float* mx = (float*)(lds + L_MX);
#define STAGE(slot, REFILL) { const char* tile = lds + L_RING + (slot) * 65536; \
      int ln = lane; asm volatile("" : "+v"(ln)); \
      const int g = ln >> 4, c = ln & 15, swz = swz16(c), h5 = ln >> 5, hq = 16 * hh + c, hp = ln & 31; \
      const char* rowp = tile + (16 * kt + c) * 256; \
      f32x4 X0 = {0.f, 0.f, 0.f, 0.f}, X1 = X0; \
      { u32x4 fa[4], fb[4]; \
        _Pragma("unroll") for (int j = 0; j < 4; ++j) fa[j] = *(const u32x4*)(rowp + 16 * ((4 * j + g) ^ swz)); \
        _Pragma("unroll") for (int j = 0; j < 4; ++j) fb[j] = *(const u32x4*)(rowp + 16384 + 16 * ((4 * j + g) ^ swz)); \
        SB(); \
        X0 = mfma16(fa[0], W[0], X0); X1 = mfma16(fa[1], W[1], X1); X0 = mfma16(fa[2], W[2], X0); X1 = mfma16(fa[3], W[3], X1); \
        SB(); \
        _Pragma("unroll") for (int j = 0; j < 4; ++j) fa[j] = *(const u32x4*)(rowp + 32768 + 16 * ((4 * j + g) ^ swz)); \
        SB(); \
        X0 = mfma16(fb[0], W[4], X0); X1 = mfma16(fb[1], W[5], X1); X0 = mfma16(fb[2], W[6], X0); X1 = mfma16(fb[3], W[7], X1); \
        SB(); \
        _Pragma("unroll") for (int j = 0; j < 4; ++j) fb[j] = *(const u32x4*)(rowp + 49152 + 16 * ((4 * j + g) ^ swz)); \
        SB(); \
        X0 = mfma16(fa[0], W[8], X0); X1 = mfma16(fa[1], W[9], X1); X0 = mfma16(fa[2], W[10], X0); X1 = mfma16(fa[3], W[11], X1); \
        X0 = mfma16(fb[0], W[12], X0); X1 = mfma16(fb[1], W[13], X1); X0 = mfma16(fb[2], W[14], X0); X1 = mfma16(fb[3], W[15], X1); } \
      float sc[4]; \
      _Pragma("unroll") for (int r = 0; r < 4; ++r) sc[r] = bfr(bfr(X0[r] + X1[r]) * SCALE); \
      const float tm = xrow_max(fmaxf(fmaxf(sc[0], sc[1]), fmaxf(sc[2], sc[3]))); \
      if (g == 0) mx[kt * 32 + hq] = tm; \
      bar(); \
      { const float q0 = mx[hq], q1 = mx[32 + hq], q2 = mx[64 + hq], q3 = mx[96 + hq], p0 = mx[hp], p1 = mx[32 + hp], p2 = mx[64 + hp], p3 = mx[96 + hp]; \
        const float mn = fmaxf(fmaxf(mq, fmaxf(q0, q1)), fmaxf(q2, q3)), ms = (mn == -INFINITY) ? 0.f : mn; \
        float p[4]; \
        _Pragma("unroll") for (int r = 0; r < 4; ++r) p[r] = ex2((sc[r] - ms) * LOG2E); \
        *(u32x2*)(lds + L_P + hq * 144 + (16 * kt + 4 * g) * 2) = u32x2{pk_bf16(p[0], p[1]), pk_bf16(p[2], p[3])}; \
        lq = lq * ex2((mq - ms) * LOG2E) + ((p[0] + p[1]) + (p[2] + p[3])); \
        mq = mn; \
        const float mn2 = fmaxf(fmaxf(mp, fmaxf(p0, p1)), fmaxf(p2, p3)), ms2 = (mn2 == -INFINITY) ? 0.f : mn2; \
        const float al = ex2((mp - ms2) * LOG2E); mp = mn2; A0 *= al; A1 *= al; } \
      SB(); REFILL; SB(); \
      { const int blk = (ln >> 4) & 1, q4 = (ln & 15) >> 2, p4 = ln & 3; \
        const char* trp = tile + (w >> 1) * 16384 + (8 * h5 + q4) * 256 + 8 * (p4 & 1); \
        const int ch0 = 8 * (w & 1) + 2 * blk + (p4 >> 1), z0 = swz16(8 * h5 + q4), z1 = swz16(8 * h5 + 4 + q4); \
        s16x4 f[4][2][2]; \
        _Pragma("unroll") for (int ks = 0; ks < 4; ++ks) _Pragma("unroll") for (int ct = 0; ct < 2; ++ct) { \
          f[ks][ct][0] = __builtin_amdgcn_ds_read_tr16_b64_v4i16((LAS s16x4*)(trp + ks * 4096 + 16 * ((ch0 + 4 * ct) ^ z0))); \
          f[ks][ct][1] = __builtin_amdgcn_ds_read_tr16_b64_v4i16((LAS s16x4*)(trp + ks * 4096 + 1024 + 16 * ((ch0 + 4 * ct) ^ z1))); } \
        SB(); \
        bar(); \
        u32x4 P[4]; \
        _Pragma("unroll") for (int ks = 0; ks < 4; ++ks) P[ks] = *(const u32x4*)(lds + L_P + hp * 144 + (16 * ks + 8 * h5) * 2); \
        SB(); \
        _Pragma("unroll") for (int ks = 0; ks < 4; ++ks) { \
          A0 = mfma32(__builtin_bit_cast(u32x4, __builtin_shufflevector(f[ks][0][0], f[ks][0][1], 0, 1, 2, 3, 4, 5, 6, 7)), P[ks], A0); \
          A1 = mfma32(__builtin_bit_cast(u32x4, __builtin_shufflevector(f[ks][1][0], f[ks][1][1], 0, 1, 2, 3, 4, 5, 6, 7)), P[ks], A1); } \
        SB(); } }
    STAGE(0, ;);
    STAMP(8);
#pragma unroll 1
    for (int it = 0; it < 3; ++it) {
      STAGE(1, { STC(0, S0); SB(); LDS_(S0, 2 * it + 4); });
      STAGE(0, { STC(1, S1); SB(); LDS_(S1, 2 * it + 5); });
      STAMP(10 + it);
    }
    STAGE(1, ;);
    STAMP(13);
    const int h5 = lane >> 5, hq = 16 * hh + c, hp = lane & 31;
    float* lx = (float*)(lds + L_ML);
    lq = xrow_sum(lq);
    if (g == 0) lx[kt * 32 + hq] = lq;
    bar();
    const float lt = (lx[hp] + lx[32 + hp]) + (lx[64 + hp] + lx[96 + hp]);
    const float inv = 1.f / lt;
    char* img = lds + L_RING + hp * 1040 + (64 * w + 4 * h5) * 2;
#pragma unroll
    for (int k = 0; k < 4; ++k) {
      *(u32x2*)(img + 16 * k) = u32x2{pk_bf16(A0[4 * k] * inv, A0[4 * k + 1] * inv), pk_bf16(A0[4 * k + 2] * inv, A0[4 * k + 3] * inv)};
      *(u32x2*)(img + 64 + 16 * k) = u32x2{pk_bf16(A1[4 * k] * inv, A1[4 * k + 1] * inv), pk_bf16(A1[4 * k + 2] * inv, A1[4 * k + 3] * inv)};
    }
    if (tid < 32) __builtin_amdgcn_raw_buffer_store_b64(u32x2{f2u(mp), f2u(lt)}, mk_rsrc(ws, (OFFM & 512) ? 0 : WS_END), hp * 128, WS_ML + b * 4096 + sp * 8, AUX_SC1);
    bar();
#pragma unroll
    for (int k = 0; k < 4; ++k)
      __builtin_amdgcn_raw_buffer_store_b128(*(const u32x4*)(lds + L_RING + (4 * w + k) * 1040 + lane * 16), mk_rsrc(ws, (OFFM & 32) ? 0 : WS_END), lane * 16, WS_PB + ((b * 32 + 4 * w + k) * 16 + sp) * 1024, PB_ST);
    DRAIN();
    bar();
    if (tid == 0) __hip_atomic_store((GAS u64*)(ws + WS_F2) + rs, tag0, RLX_AGENT);
    STAMP(3); STAMP(4);
  }
  STAMP(5);
  {
    const bool isb = (lane & 12) == 12;
    float* fr = (float*)(lds + L_RING);
    u32x4 y = {0u, 0u, 0u, 0u};
#define POLL_Y(SLP) _Pragma("unroll 1") for (unsigned spins = 0; spins < SPIN_CAP; ++spins) { \
      y = ld128<A_POLL>(rws, fy_off, WS_Y); if ((lsb4(y) ^ fold) == 15u) break; __builtin_amdgcn_s_sleep(SLP); }
#define SUM_ROWS(v) { const float a0 = v[0] + dppf<ROR8>(v[0]), a1 = v[1] + dppf<ROR8>(v[1]), a2 = v[2] + dppf<ROR8>(v[2]), a3 = v[3] + dppf<ROR8>(v[3]); \
      const float b0 = (c & 8) ? a2 : a0, b1 = (c & 8) ? a3 : a1; \
      const float c0 = b0 + dppf<0x124>(b0), c1 = b1 + dppf<0x12C>(b1); \
      v[0] = xrow_sum((c & 4) ? c0 : c1); }
    if (isb) { POLL_Y(16); }
    {
      float v[4];
#pragma unroll
      for (int j = 0; j < 4; ++j) v[j] = u2f(y[j] & ~0xffu);
      SUM_ROWS(v);
      if (lane < 16) fr[128 + w * 16 + lane] = v[0];
      if (lane == 0) *(unsigned*)(lds + L_FIN + w * 4) = fold;
    }
    bar();
    float bpart = 0.f;
    if (w == 0) {
#pragma unroll
      for (int ww = 0; ww < 8; ++ww) bpart += fr[128 + ww * 16 + c];
      bpart = bfr(bfr(1.f - gate) * bfr(bpart));
      const int po = (rb * 128 + 16 * (lane & 7)) * 2048 + rs * 64;
      const unsigned po_old = *(const unsigned*)(lds + L_FIN + (lane & 7) * 4);
#pragma unroll 1
      for (unsigned spins = 0; spins < SPIN_CAP; ++spins) {
        if (__any((ld32<A_POLL>(rws, po, WS_Y) ^ po_old) & 1u)) break;
        __builtin_amdgcn_s_sleep(SSLEEP);
      }
    }
    GAS u16* op = (GAS u16*)out + rb * 512 + rs * 16 + 4 * (c & 3) + (((c >> 2) & 2) | (((c >> 2) & 1) ^ 1));
    asm volatile("" : "+v"(bpart), "+v"(op));
    bar();
    y = u32x4{0u, 0u, 0u, 0xfeu};
    if (!isb) { POLL_Y(FSLEEP); }
    STAMP(6);
    const float lse = u2f((((y[0] >> 1) & 0x7fu) | (((y[1] >> 1) & 0x7fu) << 7) | (((y[2] >> 1) & 0x7fu) << 14) | (((y[3] >> 1) & 0x7fu) << 21)) << 4);
    float M = fmaxf(lse, dppf<ROR8>(lse)); M = fmaxf(M, dppf<ROR4>(M));
    const float e = isb ? 0.f : ex2(lse - M);
    float L = e + dppf<ROR8>(e); L += dppf<ROR4>(L);
    const float cf = e * __builtin_amdgcn_rcpf(L);
    float v[4];
#pragma unroll
    for (int j = 0; j < 4; ++j) v[j] = cf * u2f(y[j] & ~0xffu);
    SUM_ROWS(v);
    if (lane < 16) fr[w * 16 + lane] = v[0];
    bar();
    if (tid < 16) {
      const float a = ((fr[tid] + fr[16 + tid]) + (fr[32 + tid] + fr[48 + tid])) + ((fr[64 + tid] + fr[80 + tid]) + (fr[96 + tid] + fr[112 + tid]));
      const float o = bfr(gate * bfr(a)) + bpart;
      *op = (u16)pk_bf16(o, o);
    }
    if (rs == 0 && tid == 0) __hip_atomic_store((GAS u64*)(ws + WS_EPOCH + rb * 128), tagr, RLX_AGENT);
  }
  STAMP(7);
    return;
  }
  {
    const int h = __builtin_amdgcn_readfirstlane(4 * x8 + idx / 6), b = __builtin_amdgcn_readfirstlane((idx % 6) / 3), sp = __builtin_amdgcn_readfirstlane(idx % 3);
    const int src = b * 128 + h * 4 + sp;
    const u32x4 sel = make_sel(c);
    const int tb = (b * 32 + h) * 512, t1 = tb + 512 * (sp + 1) / 3, t0 = tb + 512 * sp / 3 + w;
    const rsrc_t rk = mk_rsrc(Kc, t1 * 4096), rv = mk_rsrc(Vc, t1 * 4096), ro = mk_rsrc(wo, (OFFM & 128) ? 0 : 4096 * 512 * 2);
#define LDT(r, i_, j) ld128<AUX_NT>(r, lane * 16 + (j) * 1024, (t0 + 8 * (i_)) * 4096)
#define REQ(a, r, i_) { _Pragma("unroll") for (int j = 0; j < 4; ++j) a[j] = LDT(r, i_, j); SB(); }
#define REQ_REST(a, r) { _Pragma("unroll") for (int j = 0; j < 4; ++j) a[j] = ld128<AUX_NT>(r, lane * 16, (t0 - w + 168) * 4096 + (w + 8 * j) * 1024); SB(); }
#define ARRIVED(a) { _Pragma("unroll") for (int j = 0; j < 4; ++j) asm volatile("" : "+v"(a[j])); SB(); }
    u32x4 W[16], A[4], B[4], C[4], D4[4], E[4], F[4];
    const int wr = __builtin_amdgcn_readfirstlane((w + (idx % 6) * 4 / 3) & 7);
    unsigned x0 = x[64 * wr + lane], x1 = x[512 + 64 * wr + lane];
    LD_EPOCH();
    unsigned old = ld32<A_PRE>(rws, lane * 4, WS_Y + src * 2048 + w * 256);
    const unsigned touch = ld32<AUX_NT>(ro, 0, h * 131072), touch2 = ld32<AUX_NT>(rv, 0, t0 * 4096);
    const unsigned touch3 = TOUCH ? ld32<AUX_NT>(rk, 0, t0 * 4096) : 0u;
    SB();
    {
      const rsrc_t rq = mk_rsrc(wq, (OFFM & 64) ? 0 : 512 * 4096 * 2);
#pragma unroll
      for (int i = 0; i < 16; ++i) W[i] = ld128<AUX_NT>(rq, g * (16 * 8192) + c * 16, (64 * wr + i) * 8192 + h * 256);
    }
    SB();
    asm volatile("" : "+v"(x0), "+v"(x1) : "v"(touch3));
    {
      f32x4 D = {0.f, 0.f, 0.f, 0.f};
      rowsum_refill<0>(W, x0 | (x1 << 16), sel, D, [&](auto T) {
        constexpr int j = T & 3;
        if constexpr (T < 4) A[j] = LDT(rk, 0, j); else if constexpr (T < 8) B[j] = LDT(rk, 1, j);
        else if constexpr (T < 12) C[j] = LDT(rk, 2, j); else D4[j] = LDT(rk, 3, j); });
#pragma unroll
      for (int r = 0; r < 4; ++r) red[(wr * 2 + (c >> 3)) * 128 + 32 * g + 8 * r + (c & 7)] = D[r];
    }
    SB();
    REQ(E, rk, 4); REQ_REST(F, rk);
    bar();
    u32x4 q;
    QREAD(q, b);
    STAMP(1);
#define TLREC()
    float* scw = (float*)(lds + L_SC + w * 1536);
    float m = -INFINITY;
#define SCORE(a, i_) { float xs[4]; _Pragma("unroll") for (int j = 0; j < 4; ++j) xs[j] = dot8(a[j], q); \
      float s = bfr(bfr(treduce<4>(xs, c)) * SCALE); \
      if ((i_) == 21) s = (w + 8 * (c >> 2) < 4 * (t1 - t0 + w - 168)) ? s : -INFINITY; \
      m = fmaxf(m, s); scw[(i_) * 16 + (lane >> 2)] = s; asm volatile("" : "+v"(m)); SB(); TLREC(); }
    SCORE(A, 0); REQ(A, rk, 5);
    SCORE(B, 1);
    if (DEEP & 1) REQ(B, rk, 6);
    SCORE(C, 2); SCORE(D4, 3); SCORE(E, 4); SCORE(F, 21);
    USE_EPOCH();
    u64 tag = b ? tag1 : tag0;
    asm volatile("" : "+s"(tag));
    asm volatile("" : "+v"(old) : "v"(touch), "v"(touch2));
    SB();
    const unsigned img = (unsigned)(uintptr_t)(LAS char*)(lds + L_RING) + w * 16384;
    const int dvo = (lane >> 3) * 1024 + 16 * ((lane & 7) ^ ((lane >> 4 & 1) << 1));
#define DMA8(k_, vo) { unsigned keep; \
      asm volatile("s_mov_b32 %0, m0\n\ts_mov_b32 m0, %1\n\ts_nop 4\n\tbuffer_load_dwordx4 %2, %3, %4 offen nt lds\n\ts_mov_b32 m0, %0" : "=&s"(keep) \
                   : "s"(__builtin_amdgcn_readfirstlane(img + (k_) * 1024)), "v"(vo), "s"(ro), "s"(__builtin_amdgcn_readfirstlane((h * 128 + 8 * (k_)) * 1024 + w * 128)) : "memory"); SB(); }
#pragma unroll 1
    for (int i = 5; i < 21; i += 2) {
      const rsrc_t rn = mk_rsrc(i < 19 ? Kc : Vc, t1 * 4096);
      if (DEEP & 1) {
        SCORE(A, i); REQ(A, rn, i < 19 ? i + 2 : 0); DMA8(i - 5, dvo);
        SCORE(B, i + 1); REQ(B, rn, i < 19 ? i + 3 : 1); DMA8(i - 4, dvo ^ 64);
      } else {
        ARRIVED(A); REQ(B, rk, i + 1); DMA8(i - 5, dvo);
        SCORE(A, i);
        ARRIVED(B); REQ(A, rn, i < 19 ? i + 2 : 0); DMA8(i - 4, dvo ^ 64);
        SCORE(B, i + 1);
      }
    }
    m = xrow_max(row_max(m));
    float l = 0.f;
    {
      const float ms = (m == -INFINITY) ? 0.f : m;
      if (lane < 32) scw[352 + lane] = -INFINITY;
#pragma unroll
      for (int k = 0; k < 6; ++k) { const float p = ex2((scw[64 * k + lane] - ms) * LOG2E); l += p; scw[64 * k + lane] = u2f(hilo(p)); }
      l = xrow_sum(row_sum(l));
    }
    f32x4 O = {0.f, 0.f, 0.f, 0.f};
#define PV(a, i_) { rowsum<4, 4>(a, f2u(scw[(i_) * 16 + (lane >> 2)]), sel, O); asm volatile("" : "+v"(O)); SB(); TLREC(); }
    if (!(DEEP & 1)) { ARRIVED(A); REQ(B, rv, 1); }
    else { ARRIVED(B); }
    asm volatile("s_waitcnt vmcnt(%0)" :: "i"((DEEP & 1) ? 0 : 4) : "memory");
    {
      const int q4 = c >> 2, p4 = lane & 3, phi = (q4 >> 1) | ((g & 1) << 1);
#pragma unroll
      for (int s = 0; s < 4; ++s)
#pragma unroll
        for (int ct = 0; ct < 4; ++ct) {
          const char* a = lds + L_RING + w * 16384 + (32 * s + 8 * g + q4) * 128 + 16 * ((p4 >> 1) | ((ct ^ phi) << 1)) + 8 * (p4 & 1);
          const s16x4 lo = __builtin_amdgcn_ds_read_tr16_b64_v4i16((LAS s16x4*)a), hi = __builtin_amdgcn_ds_read_tr16_b64_v4i16((LAS s16x4*)(a + 512));
          W[4 * s + ct] = __builtin_bit_cast(u32x4, __builtin_shufflevector(lo, hi, 0, 1, 2, 3, 4, 5, 6, 7));
        }
    }
#pragma unroll
    for (int i = 0; i < 16; ++i) asm volatile("" : "+v"(W[i]));
    if (DEEP & 2) {
      PV(A, 0); REQ(A, rv, 2); PV(B, 1); REQ(B, rv, 3);
#pragma unroll 1
      for (int i = 2; i < 18; i += 2) { PV(A, i); REQ(A, rv, i + 2); PV(B, i + 1); REQ(B, rv, i + 3); }
    } else {
      PV(A, 0);
      ARRIVED(B); REQ(A, rv, 2); PV(B, 1);
#pragma unroll 1
      for (int i = 2; i < 21 - NBOOST; i += 2) {
        ARRIVED(A); REQ(B, rv, i + 1); PV(A, i);
        ARRIVED(B); REQ(A, rv, i + 2); PV(B, i + 1);
      }
    }
    const GAS u64* go = (const GAS u64*)(ws + WS_GO) + h * 16 + b;
    u64 gov;
    if constexpr (DEEP & 2) {
      REQ(C, rv, 20); REQ_REST(D4, rv);
      gov = __hip_atomic_load(go, RLX_AGENT); SB();
      PV(A, 18); PV(B, 19); PV(C, 20); PV(D4, 21);
    } else if constexpr (NBOOST == 5) {
      ARRIVED(A); REQ(B, rv, 17); REQ(C, rv, 18); REQ(D4, rv, 19); REQ(E, rv, 20); REQ_REST(F, rv);
      gov = __hip_atomic_load(go, RLX_AGENT); SB();
      PV(A, 16); PV(B, 17); PV(C, 18); PV(D4, 19); PV(E, 20); PV(F, 21);
    } else if constexpr (NBOOST == 3) {
      ARRIVED(A); REQ(B, rv, 19); REQ(C, rv, 20); REQ_REST(D4, rv);
      gov = __hip_atomic_load(go, RLX_AGENT); SB();
      PV(A, 18); PV(B, 19); PV(C, 20); PV(D4, 21);
    } else {
      REQ_REST(B, rv);
      gov = __hip_atomic_load(go, RLX_AGENT); SB();
      PV(A, 20); PV(B, 21);
    }
    STAMP(2);
    float* ml = (float*)(lds + L_ML);
#pragma unroll
    for (int r = 0; r < 4; ++r) {
      const unsigned hl = hilo(O[r] + dppf<ROR8>(O[r]));
      *(u16*)(lds + L_P + (2 * w + (c >> 3)) * 272 + (32 * g + 8 * r + (c & 7)) * 2) = (u16)((c & 8) ? (hl >> 16) : hl);
    }
    if (lane == 0) { ml[w] = m; ml[8 + w] = l; }
    bar();
    STAMP(4);
    f32x4 D[4];
    const float mj = ml[lane & 7], lj = ml[8 + (lane & 7)], ma = ml[2 * g], mb = ml[2 * g + 1];
    project(W, lds, g, c, D);
    float M = fmaxf(mj, dppf<XOR7>(mj)); M = fmaxf(M, dppf<XOR2>(M)); M = fmaxf(M, dppf<XOR1>(M));
    const float Ms = (M == -INFINITY) ? 0.f : M;
    float L = ex2((mj - Ms) * LOG2E) * lj; L += dppf<XOR7>(L); L += dppf<XOR2>(L); L += dppf<XOR1>(L);
    const float inv = __builtin_amdgcn_rcpf(L), fa = ex2((ma - Ms) * LOG2E) * inv, fb = ex2((mb - Ms) * LOG2E) * inv;
    float y4[4];
#pragma unroll
    for (int k = 0; k < 4; ++k) y4[k] = fa * (D[k][0] + D[k][1]) + fb * (D[k][2] + D[k][3]);
    const auto s0 = __builtin_amdgcn_permlane32_swap(f2u(y4[0]), f2u(y4[2]), false, false), s1 = __builtin_amdgcn_permlane32_swap(f2u(y4[1]), f2u(y4[3]), false, false);
    const auto s2 = __builtin_amdgcn_permlane16_swap(f2u(u2f(s0[0]) + u2f(s0[1])), f2u(u2f(s1[0]) + u2f(s1[1])), false, false);
    const unsigned code = f2u(Ms * LOG2E + __builtin_amdgcn_logf(L)) >> 4;
    const unsigned yw = (f2u(u2f(s2[0]) + u2f(s2[1])) & ~0xffu) | (((code >> (7 * (lane & 3))) & 0x7fu) << 1) | ((old & 1u) ^ 1u);
    const u32x4 yv = {yw, f2u(dppf<0x55>(u2f(yw))), f2u(dppf<0xAA>(u2f(yw))), f2u(dppf<0xFF>(u2f(yw)))};
#pragma unroll 1
    for (unsigned spins = 0; !(OFFM & 256) && uniform64(gov) != tag && spins < SPIN_CAP; ++spins) gov = __hip_atomic_load(go, RLX_AGENT);
    if ((lane & 3) == 0) __builtin_amdgcn_raw_buffer_store_b128(yv, rws, lane * 4, WS_Y + src * 2048 + w * 256, A_YST);
    STAMP(5); STAMP(6); STAMP(3);
  }
}

extern "C" void kernel_launch(void* const* d_in, const int* in_sizes, int n_in, void* d_out, int out_size, void* d_ws, size_t ws_size, hipStream_t stream) {
  fused_decode<<<256, 512, 0, stream>>>((const u16*)d_in[0], (const u16*)d_in[1], (const u16*)d_in[2], (const u16*)d_in[3], (const u16*)d_in[4],
                                        (const u16*)d_in[6], (const u16*)d_in[7], (const u16*)d_in[8], (const u16*)d_in[9], (const u16*)d_in[10],
                                        (const u16*)d_in[11], (u16*)d_out, (char*)d_ws);
}
```

```cpp
#include <hip/hip_runtime.h>
#include <stdint.h>
#include <type_traits>

typedef unsigned short u16;
typedef unsigned long long u64;
typedef unsigned u32x4 __attribute__((ext_vector_type(4)));
typedef unsigned u32x2 __attribute__((ext_vector_type(2)));
typedef float f32x4 __attribute__((ext_vector_type(4)));
typedef float f32x16 __attribute__((ext_vector_type(16)));
typedef __bf16 bf16x8 __attribute__((ext_vector_type(8)));
typedef __bf16 bf16x2 __attribute__((ext_vector_type(2)));
typedef short s16x4 __attribute__((ext_vector_type(4)));
typedef __amdgpu_buffer_rsrc_t rsrc_t;
#define GAS __attribute__((address_space(1)))
#define LAS __attribute__((address_space(3)))
#define DI __device__ __forceinline__
#define RLX_AGENT __ATOMIC_RELAXED, __HIP_MEMORY_SCOPE_AGENT

#define DEEP 0
constexpr int AUX_NT = 2, AUX_SC1 = 16, AUX_SC1_NT = 18;
constexpr int W_BYTES = 512 * 4096 * 2;
constexpr float SCALE = 0.08838834764831845f;
constexpr float LOG2E = 1.4426950408889634f;

constexpr int WS_EPOCH = 0;
constexpr int WS_F1 = 1024;
constexpr int WS_F2 = 2048;
constexpr int WS_GO = 4096;
constexpr int WS_QA = 8192;
constexpr int WS_ML = WS_QA + 65536;
constexpr int WS_PB = WS_ML + 8192;
constexpr int WS_Y = WS_PB + 2 * 32 * 16 * 1024;
constexpr int WS_END = WS_Y + 2 * 128 * 2048;

constexpr int L_RING = 0;
constexpr int L_RED = 131072;
constexpr int L_P = L_RED + 8192;
constexpr int L_MX = L_P + 32 * 144;
constexpr int L_ML = L_MX + 512;
constexpr int L_FIN = L_ML + 512;
constexpr int L_SC = 147456;
constexpr int L_QA = 131072;
constexpr int L_END = 163840;

DI rsrc_t mk_rsrc(const void* p, unsigned bytes) { return __builtin_amdgcn_make_buffer_rsrc((void*)p, 0, bytes, 0x00020000); }
template <int AUX> DI u32x4 ld128(rsrc_t r, int voff, int soff) { return __builtin_amdgcn_raw_buffer_load_b128(r, voff, soff, AUX); }
template <int AUX> DI u32x2 ld64(rsrc_t r, int voff, int soff) { return __builtin_amdgcn_raw_buffer_load_b64(r, voff, soff, AUX); }
template <int AUX> DI unsigned ld32(rsrc_t r, int voff, int soff) { return __builtin_amdgcn_raw_buffer_load_b32(r, voff, soff, AUX); }
DI float u2f(unsigned u) { return __builtin_bit_cast(float, u); }
DI unsigned f2u(float f) { return __builtin_bit_cast(unsigned, f); }
DI unsigned pk_bf16(float lo, float hi) { bf16x2 r; r[0] = (__bf16)lo; r[1] = (__bf16)hi; return __builtin_bit_cast(unsigned, r); }
DI float bfr(float x) { return u2f(pk_bf16(x, x) << 16); }
DI unsigned hilo(float x) { const float h = bfr(x); return pk_bf16(h, x - h); }
DI float ex2(float x) { return __builtin_amdgcn_exp2f(x); }
DI float dot2(unsigned a, unsigned b, float c) { return __builtin_amdgcn_fdot2_f32_bf16(__builtin_bit_cast(bf16x2, a), __builtin_bit_cast(bf16x2, b), c, false); }
DI float dot8(const u32x4& a, const u32x4& b) { return dot2(a[3], b[3], dot2(a[2], b[2], dot2(a[1], b[1], dot2(a[0], b[0], 0.f)))); }
template <int CTRL> DI float dppf(float x) { return u2f(__builtin_amdgcn_mov_dpp(f2u(x), CTRL, 0xf, 0xf, true)); }
template <int T> DI unsigned bcast16(unsigned v) { return __builtin_amdgcn_mov_dpp(v, 0x150 + T, 0xf, 0xf, true); }
constexpr int XOR1 = 0xB1, XOR2 = 0x4E, XOR7 = 0x141, ROR8 = 0x128, ROR4 = 0x124;
DI float xrow_max(float x) {
  auto s = __builtin_amdgcn_permlane16_swap(f2u(x), f2u(x), false, false); x = fmaxf(u2f(s[0]), u2f(s[1]));
  auto t = __builtin_amdgcn_permlane32_swap(f2u(x), f2u(x), false, false); return fmaxf(u2f(t[0]), u2f(t[1]));
}
DI float xrow_sum(float x) {
  auto s = __builtin_amdgcn_permlane16_swap(f2u(x), f2u(x), false, false); x = u2f(s[0]) + u2f(s[1]);
  auto t = __builtin_amdgcn_permlane32_swap(f2u(x), f2u(x), false, false); return u2f(t[0]) + u2f(t[1]);
}
DI float row_max(float x) { x = fmaxf(x, dppf<ROR8>(x)); x = fmaxf(x, dppf<ROR4>(x)); x = fmaxf(x, dppf<XOR2>(x)); return fmaxf(x, dppf<XOR1>(x)); }
DI float row_sum(float x) { x += dppf<ROR8>(x); x += dppf<ROR4>(x); x += dppf<XOR2>(x); return x + dppf<XOR1>(x); }
DI f32x4 mfma16(u32x4 a, u32x4 b, f32x4 c) { return __builtin_amdgcn_mfma_f32_16x16x32_bf16(__builtin_bit_cast(bf16x8, a), __builtin_bit_cast(bf16x8, b), c, 0, 0, 0); }
DI f32x16 mfma32(u32x4 a, u32x4 b, f32x16 c) { return __builtin_amdgcn_mfma_f32_32x32x16_bf16(__builtin_bit_cast(bf16x8, a), __builtin_bit_cast(bf16x8, b), c, 0, 0, 0); }
DI u64 uniform64(u64 v) { return ((u64)(unsigned)__builtin_amdgcn_readfirstlane((unsigned)(v >> 32)) << 32) | (unsigned)__builtin_amdgcn_readfirstlane((unsigned)v); }
DI u64 next_tag(u64 e) { return e * 0x9E3779B97F4A7C15ull + 0x632BE59BD9B4E019ull; }
DI void bar() { __syncthreads(); }
#define DRAIN() asm volatile("s_waitcnt vmcnt(0)" ::: "memory")
#define PIN() asm volatile("" ::: "memory")
#define SB() __builtin_amdgcn_sched_barrier(0)
constexpr unsigned SPIN_CAP = 1u << 22;

template <int N, int STEP, int T = 0> DI void rowsum(const u32x4 (&W)[N], unsigned pair, const u32x4& sel, f32x4& D) {
  if constexpr (T < N) {
    const unsigned s = bcast16<T * STEP>(pair);
    const u32x4 B = {__builtin_amdgcn_perm(s, s, sel[0]), __builtin_amdgcn_perm(s, s, sel[1]), __builtin_amdgcn_perm(s, s, sel[2]), __builtin_amdgcn_perm(s, s, sel[3])};
    D = mfma16(W[T], B, D);
    rowsum<N, STEP, T + 1>(W, pair, sel, D);
  }
}
template <int T, class F> DI void rowsum_refill(const u32x4 (&W)[16], unsigned pair, const u32x4& sel, f32x4& D, F&& issue) {
  if constexpr (T < 16) {
    const unsigned s = bcast16<T>(pair);
    const u32x4 B = {__builtin_amdgcn_perm(s, s, sel[0]), __builtin_amdgcn_perm(s, s, sel[1]), __builtin_amdgcn_perm(s, s, sel[2]), __builtin_amdgcn_perm(s, s, sel[3])};
    D = mfma16(W[T], B, D);
    asm volatile("" : "+v"(D));
    __builtin_amdgcn_sched_barrier(0);
    issue(std::integral_constant<int, T>{});
    __builtin_amdgcn_sched_barrier(0);
    rowsum_refill<T + 1>(W, pair, sel, D, issue);
  }
}
DI int swz16(int row) { const int u = (row >> 2) & 3; return ((row & 3) << 2) | (((u ^ (u >> 1)) & 1) << 1) | (u >> 1); }
DI u32x4 make_sel(int c) {
  const int j = c & 7; const unsigned b = (c & 8) ? 0x0302u : 0x0100u;
  const unsigned v = (j & 1) ? ((b << 16) | 0x0c0cu) : (0x0c0c0000u | b);
  u32x4 s;
#pragma unroll
  for (int w = 0; w < 4; ++w) s[w] = (w == (j >> 1)) ? v : 0x0c0c0c0cu;
  return s;
}
template <int N> DI float treduce(float (&x)[N], int c) {
#define TR_STEP(CNT, CTRL, BIT) _Pragma("unroll") for (int i = 0; i < (CNT); ++i) { const float a = x[i] + dppf<CTRL>(x[i]), b = x[i + (CNT)] + dppf<CTRL>(x[i + (CNT)]); x[i] = (c & (BIT)) ? b : a; }
  TR_STEP(N / 2, ROR8, 8)
  TR_STEP(N / 4, XOR7, 4)
  if constexpr (N >= 8) { TR_STEP(N / 8, XOR2, 2) } else { x[0] += dppf<XOR2>(x[0]); }
  if constexpr (N >= 16) { TR_STEP(N / 16, XOR1, 1) } else { x[0] += dppf<XOR1>(x[0]); }
#undef TR_STEP
  return x[0];
}
DI void wait_flags32(const GAS u64* f, u64 tag, int lane) {
#pragma unroll 1
  for (unsigned spins = 0; spins < SPIN_CAP; ++spins) {
    if (__all(__hip_atomic_load(f + (lane & 31), RLX_AGENT) == tag)) break;
    __builtin_amdgcn_s_sleep(3);
  }
  __builtin_amdgcn_fence(__ATOMIC_ACQUIRE, "wavefront");
  asm volatile("" ::: "memory");
}
DI void transpose_w(const u32x2 (&R)[32], u32x4 (&F)[16]) {
#pragma unroll
  for (int s = 0; s < 4; ++s)
#pragma unroll
    for (int t = 0; t < 4; ++t)
#pragma unroll
      for (int jj = 0; jj < 4; ++jj) F[4 * s + t][jj] = __builtin_amdgcn_perm(R[8 * s + 2 * jj + 1][t >> 1], R[8 * s + 2 * jj][t >> 1], (t & 1) ? 0x07060302u : 0x05040100u);
}
DI void project(const u32x4 (&F)[16], const char* lds, int g, int c, f32x4 (&D)[4]) {
  u32x4 A[4];
#pragma unroll
  for (int s = 0; s < 4; ++s) A[s] = *(const u32x4*)(lds + L_P + c * 272 + 64 * s + 16 * g);
  __builtin_amdgcn_sched_barrier(0);
#pragma unroll
  for (int t = 0; t < 4; ++t) D[t] = f32x4{0.f, 0.f, 0.f, 0.f};
#pragma unroll
  for (int s = 0; s < 4; ++s)
#pragma unroll
    for (int t = 0; t < 4; ++t) D[t] = mfma16(A[s], F[4 * s + t], D[t]);
}
DI unsigned lsb4(const u32x4& v) { return (v[0] & 1u) | ((v[1] & 1u) << 1) | ((v[2] & 1u) << 2) | ((v[3] & 1u) << 3); }
DI unsigned mark(float v, unsigned old, int k) { return (f2u(v) & ~1u) | (((old >> k) & 1u) ^ 1u); }

__global__ void __launch_bounds__(512, 2)
fused_decode(const u16* __restrict__ x, const u16* __restrict__ Kc, const u16* __restrict__ Vc, const u16* __restrict__ wq, const u16* __restrict__ wo,
             const u16* __restrict__ Cc, const u16* __restrict__ w_q, const u16* __restrict__ w_uk, const u16* __restrict__ w_uv, const u16* __restrict__ w_o,
             const u16* __restrict__ gw, u16* __restrict__ out, char* ws) {
  __shared__ __attribute__((aligned(16))) char lds[L_END];
  const int bid = blockIdx.x, tid = threadIdx.x, lane = tid & 63, w = __builtin_amdgcn_readfirstlane(tid >> 6), g = lane >> 4, c = lane & 15;
  const int x8 = bid & 7, idx = bid >> 3;
  asm volatile("" :: "s"(x), "s"(Kc), "s"(Vc), "s"(wq), "s"(wo), "s"(ws));
  const rsrc_t rws = mk_rsrc(ws, WS_END);
  float* red = (float*)(lds + L_RED);
#define LD_EPOCH() u64 e0 = __hip_atomic_load((GAS u64*)(ws + WS_EPOCH), RLX_AGENT), e1 = __hip_atomic_load((GAS u64*)(ws + WS_EPOCH + 128), RLX_AGENT);
#define USE_EPOCH() asm volatile("" : "+v"(e0), "+v"(e1)); const u64 tag0 = next_tag(uniform64(e0)), tag1 = next_tag(uniform64(e1));
#define LD_ROWS(W, ptr) { const rsrc_t rq = mk_rsrc(ptr, W_BYTES); \
    _Pragma("unroll") for (int t = 0; t < 16; ++t) W[t] = ld128<AUX_NT>(rq, g * (16 * 8192) + c * 16, (64 * w + t) * 8192 + h * 256); }
#define LD_OUTW(R, ptr) { const rsrc_t ro = mk_rsrc(ptr, W_BYTES); \
    _Pragma("unroll") for (int i = 0; i < 32; ++i) R[i] = ld64<AUX_NT>(ro, g * 8192 + c * 8 + (i & 3) * 1024, (h * 128 + 32 * (i >> 3) + (i & 4)) * 1024 + w * 128); }
#define QREAD(q, bb) { f32x4 a0 = {0.f, 0.f, 0.f, 0.f}, a1 = a0; \
    _Pragma("unroll") for (int ww = 0; ww < 8; ++ww) { const f32x4* p = (const f32x4*)(red + (ww * 2 + (bb)) * 128 + 8 * c); a0 += p[0]; a1 += p[1]; } \
    q = u32x4{pk_bf16(a0[0], a0[1]), pk_bf16(a0[2], a0[3]), pk_bf16(a1[0], a1[1]), pk_bf16(a1[2], a1[3])}; }

  if (idx >= 24) {
  const int rb = idx >= 28, rs = __builtin_amdgcn_readfirstlane(4 * x8 + (idx & 3));
  const int fy_off = (rb * 128 + (tid >> 2)) * 2048 + rs * 64 + (tid & 3) * 16;
  unsigned fold; u64 tagr;
  float gate;
#define LD_GATE() const u32x4 xv = *(const u32x4*)(x + rb * 512 + lane * 8), gv = *(const u32x4*)(gw + lane * 8);
#define USE_GATE() { float s = 0.f; \
    _Pragma("unroll") for (int j = 0; j < 4; ++j) s += bfr(u2f(xv[j] << 16) * u2f(gv[j] << 16)) + bfr(u2f(xv[j] & 0xffff0000u) * u2f(gv[j] & 0xffff0000u)); \
    s = bfr(xrow_sum(row_sum(s))); gate = bfr(1.f / (1.f + __expf(-s))); }

  if (idx < 28) {
    const int h = rs;
    const u32x4 sel = make_sel(c);
    u32x4 W[16], U[16];
    unsigned x0 = x[tid], x1 = x[512 + tid];
    const unsigned tch = ld32<AUX_NT>(mk_rsrc(w_uk, W_BYTES), (lane & 1) << 21, h * 256) ^ ld32<AUX_NT>(mk_rsrc(w_uv, W_BYTES), (lane & 1) << 21, h * 256) ^ ld32<AUX_NT>(mk_rsrc(w_o, W_BYTES), 0, h * 131072);
    LD_ROWS(W, w_q);
    SB();
    {
      asm volatile("" : "+v"(x0), "+v"(x1) : "v"(tch));
      const unsigned xp = x0 | (x1 << 16);
      const rsrc_t ru = mk_rsrc(w_uk, W_BYTES);
      f32x4 D = {0.f, 0.f, 0.f, 0.f};
      rowsum_refill<0>(W, xp, sel, D, [&](auto T) { U[T] = ld128<AUX_NT>(ru, g * (16 * 8192) + c * 16, (64 * w + T) * 8192 + h * 256); });
#pragma unroll
      for (int r = 0; r < 4; ++r) red[(w * 2 + (c >> 3)) * 128 + 32 * g + 8 * r + (c & 7)] = D[r];
    }
    LD_EPOCH();
    const u32x4 fy = ld128<AUX_SC1_NT>(rws, fy_off, WS_Y);
    const u32x4 oy0 = ld128<AUX_SC1_NT>(rws, c * 16, WS_Y + (h * 4 + 3) * 2048 + w * 256), oy1 = ld128<AUX_SC1_NT>(rws, c * 16, WS_Y + (128 + h * 4 + 3) * 2048 + w * 256);
    LD_GATE();
    SB();
    bar();
#pragma unroll
    for (int bb = 0; bb < 2; ++bb) {
      u32x4 q; QREAD(q, bb);
      float xs[16];
#pragma unroll
      for (int t = 0; t < 16; ++t) xs[t] = dot8(U[t], q);
      const float qa = treduce<16>(xs, c);
      *(u16*)(lds + L_RING + bb * 1024 + tid * 2) = (u16)pk_bf16(qa, qa);
    }
    SB();
    USE_EPOCH(); USE_GATE();
    fold = lsb4(fy);
    unsigned old = lsb4(oy0) | (lsb4(oy1) << 4);
    asm volatile("" : "+v"(fold), "+v"(gate), "+v"(old));
    tagr = tag0;
    DRAIN();
    bar();
    if (w == 0) {
      __builtin_amdgcn_raw_buffer_store_b128(*(const u32x4*)(lds + L_RING + lane * 16), rws, lane * 16, WS_QA + h * 1024, AUX_SC1);
      __builtin_amdgcn_raw_buffer_store_b128(*(const u32x4*)(lds + L_RING + 1024 + lane * 16), rws, lane * 16, WS_QA + (32 + h) * 1024, AUX_SC1);
      DRAIN();
      if (lane == 0) __hip_atomic_store((GAS u64*)(ws + WS_F1) + h, tag0, RLX_AGENT);
    }
    {
      u32x2 R[32];
      LD_OUTW(R, w_o);
      LD_ROWS(W, w_uv);
      SB();
      transpose_w(R, U);
    }
#pragma unroll
    for (int i = 0; i < 16; ++i) asm volatile("" : "+v"(W[i]), "+v"(U[i]));
    if (w == 0) {
      wait_flags32((const GAS u64*)(ws + WS_F2), tag0, lane);
      if (lane < 2) __hip_atomic_store((GAS u64*)(ws + WS_GO) + h * 16 + lane, lane ? tag1 : tag0, RLX_AGENT);
    }
    bar();
    u32x4 T[4];
#pragma unroll
    for (int k = 0; k < 4; ++k) T[k] = ld128<AUX_SC1_NT>(rws, lane * 16, WS_PB + (((k >> 1) * 32 + h) * 16 + 2 * w + (k & 1)) * 1024);
    const u32x2 mlw = ld64<AUX_SC1_NT>(rws, (g & 1) * 4096 + c * 8, WS_ML + h * 128);
    const float ms = u2f(mlw[0]), M0 = row_max(ms), M = (M0 == -INFINITY) ? 0.f : M0;
    const float wt = ex2((ms - M) * LOG2E) * u2f(mlw[1]), cf = wt / row_sum(wt);
    float* cr = (float*)(lds + L_RING);
#pragma unroll
    for (int bb = 0; bb < 2; ++bb) {
      const float f0 = u2f(__builtin_amdgcn_readlane(f2u(cf), 16 * bb + 2 * w)), f1 = u2f(__builtin_amdgcn_readlane(f2u(cf), 16 * bb + 2 * w + 1));
      float a[8];
#pragma unroll
      for (int j = 0; j < 4; ++j) {
        a[2 * j] = f0 * u2f(T[2 * bb][j] << 16) + f1 * u2f(T[2 * bb + 1][j] << 16);
        a[2 * j + 1] = f0 * u2f(T[2 * bb][j] & 0xffff0000u) + f1 * u2f(T[2 * bb + 1][j] & 0xffff0000u);
      }
      *(f32x4*)(cr + (w * 2 + bb) * 512 + lane * 8) = f32x4{a[0], a[1], a[2], a[3]};
      *(f32x4*)(cr + (w * 2 + bb) * 512 + lane * 8 + 4) = f32x4{a[4], a[5], a[6], a[7]};
    }
    bar();
    {
      float c0 = 0.f, c1 = 0.f;
#pragma unroll
      for (int ww = 0; ww < 8; ++ww) { c0 += cr[ww * 1024 + tid]; c1 += cr[ww * 1024 + 512 + tid]; }
      const unsigned h0 = hilo(c0), h1 = hilo(c1);
      f32x4 D = {0.f, 0.f, 0.f, 0.f}, D2 = D;
      rowsum<16, 1>(W, __builtin_amdgcn_perm(h1, h0, 0x05040100u), sel, D);
      rowsum<16, 1>(W, __builtin_amdgcn_perm(h1, h0, 0x07060302u), sel, D2);
      D += D2;
#pragma unroll
      for (int r = 0; r < 4; ++r) red[(w * 2 + (c >> 3)) * 128 + 32 * g + 8 * r + (c & 7)] = D[r];
    }
    bar();
    if (tid < 256) {
      float v = 0.f;
#pragma unroll
      for (int ww = 0; ww < 8; ++ww) v += red[ww * 256 + tid];
      *(u16*)(lds + L_P + (tid >> 7) * 272 + (tid & 127) * 2) = (u16)pk_bf16(v, v);
    }
    bar();
    f32x4 D[4];
    project(U, lds, g, c & 1, D);
    if (g == 0) {
      __builtin_amdgcn_raw_buffer_store_b128(u32x4{mark(D[0][0], old, 0), mark(D[1][0], old, 1), mark(D[2][0], old, 2), mark(D[3][0], old, 3)}, rws, c * 16, WS_Y + (h * 4 + 3) * 2048 + w * 256, AUX_SC1);
      __builtin_amdgcn_raw_buffer_store_b128(u32x4{mark(D[0][1], old, 4), mark(D[1][1], old, 5), mark(D[2][1], old, 6), mark(D[3][1], old, 7)}, rws, c * 16, WS_Y + (128 + h * 4 + 3) * 2048 + w * 256, AUX_SC1);
    }
  } else {
    const int b = rs & 1, sp = rs >> 1;
    u32x4 W[16], S0[8], S1[8];
#define LDC(st, i) ld128<AUX_NT>(mk_rsrc(Cc, (st) < 8 ? (16u << 20) : 0u), lane * 16 + ((i) & 3) * 1024, (b * 8192 + sp * 512 + ((st) & 7) * 64 + 8 * w + ((i) & 4)) * 1024)
#define LDS_(S, st) _Pragma("unroll") for (int i = 0; i < 8; ++i) S[i] = LDC(st, i);
#define STC(slot, S) { int ln = lane; asm volatile("" : "+v"(ln)); char* base = lds + L_RING + (slot) * 65536 + (ln >> 4) * 16384 + 8 * w * 256; \
      _Pragma("unroll") for (int i = 0; i < 8; ++i) *(u32x4*)(base + i * 256 + 16 * ((ln & 15) ^ swz16(8 * w + i))) = S[i]; }
    LD_EPOCH();
    const u32x4 fy = ld128<AUX_SC1_NT>(rws, fy_off, WS_Y);
    LD_GATE();
    LDS_(S0, 0);
    LDS_(S1, 1);
    SB();
    USE_EPOCH(); USE_GATE();
    fold = lsb4(fy);
    asm volatile("" : "+v"(fold), "+v"(gate));
    tagr = tag1;
    STC(0, S0);
    SB();
    LDS_(S0, 2);
    SB();
    STC(1, S1);
    SB();
    LDS_(S1, 3);
    PIN();
    if (w == 0) wait_flags32((const GAS u64*)(ws + WS_F1), tag0, lane);
    bar();
#pragma unroll
    for (int k = 0; k < 4; ++k) *(u32x4*)(lds + L_QA + (4 * w + k) * 1024 + 16 * (lane ^ swz16(4 * w + k))) = ld128<AUX_SC1>(rws, lane * 16, WS_QA + (b * 32 + 4 * w + k) * 1024);
    bar();
    const int kt = w & 3, hh = w >> 2;
#pragma unroll
    for (int s = 0; s < 16; ++s) W[s] = *(const u32x4*)(lds + L_QA + (16 * hh + c) * 1024 + 16 * ((4 * s + g) ^ swz16(c)));
#pragma unroll
    for (int s = 0; s < 16; ++s) asm volatile("" : "+v"(W[s]));
    bar();
    float mq = -INFINITY, lq = 0.f, mp = -INFINITY;
    f32x16 A0, A1;
#pragma unroll
    for (int i = 0; i < 16; ++i) { A0[i] = 0.f; A1[i] = 0.f; }
    float* mx = (float*)(lds + L_MX);
#define STAGE(slot, REFILL) { const char* tile = lds + L_RING + (slot) * 65536; \
      int ln = lane; asm volatile("" : "+v"(ln)); \
      const int g = ln >> 4, c = ln & 15, swz = swz16(c), h5 = ln >> 5, hq = 16 * hh + c, hp = ln & 31; \
      const char* rowp = tile + (16 * kt + c) * 256; \
      f32x4 X0 = {0.f, 0.f, 0.f, 0.f}, X1 = X0; \
      { u32x4 fa[4], fb[4]; \
        _Pragma("unroll") for (int j = 0; j < 4; ++j) fa[j] = *(const u32x4*)(rowp + 16 * ((4 * j + g) ^ swz)); \
        _Pragma("unroll") for (int j = 0; j < 4; ++j) fb[j] = *(const u32x4*)(rowp + 16384 + 16 * ((4 * j + g) ^ swz)); \
        SB(); \
        X0 = mfma16(fa[0], W[0], X0); X1 = mfma16(fa[1], W[1], X1); X0 = mfma16(fa[2], W[2], X0); X1 = mfma16(fa[3], W[3], X1); \
        SB(); \
        _Pragma("unroll") for (int j = 0; j < 4; ++j) fa[j] = *(const u32x4*)(rowp + 32768 + 16 * ((4 * j + g) ^ swz)); \
        SB(); \
        X0 = mfma16(fb[0], W[4], X0); X1 = mfma16(fb[1], W[5], X1); X0 = mfma16(fb[2], W[6], X0); X1 = mfma16(fb[3], W[7], X1); \
        SB(); \
        _Pragma("unroll") for (int j = 0; j < 4; ++j) fb[j] = *(const u32x4*)(rowp + 49152 + 16 * ((4 * j + g) ^ swz)); \
        SB(); \
        X0 = mfma16(fa[0], W[8], X0); X1 = mfma16(fa[1], W[9], X1); X0 = mfma16(fa[2], W[10], X0); X1 = mfma16(fa[3], W[11], X1); \
        X0 = mfma16(fb[0], W[12], X0); X1 = mfma16(fb[1], W[13], X1); X0 = mfma16(fb[2], W[14], X0); X1 = mfma16(fb[3], W[15], X1); } \
      float sc[4]; \
      _Pragma("unroll") for (int r = 0; r < 4; ++r) sc[r] = bfr(bfr(X0[r] + X1[r]) * SCALE); \
      const float tm = xrow_max(fmaxf(fmaxf(sc[0], sc[1]), fmaxf(sc[2], sc[3]))); \
      if (g == 0) mx[kt * 32 + hq] = tm; \
      bar(); \
      { const float q0 = mx[hq], q1 = mx[32 + hq], q2 = mx[64 + hq], q3 = mx[96 + hq], p0 = mx[hp], p1 = mx[32 + hp], p2 = mx[64 + hp], p3 = mx[96 + hp]; \
        const float mn = fmaxf(fmaxf(mq, fmaxf(q0, q1)), fmaxf(q2, q3)), ms = (mn == -INFINITY) ? 0.f : mn; \
        float p[4]; \
        _Pragma("unroll") for (int r = 0; r < 4; ++r) p[r] = ex2((sc[r] - ms) * LOG2E); \
        *(u32x2*)(lds + L_P + hq * 144 + (16 * kt + 4 * g) * 2) = u32x2{pk_bf16(p[0], p[1]), pk_bf16(p[2], p[3])}; \
        lq = lq * ex2((mq - ms) * LOG2E) + ((p[0] + p[1]) + (p[2] + p[3])); \
        mq = mn; \
        const float mn2 = fmaxf(fmaxf(mp, fmaxf(p0, p1)), fmaxf(p2, p3)), ms2 = (mn2 == -INFINITY) ? 0.f : mn2; \
        const float al = ex2((mp - ms2) * LOG2E); mp = mn2; A0 *= al; A1 *= al; } \
      SB(); REFILL; SB(); \
      { const int blk = (ln >> 4) & 1, q4 = (ln & 15) >> 2, p4 = ln & 3; \
        const char* trp = tile + (w >> 1) * 16384 + (8 * h5 + q4) * 256 + 8 * (p4 & 1); \
        const int ch0 = 8 * (w & 1) + 2 * blk + (p4 >> 1), z0 = swz16(8 * h5 + q4), z1 = swz16(8 * h5 + 4 + q4); \
        s16x4 f[4][2][2]; \
        _Pragma("unroll") for (int ks = 0; ks < 4; ++ks) _Pragma("unroll") for (int ct = 0; ct < 2; ++ct) { \
          f[ks][ct][0] = __builtin_amdgcn_ds_read_tr16_b64_v4i16((LAS s16x4*)(trp + ks * 4096 + 16 * ((ch0 + 4 * ct) ^ z0))); \
          f[ks][ct][1] = __builtin_amdgcn_ds_read_tr16_b64_v4i16((LAS s16x4*)(trp + ks * 4096 + 1024 + 16 * ((ch0 + 4 * ct) ^ z1))); } \
        SB(); \
        bar(); \
        u32x4 P[4]; \
        _Pragma("unroll") for (int ks = 0; ks < 4; ++ks) P[ks] = *(const u32x4*)(lds + L_P + hp * 144 + (16 * ks + 8 * h5) * 2); \
        SB(); \
        _Pragma("unroll") for (int ks = 0; ks < 4; ++ks) { \
          A0 = mfma32(__builtin_bit_cast(u32x4, __builtin_shufflevector(f[ks][0][0], f[ks][0][1], 0, 1, 2, 3, 4, 5, 6, 7)), P[ks], A0); \
          A1 = mfma32(__builtin_bit_cast(u32x4, __builtin_shufflevector(f[ks][1][0], f[ks][1][1], 0, 1, 2, 3, 4, 5, 6, 7)), P[ks], A1); } \
        SB(); } }
    STAGE(0, ;);
#pragma unroll 1
    for (int it = 0; it < 3; ++it) {
      STAGE(1, { STC(0, S0); SB(); LDS_(S0, 2 * it + 4); });
      STAGE(0, { STC(1, S1); SB(); LDS_(S1, 2 * it + 5); });
    }
    STAGE(1, ;);
    const int h5 = lane >> 5, hq = 16 * hh + c, hp = lane & 31;
    float* lx = (float*)(lds + L_ML);
    lq = xrow_sum(lq);
    if (g == 0) lx[kt * 32 + hq] = lq;
    bar();
    const float lt = (lx[hp] + lx[32 + hp]) + (lx[64 + hp] + lx[96 + hp]);
    const float inv = 1.f / lt;
    char* img = lds + L_RING + hp * 1040 + (64 * w + 4 * h5) * 2;
#pragma unroll
    for (int k = 0; k < 4; ++k) {
      *(u32x2*)(img + 16 * k) = u32x2{pk_bf16(A0[4 * k] * inv, A0[4 * k + 1] * inv), pk_bf16(A0[4 * k + 2] * inv, A0[4 * k + 3] * inv)};
      *(u32x2*)(img + 64 + 16 * k) = u32x2{pk_bf16(A1[4 * k] * inv, A1[4 * k + 1] * inv), pk_bf16(A1[4 * k + 2] * inv, A1[4 * k + 3] * inv)};
    }
    if (tid < 32) __builtin_amdgcn_raw_buffer_store_b64(u32x2{f2u(mp), f2u(lt)}, rws, hp * 128, WS_ML + b * 4096 + sp * 8, AUX_SC1);
    bar();
#pragma unroll
    for (int k = 0; k < 4; ++k)
      __builtin_amdgcn_raw_buffer_store_b128(*(const u32x4*)(lds + L_RING + (4 * w + k) * 1040 + lane * 16), rws, lane * 16, WS_PB + ((b * 32 + 4 * w + k) * 16 + sp) * 1024, AUX_SC1_NT);
    DRAIN();
    bar();
    if (tid == 0) __hip_atomic_store((GAS u64*)(ws + WS_F2) + rs, tag0, RLX_AGENT);
  }
  {
    const bool isb = (lane & 12) == 12;
    float* fr = (float*)(lds + L_RING);
    u32x4 y = {0u, 0u, 0u, 0u};
#define POLL_Y(SLP) _Pragma("unroll 1") for (unsigned spins = 0; spins < SPIN_CAP; ++spins) { \
      PIN(); y = ld128<AUX_SC1>(rws, fy_off, WS_Y); if ((lsb4(y) ^ fold) == 15u) break; __builtin_amdgcn_s_sleep(SLP); }
#define SUM_ROWS(v) { const float a0 = v[0] + dppf<ROR8>(v[0]), a1 = v[1] + dppf<ROR8>(v[1]), a2 = v[2] + dppf<ROR8>(v[2]), a3 = v[3] + dppf<ROR8>(v[3]); \
      const float b0 = (c & 8) ? a2 : a0, b1 = (c & 8) ? a3 : a1; \
      const float c0 = b0 + dppf<0x124>(b0), c1 = b1 + dppf<0x12C>(b1); \
      v[0] = xrow_sum((c & 4) ? c0 : c1); }
    if (isb) { POLL_Y(16); }
    {
      float v[4];
#pragma unroll
      for (int j = 0; j < 4; ++j) v[j] = u2f(y[j] & ~0xffu);
      SUM_ROWS(v);
      if (lane < 16) fr[128 + w * 16 + lane] = v[0];
      if (lane == 0) *(unsigned*)(lds + L_FIN + w * 4) = fold;
    }
    bar();
    float bpart = 0.f;
    if (w == 0) {
#pragma unroll
      for (int ww = 0; ww < 8; ++ww) bpart += fr[128 + ww * 16 + c];
      bpart = bfr(bfr(1.f - gate) * bfr(bpart));
      const int po = (rb * 128 + 16 * (lane & 7)) * 2048 + rs * 64;
      const unsigned po_old = *(const unsigned*)(lds + L_FIN + (lane & 7) * 4);
#pragma unroll 1
      for (unsigned spins = 0; spins < SPIN_CAP; ++spins) {
        PIN();
        if (__any((ld32<AUX_SC1>(rws, po, WS_Y) ^ po_old) & 1u)) break;
        __builtin_amdgcn_s_sleep(24);
      }
    }
    GAS u16* op = (GAS u16*)out + rb * 512 + rs * 16 + 4 * (c & 3) + (((c >> 2) & 2) | (((c >> 2) & 1) ^ 1));
    asm volatile("" : "+v"(bpart), "+v"(op));
    bar();
    y = u32x4{0u, 0u, 0u, 0xfeu};
    if (!isb) { POLL_Y(0); }
    const float lse = u2f((((y[0] >> 1) & 0x7fu) | (((y[1] >> 1) & 0x7fu) << 7) | (((y[2] >> 1) & 0x7fu) << 14) | (((y[3] >> 1) & 0x7fu) << 21)) << 4);
    float M = fmaxf(lse, dppf<ROR8>(lse)); M = fmaxf(M, dppf<ROR4>(M));
    const float e = isb ? 0.f : ex2(lse - M);
    float L = e + dppf<ROR8>(e); L += dppf<ROR4>(L);
    const float cf = e * __builtin_amdgcn_rcpf(L);
    float v[4];
#pragma unroll
    for (int j = 0; j < 4; ++j) v[j] = cf * u2f(y[j] & ~0xffu);
    SUM_ROWS(v);
    if (lane < 16) fr[w * 16 + lane] = v[0];
    bar();
    if (tid < 16) {
      const float a = ((fr[tid] + fr[16 + tid]) + (fr[32 + tid] + fr[48 + tid])) + ((fr[64 + tid] + fr[80 + tid]) + (fr[96 + tid] + fr[112 + tid]));
      const float o = bfr(gate * bfr(a)) + bpart;
      *op = (u16)pk_bf16(o, o);
    }
    if (rs == 0 && tid == 0) __hip_atomic_store((GAS u64*)(ws + WS_EPOCH + rb * 128), tagr, RLX_AGENT);
  }
    return;
  }
  {
    const int h = __builtin_amdgcn_readfirstlane(4 * x8 + idx / 6), b = __builtin_amdgcn_readfirstlane((idx % 6) / 3), sp = __builtin_amdgcn_readfirstlane(idx % 3);
    const int src = b * 128 + h * 4 + sp;
    const u32x4 sel = make_sel(c);
    const int tb = (b * 32 + h) * 512, t1 = tb + 512 * (sp + 1) / 3, t0 = tb + 512 * sp / 3 + w;
    const rsrc_t rk = mk_rsrc(Kc, t1 * 4096), rv = mk_rsrc(Vc, t1 * 4096), ro = mk_rsrc(wo, W_BYTES);
#define LDT(r, i_, j) ld128<AUX_NT>(r, lane * 16 + (j) * 1024, (t0 + 8 * (i_)) * 4096)
#define REQ(a, r, i_) { _Pragma("unroll") for (int j = 0; j < 4; ++j) a[j] = LDT(r, i_, j); SB(); }
#define REQ_REST(a, r) { _Pragma("unroll") for (int j = 0; j < 4; ++j) a[j] = ld128<AUX_NT>(r, lane * 16, (t0 - w + 168) * 4096 + (w + 8 * j) * 1024); SB(); }
#define ARRIVED(a) { _Pragma("unroll") for (int j = 0; j < 4; ++j) asm volatile("" : "+v"(a[j])); SB(); }
    u32x4 W[16], A[4], B[4], C[4], D4[4], E[4], F[4];
    const int wr = __builtin_amdgcn_readfirstlane((w + (idx % 6) * 4 / 3) & 7);
    unsigned x0 = x[64 * wr + lane], x1 = x[512 + 64 * wr + lane];
    LD_EPOCH();
    unsigned old = ld32<AUX_SC1_NT>(rws, lane * 4, WS_Y + src * 2048 + w * 256);
    const unsigned touch = ld32<AUX_NT>(ro, 0, h * 131072), touch2 = ld32<AUX_NT>(rv, 0, t0 * 4096);
    const unsigned touch3 = ld32<AUX_NT>(rk, 0, t0 * 4096);
    SB();
    {
      const rsrc_t rq = mk_rsrc(wq, W_BYTES);
#pragma unroll
      for (int i = 0; i < 16; ++i) W[i] = ld128<AUX_NT>(rq, g * (16 * 8192) + c * 16, (64 * wr + i) * 8192 + h * 256);
    }
    SB();
    asm volatile("" : "+v"(x0), "+v"(x1) : "v"(touch3));
    {
      f32x4 D = {0.f, 0.f, 0.f, 0.f};
      rowsum_refill<0>(W, x0 | (x1 << 16), sel, D, [&](auto T) {
        constexpr int j = T & 3;
        if constexpr (T < 4) A[j] = LDT(rk, 0, j); else if constexpr (T < 8) B[j] = LDT(rk, 1, j);
        else if constexpr (T < 12) C[j] = LDT(rk, 2, j); else D4[j] = LDT(rk, 3, j); });
#pragma unroll
      for (int r = 0; r < 4; ++r) red[(wr * 2 + (c >> 3)) * 128 + 32 * g + 8 * r + (c & 7)] = D[r];
    }
    SB();
    REQ(E, rk, 4); REQ_REST(F, rk);
    bar();
    u32x4 q;
    QREAD(q, b);
    float* scw = (float*)(lds + L_SC + w * 1536);
    float m = -INFINITY;
#define SCORE(a, i_, REST) { float xs[4]; _Pragma("unroll") for (int j = 0; j < 4; ++j) xs[j] = dot8(a[j], q); \
      float s = bfr(bfr(treduce<4>(xs, c)) * SCALE); \
      if (REST) s = (w + 8 * (c >> 2) < 4 * (t1 - t0 + w - 168)) ? s : -INFINITY; \
      m = fmaxf(m, s); scw[(i_) * 16 + (lane >> 2)] = s; asm volatile("" : "+v"(m)); SB(); }
    SCORE(A, 0, false); REQ(A, rk, 5);
    SCORE(B, 1, false);
    if (DEEP) REQ(B, rk, 6);
    SCORE(C, 2, false); SCORE(D4, 3, false); SCORE(E, 4, false); SCORE(F, 21, true);
    USE_EPOCH();
    u64 tag = b ? tag1 : tag0;
    asm volatile("" : "+s"(tag));
    asm volatile("" : "+v"(old) : "v"(touch), "v"(touch2));
    SB();
    const unsigned img = (unsigned)(uintptr_t)(LAS char*)(lds + L_RING) + w * 16384;
    const int dvo = (lane >> 3) * 1024 + 16 * ((lane & 7) ^ ((lane >> 4 & 1) << 1));
#define DMA8(k_, vo) { unsigned keep; \
      asm volatile("s_mov_b32 %0, m0\n\ts_mov_b32 m0, %1\n\ts_nop 4\n\tbuffer_load_dwordx4 %2, %3, %4 offen nt lds\n\ts_mov_b32 m0, %0" : "=&s"(keep) \
                   : "s"(__builtin_amdgcn_readfirstlane(img + (k_) * 1024)), "v"(vo), "s"(ro), "s"(__builtin_amdgcn_readfirstlane((h * 128 + 8 * (k_)) * 1024 + w * 128)) : "memory"); SB(); }
#pragma unroll 1
    for (int i = 5; i < 21; i += 2) {
      const rsrc_t rn = mk_rsrc(i < 19 ? Kc : Vc, t1 * 4096);
      if (DEEP) {
        SCORE(A, i, false); REQ(A, rn, i < 19 ? i + 2 : 0); DMA8(i - 5, dvo);
        SCORE(B, i + 1, false); REQ(B, rn, i < 19 ? i + 3 : 1); DMA8(i - 4, dvo ^ 64);
      } else {
        ARRIVED(A); REQ(B, rk, i + 1); DMA8(i - 5, dvo);
        SCORE(A, i, false);
        ARRIVED(B); REQ(A, rn, i < 19 ? i + 2 : 0); DMA8(i - 4, dvo ^ 64);
        SCORE(B, i + 1, false);
      }
    }
    float* ml = (float*)(lds + L_ML);
    m = xrow_max(row_max(m));
    float l = 0.f;
    {
      const float ms = (m == -INFINITY) ? 0.f : m;
      if (lane < 32) scw[352 + lane] = -INFINITY;
#pragma unroll
      for (int k = 0; k < 6; ++k) { const float p = ex2((scw[64 * k + lane] - ms) * LOG2E); l += p; scw[64 * k + lane] = u2f(hilo(p)); }
      l = xrow_sum(row_sum(l));
      if (lane == 0) { ml[w] = m; ml[8 + w] = l; }
    }
    f32x4 O = {0.f, 0.f, 0.f, 0.f};
#define PV(a, i_) { rowsum<4, 4>(a, f2u(scw[(i_) * 16 + (lane >> 2)]), sel, O); asm volatile("" : "+v"(O)); SB(); }
    if (DEEP) { PV(A, 0); REQ(A, rv, 2); } else { ARRIVED(A); REQ(B, rv, 1); }
    asm volatile("s_waitcnt vmcnt(4)" ::: "memory");
    {
      const int q4 = c >> 2, p4 = lane & 3, phi = (q4 >> 1) | ((g & 1) << 1);
#pragma unroll
      for (int s = 0; s < 4; ++s)
#pragma unroll
        for (int ct = 0; ct < 4; ++ct) {
          const char* a = lds + L_RING + w * 16384 + (32 * s + 8 * g + q4) * 128 + 16 * ((p4 >> 1) | ((ct ^ phi) << 1)) + 8 * (p4 & 1);
          const s16x4 lo = __builtin_amdgcn_ds_read_tr16_b64_v4i16((LAS s16x4*)a), hi = __builtin_amdgcn_ds_read_tr16_b64_v4i16((LAS s16x4*)(a + 512));
          W[4 * s + ct] = __builtin_bit_cast(u32x4, __builtin_shufflevector(lo, hi, 0, 1, 2, 3, 4, 5, 6, 7));
        }
    }
#pragma unroll
    for (int i = 0; i < 16; ++i) asm volatile("" : "+v"(W[i]));
    if (!DEEP) { PV(A, 0); ARRIVED(B); REQ(A, rv, 2); }
    PV(B, 1);
#pragma unroll 1
    for (int i = 2; i < 20; i += 2) {
      ARRIVED(A); REQ(B, rv, i + 1); PV(A, i);
      ARRIVED(B); REQ(A, rv, i + 2); PV(B, i + 1);
    }
    const GAS u64* go = (const GAS u64*)(ws + WS_GO) + h * 16 + b;
    u64 gov = __hip_atomic_load(go, RLX_AGENT);
    SB();
    REQ_REST(B, rv);
    bar();
    const float mj = ml[lane & 7], lj = ml[8 + (lane & 7)];
    float M = fmaxf(mj, dppf<XOR7>(mj)); M = fmaxf(M, dppf<XOR2>(M)); M = fmaxf(M, dppf<XOR1>(M));
    const float Ms = (M == -INFINITY) ? 0.f : M;
    float L = ex2((mj - Ms) * LOG2E) * lj; L += dppf<XOR7>(L); L += dppf<XOR2>(L); L += dppf<XOR1>(L);
    float fw = ex2((m - Ms) * LOG2E) * __builtin_amdgcn_rcpf(L);
    const unsigned code = f2u(Ms * LOG2E + __builtin_amdgcn_logf(L)) >> 4;
    unsigned low = (((code >> (7 * (lane & 3))) & 0x7fu) << 1) | ((old & 1u) ^ 1u);
    unsigned wa = L_P + (2 * w + (c >> 3)) * 272 + (32 * g + (c & 7)) * 2, sh = (c & 8) * 2;
    asm volatile("" : "+v"(fw), "+v"(low), "+v"(wa), "+v"(sh));
    SB();
    PV(A, 20); PV(B, 21);
#pragma unroll
    for (int r = 0; r < 4; ++r) *(u16*)(lds + wa + 16 * r) = (u16)(hilo((O[r] + dppf<ROR8>(O[r])) * fw) >> sh);
#pragma unroll 1
    for (unsigned spins = 0; uniform64(gov) != tag && spins < SPIN_CAP; ++spins) gov = __hip_atomic_load(go, RLX_AGENT);
    bar();
    f32x4 D[4];
    project(W, lds, g, c, D);
    float y4[4];
#pragma unroll
    for (int k = 0; k < 4; ++k) y4[k] = (D[k][0] + D[k][1]) + (D[k][2] + D[k][3]);
    const auto s0 = __builtin_amdgcn_permlane32_swap(f2u(y4[0]), f2u(y4[2]), false, false), s1 = __builtin_amdgcn_permlane32_swap(f2u(y4[1]), f2u(y4[3]), false, false);
    const auto s2 = __builtin_amdgcn_permlane16_swap(f2u(u2f(s0[0]) + u2f(s0[1])), f2u(u2f(s1[0]) + u2f(s1[1])), false, false);
    const unsigned yw = (f2u(u2f(s2[0]) + u2f(s2[1])) & ~0xffu) | low;
    const u32x4 yv = {yw, f2u(dppf<0x55>(u2f(yw))), f2u(dppf<0xAA>(u2f(yw))), f2u(dppf<0xFF>(u2f(yw)))};
    if ((lane & 3) == 0) __builtin_amdgcn_raw_buffer_store_b128(yv, rws, lane * 4, WS_Y + src * 2048 + w * 256, AUX_SC1);
  }
}

extern "C" void kernel_launch(void* const* d_in, const int* in_sizes, int n_in, void* d_out, int out_size, void* d_ws, size_t ws_size, hipStream_t stream) {
  fused_decode<<<256, 512, 0, stream>>>((const u16*)d_in[0], (const u16*)d_in[1], (const u16*)d_in[2], (const u16*)d_in[3], (const u16*)d_in[4],
                                        (const u16*)d_in[6], (const u16*)d_in[7], (const u16*)d_in[8], (const u16*)d_in[9], (const u16*)d_in[10],
                                        (const u16*)d_in[11], (u16*)d_out, (char*)d_ws);
}
```

```cpp
#include <hip/hip_runtime.h>
#include <stdint.h>
#include <type_traits>

typedef unsigned short u16;
typedef unsigned long long u64;
typedef unsigned u32x4 __attribute__((ext_vector_type(4)));
typedef unsigned u32x2 __attribute__((ext_vector_type(2)));
typedef float f32x4 __attribute__((ext_vector_type(4)));
typedef float f32x16 __attribute__((ext_vector_type(16)));
typedef __bf16 bf16x8 __attribute__((ext_vector_type(8)));
typedef __bf16 bf16x2 __attribute__((ext_vector_type(2)));
typedef short s16x4 __attribute__((ext_vector_type(4)));
typedef __amdgpu_buffer_rsrc_t rsrc_t;
#define GAS __attribute__((address_space(1)))
#define LAS __attribute__((address_space(3)))
#define DI __device__ __forceinline__
#define RLX_AGENT __ATOMIC_RELAXED, __HIP_MEMORY_SCOPE_AGENT

#define DEEP 0
constexpr int AUX_NT = 2, AUX_SC1 = 16, AUX_SC1_NT = 18;
constexpr int W_BYTES = 512 * 4096 * 2;
constexpr float SCALE = 0.08838834764831845f;
constexpr float LOG2E = 1.4426950408889634f;

constexpr int WS_EPOCH = 0;
constexpr int WS_F1 = 8192;
constexpr int WS_F2 = WS_F1 + 2048;
constexpr int WS_GO = 4096;
constexpr int WS_QA = 16384;
constexpr int WS_ML = WS_QA + 65536;
constexpr int WS_PB = WS_ML + 8192;
constexpr int WS_Y = WS_PB + 2 * 32 * 16 * 1024;
constexpr int WS_END = WS_Y + 2 * 128 * 2048;

constexpr int L_RING = 0;
constexpr int L_RED = 131072;
constexpr int L_P = L_RED + 8192;
constexpr int L_MX = L_P + 32 * 144;
constexpr int L_ML = L_MX + 512;
constexpr int L_FIN = L_ML + 512;
constexpr int L_SC = 147456;
constexpr int L_QA = 131072;
constexpr int L_END = 163840;

DI rsrc_t mk_rsrc(const void* p, unsigned bytes) { return __builtin_amdgcn_make_buffer_rsrc((void*)p, 0, bytes, 0x00020000); }
template <int AUX> DI u32x4 ld128(rsrc_t r, int voff, int soff) { return __builtin_amdgcn_raw_buffer_load_b128(r, voff, soff, AUX); }
template <int AUX> DI u32x2 ld64(rsrc_t r, int voff, int soff) { return __builtin_amdgcn_raw_buffer_load_b64(r, voff, soff, AUX); }
template <int AUX> DI unsigned ld32(rsrc_t r, int voff, int soff) { return __builtin_amdgcn_raw_buffer_load_b32(r, voff, soff, AUX); }
DI float u2f(unsigned u) { return __builtin_bit_cast(float, u); }
DI unsigned f2u(float f) { return __builtin_bit_cast(unsigned, f); }
DI unsigned pk_bf16(float lo, float hi) { bf16x2 r; r[0] = (__bf16)lo; r[1] = (__bf16)hi; return __builtin_bit_cast(unsigned, r); }
DI float bfr(float x) { return u2f(pk_bf16(x, x) << 16); }
DI unsigned hilo(float x) { const float h = bfr(x); return pk_bf16(h, x - h); }
DI float ex2(float x) { return __builtin_amdgcn_exp2f(x); }
DI float dot2(unsigned a, unsigned b, float c) { return __builtin_amdgcn_fdot2_f32_bf16(__builtin_bit_cast(bf16x2, a), __builtin_bit_cast(bf16x2, b), c, false); }
DI float dot8(const u32x4& a, const u32x4& b) { return dot2(a[3], b[3], dot2(a[2], b[2], dot2(a[1], b[1], dot2(a[0], b[0], 0.f)))); }
template <int CTRL> DI float dppf(float x) { return u2f(__builtin_amdgcn_mov_dpp(f2u(x), CTRL, 0xf, 0xf, true)); }
template <int T> DI unsigned bcast16(unsigned v) { return __builtin_amdgcn_mov_dpp(v, 0x150 + T, 0xf, 0xf, true); }
constexpr int XOR1 = 0xB1, XOR2 = 0x4E, XOR7 = 0x141, ROR8 = 0x128, ROR4 = 0x124;
DI float xrow_max(float x) {
  auto s = __builtin_amdgcn_permlane16_swap(f2u(x), f2u(x), false, false); x = fmaxf(u2f(s[0]), u2f(s[1]));
  auto t = __builtin_amdgcn_permlane32_swap(f2u(x), f2u(x), false, false); return fmaxf(u2f(t[0]), u2f(t[1]));
}
DI float xrow_sum(float x) {
  auto s = __builtin_amdgcn_permlane16_swap(f2u(x), f2u(x), false, false); x = u2f(s[0]) + u2f(s[1]);
  auto t = __builtin_amdgcn_permlane32_swap(f2u(x), f2u(x), false, false); return u2f(t[0]) + u2f(t[1]);
}
DI float row_max(float x) { x = fmaxf(x, dppf<ROR8>(x)); x = fmaxf(x, dppf<ROR4>(x)); x = fmaxf(x, dppf<XOR2>(x)); return fmaxf(x, dppf<XOR1>(x)); }
DI float row_sum(float x) { x += dppf<ROR8>(x); x += dppf<ROR4>(x); x += dppf<XOR2>(x); return x + dppf<XOR1>(x); }
DI f32x4 mfma16(u32x4 a, u32x4 b, f32x4 c) { return __builtin_amdgcn_mfma_f32_16x16x32_bf16(__builtin_bit_cast(bf16x8, a), __builtin_bit_cast(bf16x8, b), c, 0, 0, 0); }
DI f32x16 mfma32(u32x4 a, u32x4 b, f32x16 c) { return __builtin_amdgcn_mfma_f32_32x32x16_bf16(__builtin_bit_cast(bf16x8, a), __builtin_bit_cast(bf16x8, b), c, 0, 0, 0); }
DI u64 uniform64(u64 v) { return ((u64)(unsigned)__builtin_amdgcn_readfirstlane((unsigned)(v >> 32)) << 32) | (unsigned)__builtin_amdgcn_readfirstlane((unsigned)v); }
DI u64 next_tag(u64 e) { return e * 0x9E3779B97F4A7C15ull + 0x632BE59BD9B4E019ull; }
DI void bar() { __syncthreads(); }
#define DRAIN() asm volatile("s_waitcnt vmcnt(0)" ::: "memory")
#define PIN() asm volatile("" ::: "memory")
#define SB() __builtin_amdgcn_sched_barrier(0)
constexpr unsigned SPIN_CAP = 1u << 22;

template <int N, int STEP, int T = 0> DI void rowsum(const u32x4 (&W)[N], unsigned pair, const u32x4& sel, f32x4& D) {
  if constexpr (T < N) {
    const unsigned s = bcast16<T * STEP>(pair);
    const u32x4 B = {__builtin_amdgcn_perm(s, s, sel[0]), __builtin_amdgcn_perm(s, s, sel[1]), __builtin_amdgcn_perm(s, s, sel[2]), __builtin_amdgcn_perm(s, s, sel[3])};
    D = mfma16(W[T], B, D);
    rowsum<N, STEP, T + 1>(W, pair, sel, D);
  }
}
template <int T, class F> DI void rowsum_refill(const u32x4 (&W)[16], unsigned pair, const u32x4& sel, f32x4& D, F&& issue) {
  if constexpr (T < 16) {
    const unsigned s = bcast16<T>(pair);
    const u32x4 B = {__builtin_amdgcn_perm(s, s, sel[0]), __builtin_amdgcn_perm(s, s, sel[1]), __builtin_amdgcn_perm(s, s, sel[2]), __builtin_amdgcn_perm(s, s, sel[3])};
    D = mfma16(W[T], B, D);
    asm volatile("" : "+v"(D));
    __builtin_amdgcn_sched_barrier(0);
    issue(std::integral_constant<int, T>{});
    __builtin_amdgcn_sched_barrier(0);
    rowsum_refill<T + 1>(W, pair, sel, D, issue);
  }
}
DI int swz16(int row) { const int u = (row >> 2) & 3; return ((row & 3) << 2) | (((u ^ (u >> 1)) & 1) << 1) | (u >> 1); }
DI u32x4 make_sel(int c) {
  const int j = c & 7; const unsigned b = (c & 8) ? 0x0302u : 0x0100u;
  const unsigned v = (j & 1) ? ((b << 16) | 0x0c0cu) : (0x0c0c0000u | b);
  u32x4 s;
#pragma unroll
  for (int w = 0; w < 4; ++w) s[w] = (w == (j >> 1)) ? v : 0x0c0c0c0cu;
  return s;
}
template <int N> DI float treduce(float (&x)[N], int c) {
#define TR_STEP(CNT, CTRL, BIT) _Pragma("unroll") for (int i = 0; i < (CNT); ++i) { const float a = x[i] + dppf<CTRL>(x[i]), b = x[i + (CNT)] + dppf<CTRL>(x[i + (CNT)]); x[i] = (c & (BIT)) ? b : a; }
  TR_STEP(N / 2, ROR8, 8)
  TR_STEP(N / 4, XOR7, 4)
  if constexpr (N >= 8) { TR_STEP(N / 8, XOR2, 2) } else { x[0] += dppf<XOR2>(x[0]); }
  if constexpr (N >= 16) { TR_STEP(N / 16, XOR1, 1) } else { x[0] += dppf<XOR1>(x[0]); }
#undef TR_STEP
  return x[0];
}
DI void wait_flags32(const GAS u64* f, u64 tag, int lane) {
#pragma unroll 1
  for (unsigned spins = 0; spins < SPIN_CAP; ++spins) {
    if (__all(__hip_atomic_load(f + (lane & 31), RLX_AGENT) == tag)) break;
    __builtin_amdgcn_s_sleep(3);
  }
  __builtin_amdgcn_fence(__ATOMIC_ACQUIRE, "wavefront");
  asm volatile("" ::: "memory");
}
DI void transpose_w(const u32x2 (&R)[32], u32x4 (&F)[16]) {
#pragma unroll
  for (int s = 0; s < 4; ++s)
#pragma unroll
    for (int t = 0; t < 4; ++t)
#pragma unroll
      for (int jj = 0; jj < 4; ++jj) F[4 * s + t][jj] = __builtin_amdgcn_perm(R[8 * s + 2 * jj + 1][t >> 1], R[8 * s + 2 * jj][t >> 1], (t & 1) ? 0x07060302u : 0x05040100u);
}
DI void project(const u32x4 (&F)[16], const char* lds, int g, int c, f32x4 (&D)[4]) {
  u32x4 A[4];
#pragma unroll
  for (int s = 0; s < 4; ++s) A[s] = *(const u32x4*)(lds + L_P + c * 272 + 64 * s + 16 * g);
  __builtin_amdgcn_sched_barrier(0);
#pragma unroll
  for (int t = 0; t < 4; ++t) D[t] = f32x4{0.f, 0.f, 0.f, 0.f};
#pragma unroll
  for (int s = 0; s < 4; ++s)
#pragma unroll
    for (int t = 0; t < 4; ++t) D[t] = mfma16(A[s], F[4 * s + t], D[t]);
}
DI unsigned lsb4(const u32x4& v) { return (v[0] & 1u) | ((v[1] & 1u) << 1) | ((v[2] & 1u) << 2) | ((v[3] & 1u) << 3); }
DI unsigned mark(float v, unsigned old, int k) { return (f2u(v) & ~1u) | (((old >> k) & 1u) ^ 1u); }

__global__ void __launch_bounds__(512, 2)
fused_decode(const u16* __restrict__ x, const u16* __restrict__ Kc, const u16* __restrict__ Vc, const u16* __restrict__ wq, const u16* __restrict__ wo,
             const u16* __restrict__ Cc, const u16* __restrict__ w_q, const u16* __restrict__ w_uk, const u16* __restrict__ w_uv, const u16* __restrict__ w_o,
             const u16* __restrict__ gw, u16* __restrict__ out, char* ws) {
  __shared__ __attribute__((aligned(16))) char lds[L_END];
  const int bid = blockIdx.x, tid = threadIdx.x, lane = tid & 63, w = __builtin_amdgcn_readfirstlane(tid >> 6), g = lane >> 4, c = lane & 15;
  const int x8 = bid & 7, idx = bid >> 3;
  asm volatile("" :: "s"(x), "s"(Kc), "s"(Vc), "s"(wq), "s"(wo), "s"(ws));
  const rsrc_t rws = mk_rsrc(ws, WS_END);
  float* red = (float*)(lds + L_RED);
#define LD_EPOCH() u64 e0 = __hip_atomic_load((GAS u64*)(ws + WS_EPOCH), RLX_AGENT), e1 = __hip_atomic_load((GAS u64*)(ws + WS_EPOCH + 128), RLX_AGENT);
#define USE_EPOCH() asm volatile("" : "+v"(e0), "+v"(e1)); const u64 tag0 = next_tag(uniform64(e0)), tag1 = next_tag(uniform64(e1));
#define LD_ROWS(W, ptr) { const rsrc_t rq = mk_rsrc(ptr, W_BYTES); \
    _Pragma("unroll") for (int t = 0; t < 16; ++t) W[t] = ld128<AUX_NT>(rq, g * (16 * 8192) + c * 16, (64 * w + t) * 8192 + h * 256); }
#define LD_OUTW(R, ptr) { const rsrc_t ro = mk_rsrc(ptr, W_BYTES); \
    _Pragma("unroll") for (int i = 0; i < 32; ++i) R[i] = ld64<AUX_NT>(ro, g * 8192 + c * 8 + (i & 3) * 1024, (h * 128 + 32 * (i >> 3) + (i & 4)) * 1024 + w * 128); }
#define QREAD(q, bb) { f32x4 a0 = {0.f, 0.f, 0.f, 0.f}, a1 = a0; \
    _Pragma("unroll") for (int ww = 0; ww < 8; ++ww) { const f32x4* p = (const f32x4*)(red + (ww * 2 + (bb)) * 128 + 8 * c); a0 += p[0]; a1 += p[1]; } \
    q = u32x4{pk_bf16(a0[0], a0[1]), pk_bf16(a0[2], a0[3]), pk_bf16(a1[0], a1[1]), pk_bf16(a1[2], a1[3])}; }

  if (idx >= 24) {
  const int rb = idx >= 28, rs = __builtin_amdgcn_readfirstlane(4 * x8 + (idx & 3));
  const int fy_off = (rb * 128 + (tid >> 2)) * 2048 + rs * 64 + (tid & 3) * 16;
  unsigned fold; u64 tagr;
  float gate;
#define LD_GATE() const u32x4 xv = *(const u32x4*)(x + rb * 512 + lane * 8), gv = *(const u32x4*)(gw + lane * 8);
#define USE_GATE() { float s = 0.f; \
    _Pragma("unroll") for (int j = 0; j < 4; ++j) s += bfr(u2f(xv[j] << 16) * u2f(gv[j] << 16)) + bfr(u2f(xv[j] & 0xffff0000u) * u2f(gv[j] & 0xffff0000u)); \
    s = bfr(xrow_sum(row_sum(s))); gate = bfr(1.f / (1.f + __expf(-s))); }

  if (idx < 28) {
    const int h = rs;
    const u32x4 sel = make_sel(c);
    u32x4 W[16], U[16];
    unsigned x0 = x[tid], x1 = x[512 + tid];
    const unsigned tch = ld32<AUX_NT>(mk_rsrc(w_uk, W_BYTES), (lane & 1) << 21, h * 256) ^ ld32<AUX_NT>(mk_rsrc(w_uv, W_BYTES), (lane & 1) << 21, h * 256) ^ ld32<AUX_NT>(mk_rsrc(w_o, W_BYTES), 0, h * 131072);
    LD_ROWS(W, w_q);
    SB();
    {
      asm volatile("" : "+v"(x0), "+v"(x1) : "v"(tch));
      const unsigned xp = x0 | (x1 << 16);
      const rsrc_t ru = mk_rsrc(w_uk, W_BYTES);
      f32x4 D = {0.f, 0.f, 0.f, 0.f};
      rowsum_refill<0>(W, xp, sel, D, [&](auto T) { U[T] = ld128<AUX_NT>(ru, g * (16 * 8192) + c * 16, (64 * w + T) * 8192 + h * 256); });
#pragma unroll
      for (int r = 0; r < 4; ++r) red[(w * 2 + (c >> 3)) * 128 + 32 * g + 8 * r + (c & 7)] = D[r];
    }
    LD_EPOCH();
    const u32x4 fy = ld128<AUX_SC1_NT>(rws, fy_off, WS_Y);
    const u32x4 oy0 = ld128<AUX_SC1_NT>(rws, c * 16, WS_Y + (h * 4 + 3) * 2048 + w * 256), oy1 = ld128<AUX_SC1_NT>(rws, c * 16, WS_Y + (128 + h * 4 + 3) * 2048 + w * 256);
    LD_GATE();
    SB();
    bar();
#pragma unroll
    for (int bb = 0; bb < 2; ++bb) {
      u32x4 q; QREAD(q, bb);
      float xs[16];
#pragma unroll
      for (int t = 0; t < 16; ++t) xs[t] = dot8(U[t], q);
      const float qa = treduce<16>(xs, c);
      *(u16*)(lds + L_RING + bb * 1024 + tid * 2) = (u16)pk_bf16(qa, qa);
    }
    SB();
    USE_EPOCH(); USE_GATE();
    fold = lsb4(fy);
    unsigned old = lsb4(oy0) | (lsb4(oy1) << 4);
    asm volatile("" : "+v"(fold), "+v"(gate), "+v"(old));
    tagr = tag0;
    DRAIN();
    bar();
    if (w == 0) {
      __builtin_amdgcn_raw_buffer_store_b128(*(const u32x4*)(lds + L_RING + lane * 16), rws, lane * 16, WS_QA + h * 1024, AUX_SC1);
      __builtin_amdgcn_raw_buffer_store_b128(*(const u32x4*)(lds + L_RING + 1024 + lane * 16), rws, lane * 16, WS_QA + (32 + h) * 1024, AUX_SC1);
      DRAIN();
      if (lane < 8) __hip_atomic_store((GAS u64*)(ws + WS_F1) + lane * 32 + h, tag0, RLX_AGENT);
    }
    {
      u32x2 R[32];
      LD_OUTW(R, w_o);
      SB();
      transpose_w(R, U);
#pragma unroll
      for (int i = 0; i < 16; ++i) asm volatile("" : "+v"(U[i]));
      SB();
      u32x4 Rv[16];
      LD_ROWS(Rv, w_uv);
      SB();
#pragma unroll
      for (int s2 = 0; s2 < 2; ++s2)
#pragma unroll
        for (int t = 0; t < 8; ++t)
#pragma unroll
          for (int jj = 0; jj < 4; ++jj) W[8 * s2 + t][jj] = __builtin_amdgcn_perm(Rv[8 * s2 + 2 * jj + 1][t >> 1], Rv[8 * s2 + 2 * jj][t >> 1], (t & 1) ? 0x07060302u : 0x05040100u);
    }
#pragma unroll
    for (int i = 0; i < 16; ++i) asm volatile("" : "+v"(W[i]), "+v"(U[i]));
    if (w == 0) {
      wait_flags32((const GAS u64*)(ws + WS_F2) + x8 * 32, tag0, lane);
      if (lane < 2) __hip_atomic_store((GAS u64*)(ws + WS_GO) + h * 16 + lane, lane ? tag1 : tag0, RLX_AGENT);
    }
    bar();
    u32x4 T[4];
#pragma unroll
    for (int k = 0; k < 4; ++k) T[k] = ld128<AUX_SC1_NT>(rws, lane * 16, WS_PB + (((k >> 1) * 32 + h) * 16 + 2 * w + (k & 1)) * 1024);
    const u32x2 mlw = ld64<AUX_SC1_NT>(rws, (g & 1) * 4096 + c * 8, WS_ML + h * 128);
    const float ms = u2f(mlw[0]), M0 = row_max(ms), M = (M0 == -INFINITY) ? 0.f : M0;
    const float wt = ex2((ms - M) * LOG2E) * u2f(mlw[1]), cf = wt / row_sum(wt);
    float* cr = (float*)(lds + L_RING);
#pragma unroll
    for (int bb = 0; bb < 2; ++bb) {
      const float f0 = u2f(__builtin_amdgcn_readlane(f2u(cf), 16 * bb + 2 * w)), f1 = u2f(__builtin_amdgcn_readlane(f2u(cf), 16 * bb + 2 * w + 1));
      float a[8];
#pragma unroll
      for (int j = 0; j < 4; ++j) {
        a[2 * j] = f0 * u2f(T[2 * bb][j] << 16) + f1 * u2f(T[2 * bb + 1][j] << 16);
        a[2 * j + 1] = f0 * u2f(T[2 * bb][j] & 0xffff0000u) + f1 * u2f(T[2 * bb + 1][j] & 0xffff0000u);
      }
      *(f32x4*)(cr + (w * 2 + bb) * 512 + lane * 8) = f32x4{a[0], a[1], a[2], a[3]};
      *(f32x4*)(cr + (w * 2 + bb) * 512 + lane * 8 + 4) = f32x4{a[4], a[5], a[6], a[7]};
    }
    bar();
    {
      float c0 = 0.f, c1 = 0.f;
#pragma unroll
      for (int ww = 0; ww < 8; ++ww) { c0 += cr[ww * 1024 + tid]; c1 += cr[ww * 1024 + 512 + tid]; }
      const unsigned h0 = hilo(c0), h1 = hilo(c1);
      u16* ci = (u16*)(lds + L_RING + 32768);
      ci[tid] = (u16)h0; ci[520 + tid] = (u16)(h0 >> 16); ci[1040 + tid] = (u16)h1; ci[1560 + tid] = (u16)(h1 >> 16);
      PIN();
      const u32x4 a0 = *(const u32x4*)(ci + (c & 3) * 520 + 64 * w + 16 * g), a1 = *(const u32x4*)(ci + (c & 3) * 520 + 64 * w + 16 * g + 8);
      f32x4 D[8];
#pragma unroll
      for (int t = 0; t < 8; ++t) D[t] = mfma16(a0, W[t], f32x4{0.f, 0.f, 0.f, 0.f});
#pragma unroll
      for (int t = 0; t < 8; ++t) D[t] = mfma16(a1, W[8 + t], D[t]);
      if (g == 0) {
        f32x4* r0 = (f32x4*)(red + w * 256 + 8 * c);
        r0[0] = f32x4{D[0][0] + D[0][1], D[1][0] + D[1][1], D[2][0] + D[2][1], D[3][0] + D[3][1]};
        r0[1] = f32x4{D[4][0] + D[4][1], D[5][0] + D[5][1], D[6][0] + D[6][1], D[7][0] + D[7][1]};
        r0[32] = f32x4{D[0][2] + D[0][3], D[1][2] + D[1][3], D[2][2] + D[2][3], D[3][2] + D[3][3]};
        r0[33] = f32x4{D[4][2] + D[4][3], D[5][2] + D[5][3], D[6][2] + D[6][3], D[7][2] + D[7][3]};
      }
    }
    bar();
    if (tid < 256) {
      float v = 0.f;
#pragma unroll
      for (int ww = 0; ww < 8; ++ww) v += red[ww * 256 + tid];
      *(u16*)(lds + L_P + (tid >> 7) * 272 + (tid & 127) * 2) = (u16)pk_bf16(v, v);
    }
    bar();
    f32x4 D[4];
    project(U, lds, g, c & 1, D);
    if (g == 0) {
      __builtin_amdgcn_raw_buffer_store_b128(u32x4{mark(D[0][0], old, 0), mark(D[1][0], old, 1), mark(D[2][0], old, 2), mark(D[3][0], old, 3)}, rws, c * 16, WS_Y + (h * 4 + 3) * 2048 + w * 256, AUX_SC1);
      __builtin_amdgcn_raw_buffer_store_b128(u32x4{mark(D[0][1], old, 4), mark(D[1][1], old, 5), mark(D[2][1], old, 6), mark(D[3][1], old, 7)}, rws, c * 16, WS_Y + (128 + h * 4 + 3) * 2048 + w * 256, AUX_SC1);
    }
  } else {
    const int b = rs & 1, sp = rs >> 1;
    u32x4 W[16], S0[8], S1[8];
#define LDC(st, i) ld128<AUX_NT>(mk_rsrc(Cc, (st) < 8 ? (16u << 20) : 0u), lane * 16 + ((i) & 3) * 1024, (b * 8192 + sp * 512 + ((st) & 7) * 64 + 8 * w + ((i) & 4)) * 1024)
#define LDS_(S, st) _Pragma("unroll") for (int i = 0; i < 8; ++i) S[i] = LDC(st, i);
#define STC(slot, S) { int ln = lane; asm volatile("" : "+v"(ln)); char* base = lds + L_RING + (slot) * 65536 + (ln >> 4) * 16384 + 8 * w * 256; \
      _Pragma("unroll") for (int i = 0; i < 8; ++i) *(u32x4*)(base + i * 256 + 16 * ((ln & 15) ^ swz16(8 * w + i))) = S[i]; }
    LD_EPOCH();
    const u32x4 fy = ld128<AUX_SC1_NT>(rws, fy_off, WS_Y);
    LD_GATE();
    LDS_(S0, 0);
    LDS_(S1, 1);
    SB();
    USE_EPOCH(); USE_GATE();
    fold = lsb4(fy);
    asm volatile("" : "+v"(fold), "+v"(gate));
    tagr = tag1;
    STC(0, S0);
    SB();
    LDS_(S0, 2);
    SB();
    STC(1, S1);
    SB();
    LDS_(S1, 3);
    PIN();
    if (w == 0) wait_flags32((const GAS u64*)(ws + WS_F1) + x8 * 32, tag0, lane);
    bar();
#pragma unroll
    for (int k = 0; k < 4; ++k) *(u32x4*)(lds + L_QA + (4 * w + k) * 1024 + 16 * (lane ^ swz16(4 * w + k))) = ld128<AUX_SC1>(rws, lane * 16, WS_QA + (b * 32 + 4 * w + k) * 1024);
    bar();
    const int kt = w & 3, hh = w >> 2;
#pragma unroll
    for (int s = 0; s < 16; ++s) W[s] = *(const u32x4*)(lds + L_QA + (16 * hh + c) * 1024 + 16 * ((4 * s + g) ^ swz16(c)));
#pragma unroll
    for (int s = 0; s < 16; ++s) asm volatile("" : "+v"(W[s]));
    bar();
    float mq = -INFINITY, lq = 0.f, mp = -INFINITY;
    f32x16 A0, A1;
#pragma unroll
    for (int i = 0; i < 16; ++i) { A0[i] = 0.f; A1[i] = 0.f; }
    float* mx = (float*)(lds + L_MX);
#define STAGE(slot, REFILL) { const char* tile = lds + L_RING + (slot) * 65536; \
      int ln = lane; asm volatile("" : "+v"(ln)); \
      const int g = ln >> 4, c = ln & 15, swz = swz16(c), h5 = ln >> 5, hq = 16 * hh + c, hp = ln & 31; \
      const char* rowp = tile + (16 * kt + c) * 256; \
      f32x4 X0 = {0.f, 0.f, 0.f, 0.f}, X1 = X0; \
      { u32x4 fa[4], fb[4]; \
        _Pragma("unroll") for (int j = 0; j < 4; ++j) fa[j] = *(const u32x4*)(rowp + 16 * ((4 * j + g) ^ swz)); \
        _Pragma("unroll") for (int j = 0; j < 4; ++j) fb[j] = *(const u32x4*)(rowp + 16384 + 16 * ((4 * j + g) ^ swz)); \
        SB(); \
        X0 = mfma16(fa[0], W[0], X0); X1 = mfma16(fa[1], W[1], X1); X0 = mfma16(fa[2], W[2], X0); X1 = mfma16(fa[3], W[3], X1); \
        SB(); \
        _Pragma("unroll") for (int j = 0; j < 4; ++j) fa[j] = *(const u32x4*)(rowp + 32768 + 16 * ((4 * j + g) ^ swz)); \
        SB(); \
        X0 = mfma16(fb[0], W[4], X0); X1 = mfma16(fb[1], W[5], X1); X0 = mfma16(fb[2], W[6], X0); X1 = mfma16(fb[3], W[7], X1); \
        SB(); \
        _Pragma("unroll") for (int j = 0; j < 4; ++j) fb[j] = *(const u32x4*)(rowp + 49152 + 16 * ((4 * j + g) ^ swz)); \
        SB(); \
        X0 = mfma16(fa[0], W[8], X0); X1 = mfma16(fa[1], W[9], X1); X0 = mfma16(fa[2], W[10], X0); X1 = mfma16(fa[3], W[11], X1); \
        X0 = mfma16(fb[0], W[12], X0); X1 = mfma16(fb[1], W[13], X1); X0 = mfma16(fb[2], W[14], X0); X1 = mfma16(fb[3], W[15], X1); } \
      float sc[4]; \
      _Pragma("unroll") for (int r = 0; r < 4; ++r) sc[r] = bfr(bfr(X0[r] + X1[r]) * SCALE); \
      const float tm = xrow_max(fmaxf(fmaxf(sc[0], sc[1]), fmaxf(sc[2], sc[3]))); \
      if (g == 0) mx[kt * 32 + hq] = tm; \
      bar(); \
      { const float q0 = mx[hq], q1 = mx[32 + hq], q2 = mx[64 + hq], q3 = mx[96 + hq], p0 = mx[hp], p1 = mx[32 + hp], p2 = mx[64 + hp], p3 = mx[96 + hp]; \
        const float mn = fmaxf(fmaxf(mq, fmaxf(q0, q1)), fmaxf(q2, q3)), ms = (mn == -INFINITY) ? 0.f : mn; \
        float p[4]; \
        _Pragma("unroll") for (int r = 0; r < 4; ++r) p[r] = ex2((sc[r] - ms) * LOG2E); \
        *(u32x2*)(lds + L_P + hq * 144 + (16 * kt + 4 * g) * 2) = u32x2{pk_bf16(p[0], p[1]), pk_bf16(p[2], p[3])}; \
        lq = lq * ex2((mq - ms) * LOG2E) + ((p[0] + p[1]) + (p[2] + p[3])); \
        mq = mn; \
        const float mn2 = fmaxf(fmaxf(mp, fmaxf(p0, p1)), fmaxf(p2, p3)), ms2 = (mn2 == -INFINITY) ? 0.f : mn2; \
        const float al = ex2((mp - ms2) * LOG2E); mp = mn2; A0 *= al; A1 *= al; } \
      SB(); REFILL; SB(); \
      { const int blk = (ln >> 4) & 1, q4 = (ln & 15) >> 2, p4 = ln & 3; \
        const char* trp = tile + (w >> 1) * 16384 + (8 * h5 + q4) * 256 + 8 * (p4 & 1); \
        const int ch0 = 8 * (w & 1) + 2 * blk + (p4 >> 1), z0 = swz16(8 * h5 + q4), z1 = swz16(8 * h5 + 4 + q4); \
        s16x4 f[4][2][2]; \
        _Pragma("unroll") for (int ks = 0; ks < 4; ++ks) _Pragma("unroll") for (int ct = 0; ct < 2; ++ct) { \
          f[ks][ct][0] = __builtin_amdgcn_ds_read_tr16_b64_v4i16((LAS s16x4*)(trp + ks * 4096 + 16 * ((ch0 + 4 * ct) ^ z0))); \
          f[ks][ct][1] = __builtin_amdgcn_ds_read_tr16_b64_v4i16((LAS s16x4*)(trp + ks * 4096 + 1024 + 16 * ((ch0 + 4 * ct) ^ z1))); } \
        SB(); \
        bar(); \
        u32x4 P[4]; \
        _Pragma("unroll") for (int ks = 0; ks < 4; ++ks) P[ks] = *(const u32x4*)(lds + L_P + hp * 144 + (16 * ks + 8 * h5) * 2); \
        SB(); \
        _Pragma("unroll") for (int ks = 0; ks < 4; ++ks) { \
          A0 = mfma32(__builtin_bit_cast(u32x4, __builtin_shufflevector(f[ks][0][0], f[ks][0][1], 0, 1, 2, 3, 4, 5, 6, 7)), P[ks], A0); \
          A1 = mfma32(__builtin_bit_cast(u32x4, __builtin_shufflevector(f[ks][1][0], f[ks][1][1], 0, 1, 2, 3, 4, 5, 6, 7)), P[ks], A1); } \
        SB(); } }
    STAGE(0, ;);
#pragma unroll 1
    for (int it = 0; it < 3; ++it) {
      STAGE(1, { STC(0, S0); SB(); LDS_(S0, 2 * it + 4); });
      STAGE(0, { STC(1, S1); SB(); LDS_(S1, 2 * it + 5); });
    }
    STAGE(1, ;);
    const int h5 = lane >> 5, hq = 16 * hh + c, hp = lane & 31;
    float* lx = (float*)(lds + L_ML);
    lq = xrow_sum(lq);
    if (g == 0) lx[kt * 32 + hq] = lq;
    bar();
    const float lt = (lx[hp] + lx[32 + hp]) + (lx[64 + hp] + lx[96 + hp]);
    const float inv = 1.f / lt;
    char* img = lds + L_RING + hp * 1040 + (64 * w + 4 * h5) * 2;
#pragma unroll
    for (int k = 0; k < 4; ++k) {
      *(u32x2*)(img + 16 * k) = u32x2{pk_bf16(A0[4 * k] * inv, A0[4 * k + 1] * inv), pk_bf16(A0[4 * k + 2] * inv, A0[4 * k + 3] * inv)};
      *(u32x2*)(img + 64 + 16 * k) = u32x2{pk_bf16(A1[4 * k] * inv, A1[4 * k + 1] * inv), pk_bf16(A1[4 * k + 2] * inv, A1[4 * k + 3] * inv)};
    }
    if (tid < 32) __builtin_amdgcn_raw_buffer_store_b64(u32x2{f2u(mp), f2u(lt)}, rws, hp * 128, WS_ML + b * 4096 + sp * 8, AUX_SC1);
    bar();
#pragma unroll
    for (int k = 0; k < 4; ++k)
      __builtin_amdgcn_raw_buffer_store_b128(*(const u32x4*)(lds + L_RING + (4 * w + k) * 1040 + lane * 16), rws, lane * 16, WS_PB + ((b * 32 + 4 * w + k) * 16 + sp) * 1024, AUX_SC1_NT);
    DRAIN();
    bar();
    if (tid < 8) __hip_atomic_store((GAS u64*)(ws + WS_F2) + tid * 32 + rs, tag0, RLX_AGENT);
  }
  {
    const bool isb = (lane & 12) == 12;
    float* fr = (float*)(lds + L_RING);
    u32x4 y = {0u, 0u, 0u, 0u};
#define POLL_Y(SLP) _Pragma("unroll 1") for (unsigned spins = 0; spins < SPIN_CAP; ++spins) { \
      PIN(); y = ld128<AUX_SC1>(rws, fy_off, WS_Y); if ((lsb4(y) ^ fold) == 15u) break; __builtin_amdgcn_s_sleep(SLP); }
#define SUM_ROWS(v) { const float a0 = v[0] + dppf<ROR8>(v[0]), a1 = v[1] + dppf<ROR8>(v[1]), a2 = v[2] + dppf<ROR8>(v[2]), a3 = v[3] + dppf<ROR8>(v[3]); \
      const float b0 = (c & 8) ? a2 : a0, b1 = (c & 8) ? a3 : a1; \
      const float c0 = b0 + dppf<0x124>(b0), c1 = b1 + dppf<0x12C>(b1); \
      v[0] = xrow_sum((c & 4) ? c0 : c1); }
    if (rb) {
      if (lane == 0) *(unsigned*)(lds + L_FIN + w * 4) = fold;
      bar();
      if (w == 0) {
        const int po = (128 + 16 * (lane & 7)) * 2048 + rs * 64;
        const unsigned po_old = *(const unsigned*)(lds + L_FIN + (lane & 7) * 4);
#pragma unroll 1
        for (unsigned spins = 0; spins < SPIN_CAP; ++spins) {
          PIN();
          if (__any((ld32<AUX_SC1>(rws, po, WS_Y) ^ po_old) & 1u)) break;
          __builtin_amdgcn_s_sleep(24);
        }
      }
      bar();
    }
    GAS u16* op = (GAS u16*)out + rb * 512 + rs * 16 + 4 * (c & 3) + (((c >> 2) & 2) | (((c >> 2) & 1) ^ 1));
    float g1 = bfr(1.f - gate);
    asm volatile("" : "+v"(g1), "+v"(op));
    if (isb) { POLL_Y(0); }
    {
      float v[4];
#pragma unroll
      for (int j = 0; j < 4; ++j) v[j] = u2f(y[j] & ~0xffu);
      SUM_ROWS(v);
      if (lane < 16) fr[128 + w * 16 + lane] = v[0];
    }
    y = u32x4{0u, 0u, 0u, 0xfeu};
    if (!isb) { POLL_Y(0); }
    const float lse = u2f((((y[0] >> 1) & 0x7fu) | (((y[1] >> 1) & 0x7fu) << 7) | (((y[2] >> 1) & 0x7fu) << 14) | (((y[3] >> 1) & 0x7fu) << 21)) << 4);
    float M = fmaxf(lse, dppf<ROR8>(lse)); M = fmaxf(M, dppf<ROR4>(M));
    const float e = isb ? 0.f : ex2(lse - M);
    float L = e + dppf<ROR8>(e); L += dppf<ROR4>(L);
    const float cf = e * __builtin_amdgcn_rcpf(L);
    float v[4];
#pragma unroll
    for (int j = 0; j < 4; ++j) v[j] = cf * u2f(y[j] & ~0xffu);
    SUM_ROWS(v);
    if (lane < 16) fr[w * 16 + lane] = v[0];
    bar();
    if (tid < 16) {
      const float a = ((fr[tid] + fr[16 + tid]) + (fr[32 + tid] + fr[48 + tid])) + ((fr[64 + tid] + fr[80 + tid]) + (fr[96 + tid] + fr[112 + tid]));
      const float bb = ((fr[128 + tid] + fr[144 + tid]) + (fr[160 + tid] + fr[176 + tid])) + ((fr[192 + tid] + fr[208 + tid]) + (fr[224 + tid] + fr[240 + tid]));
      const float o = bfr(gate * bfr(a)) + bfr(g1 * bfr(bb));
      *op = (u16)pk_bf16(o, o);
    }
    if (rs == 0 && tid == 0) __hip_atomic_store((GAS u64*)(ws + WS_EPOCH + rb * 128), tagr, RLX_AGENT);
  }
    return;
  }
  {
    const int h = __builtin_amdgcn_readfirstlane(4 * x8 + idx / 6), b = __builtin_amdgcn_readfirstlane((idx % 6) / 3), sp = __builtin_amdgcn_readfirstlane(idx % 3);
    const int src = b * 128 + h * 4 + sp;
    const u32x4 sel = make_sel(c);
    const int tb = (b * 32 + h) * 512, t1 = tb + 512 * (sp + 1) / 3, t0 = tb + 512 * sp / 3 + w;
    const rsrc_t rk = mk_rsrc(Kc, t1 * 4096), rv = mk_rsrc(Vc, t1 * 4096), ro = mk_rsrc(wo, W_BYTES);
#define LDT(r, i_, j) ld128<AUX_NT>(r, lane * 16 + (j) * 1024, (t0 + 8 * (i_)) * 4096)
#define REQ(a, r, i_) { _Pragma("unroll") for (int j = 0; j < 4; ++j) a[j] = LDT(r, i_, j); SB(); }
#define REQ_REST(a, r) { _Pragma("unroll") for (int j = 0; j < 4; ++j) a[j] = ld128<AUX_NT>(r, lane * 16, (t0 - w + 168) * 4096 + (w + 8 * j) * 1024); SB(); }
#define ARRIVED(a) { _Pragma("unroll") for (int j = 0; j < 4; ++j) asm volatile("" : "+v"(a[j])); SB(); }
    u32x4 W[16], A[4], B[4], C[4], D4[4], E[4], F[4];
    const int wr = __builtin_amdgcn_readfirstlane((w + (idx % 6) * 4 / 3) & 7);
    unsigned x0 = x[64 * wr + lane], x1 = x[512 + 64 * wr + lane];
    LD_EPOCH();
    unsigned old = ld32<AUX_SC1_NT>(rws, lane * 4, WS_Y + src * 2048 + w * 256);
    const unsigned touch = ld32<AUX_NT>(ro, 0, h * 131072), touch2 = ld32<AUX_NT>(rv, 0, t0 * 4096);
    const unsigned touch3 = ld32<AUX_NT>(rk, 0, t0 * 4096);
    SB();
    {
      const rsrc_t rq = mk_rsrc(wq, W_BYTES);
#pragma unroll
      for (int i = 0; i < 16; ++i) W[i] = ld128<AUX_NT>(rq, g * (16 * 8192) + c * 16, (64 * wr + i) * 8192 + h * 256);
    }
    SB();
    asm volatile("" : "+v"(x0), "+v"(x1) : "v"(touch3));
    {
      f32x4 D = {0.f, 0.f, 0.f, 0.f};
      rowsum_refill<0>(W, x0 | (x1 << 16), sel, D, [&](auto T) {
        constexpr int j = T & 3;
        if constexpr (T < 4) A[j] = LDT(rk, 0, j); else if constexpr (T < 8) B[j] = LDT(rk, 1, j);
        else if constexpr (T < 12) C[j] = LDT(rk, 2, j); else D4[j] = LDT(rk, 3, j); });
#pragma unroll
      for (int r = 0; r < 4; ++r) red[(wr * 2 + (c >> 3)) * 128 + 32 * g + 8 * r + (c & 7)] = D[r];
    }
    SB();
    REQ(E, rk, 4); REQ_REST(F, rk);
    bar();
    u32x4 q;
    QREAD(q, b);
    float* scw = (float*)(lds + L_SC + w * 1536);
    float m = -INFINITY;
#define SCORE(a, i_, REST) { float xs[4]; _Pragma("unroll") for (int j = 0; j < 4; ++j) xs[j] = dot8(a[j], q); \
      float s = bfr(bfr(treduce<4>(xs, c)) * SCALE); \
      if (REST) s = (w + 8 * (c >> 2) < 4 * (t1 - t0 + w - 168)) ? s : -INFINITY; \
      m = fmaxf(m, s); scw[(i_) * 16 + (lane >> 2)] = s; asm volatile("" : "+v"(m)); SB(); }
    SCORE(A, 0, false); REQ(A, rk, 5);
    SCORE(B, 1, false);
    if (DEEP) REQ(B, rk, 6);
    SCORE(C, 2, false); SCORE(D4, 3, false); SCORE(E, 4, false); SCORE(F, 21, true);
    USE_EPOCH();
    u64 tag = b ? tag1 : tag0;
    asm volatile("" : "+s"(tag));
    asm volatile("" : "+v"(old) : "v"(touch), "v"(touch2));
    SB();
    const unsigned img = (unsigned)(uintptr_t)(LAS char*)(lds + L_RING) + w * 16384;
    const int dvo = (lane >> 3) * 1024 + 16 * ((lane & 7) ^ ((lane >> 4 & 1) << 1));
#define DMA8(k_, vo) { unsigned keep; \
      asm volatile("s_mov_b32 %0, m0\n\ts_mov_b32 m0, %1\n\ts_nop 4\n\tbuffer_load_dwordx4 %2, %3, %4 offen nt lds\n\ts_mov_b32 m0, %0" : "=&s"(keep) \
                   : "s"(__builtin_amdgcn_readfirstlane(img + (k_) * 1024)), "v"(vo), "s"(ro), "s"(__builtin_amdgcn_readfirstlane((h * 128 + 8 * (k_)) * 1024 + w * 128)) : "memory"); SB(); }
#pragma unroll 1
    for (int i = 5; i < 21; i += 2) {
      const rsrc_t rn = mk_rsrc(i < 19 ? Kc : Vc, t1 * 4096);
      if (DEEP) {
        SCORE(A, i, false); REQ(A, rn, i < 19 ? i + 2 : 0); DMA8(i - 5, dvo);
        SCORE(B, i + 1, false); REQ(B, rn, i < 19 ? i + 3 : 1); DMA8(i - 4, dvo ^ 64);
      } else {
        ARRIVED(A); REQ(B, rk, i + 1); DMA8(i - 5, dvo);
        SCORE(A, i, false);
        ARRIVED(B); REQ(A, rn, i < 19 ? i + 2 : 0); DMA8(i - 4, dvo ^ 64);
        SCORE(B, i + 1, false);
      }
    }
    float* ml = (float*)(lds + L_ML);
    m = xrow_max(row_max(m));
    float l = 0.f;
    {
      const float ms = (m == -INFINITY) ? 0.f : m;
      if (lane < 32) scw[352 + lane] = -INFINITY;
#pragma unroll
      for (int k = 0; k < 6; ++k) { const float p = ex2((scw[64 * k + lane] - ms) * LOG2E); l += p; scw[64 * k + lane] = u2f(hilo(p)); }
      l = xrow_sum(row_sum(l));
      if (lane == 0) { ml[w] = m; ml[8 + w] = l; }
    }
    f32x4 O = {0.f, 0.f, 0.f, 0.f};
#define PV(a, i_) { rowsum<4, 4>(a, f2u(scw[(i_) * 16 + (lane >> 2)]), sel, O); asm volatile("" : "+v"(O)); SB(); }
    if (DEEP) { PV(A, 0); REQ(A, rv, 2); } else { ARRIVED(A); REQ(B, rv, 1); }
    asm volatile("s_waitcnt vmcnt(4)" ::: "memory");
    {
      const int q4 = c >> 2, p4 = lane & 3, phi = (q4 >> 1) | ((g & 1) << 1);
#pragma unroll
      for (int s = 0; s < 4; ++s)
#pragma unroll
        for (int ct = 0; ct < 4; ++ct) {
          const char* a = lds + L_RING + w * 16384 + (32 * s + 8 * g + q4) * 128 + 16 * ((p4 >> 1) | ((ct ^ phi) << 1)) + 8 * (p4 & 1);
          const s16x4 lo = __builtin_amdgcn_ds_read_tr16_b64_v4i16((LAS s16x4*)a), hi = __builtin_amdgcn_ds_read_tr16_b64_v4i16((LAS s16x4*)(a + 512));
          W[4 * s + ct] = __builtin_bit_cast(u32x4, __builtin_shufflevector(lo, hi, 0, 1, 2, 3, 4, 5, 6, 7));
        }
    }
#pragma unroll
    for (int i = 0; i < 16; ++i) asm volatile("" : "+v"(W[i]));
    if (!DEEP) { PV(A, 0); ARRIVED(B); REQ(A, rv, 2); }
    PV(B, 1);
#pragma unroll 1
    for (int i = 2; i < 20; i += 2) {
      ARRIVED(A); REQ(B, rv, i + 1); PV(A, i);
      ARRIVED(B); REQ(A, rv, i + 2); PV(B, i + 1);
    }
    const GAS u64* go = (const GAS u64*)(ws + WS_GO) + h * 16 + b;
    u64 gov = __hip_atomic_load(go, RLX_AGENT);
    SB();
    REQ_REST(B, rv);
    bar();
    const float mj = ml[lane & 7], lj = ml[8 + (lane & 7)];
    float M = fmaxf(mj, dppf<XOR7>(mj)); M = fmaxf(M, dppf<XOR2>(M)); M = fmaxf(M, dppf<XOR1>(M));
    const float Ms = (M == -INFINITY) ? 0.f : M;
    float L = ex2((mj - Ms) * LOG2E) * lj; L += dppf<XOR7>(L); L += dppf<XOR2>(L); L += dppf<XOR1>(L);
    float fw = ex2((m - Ms) * LOG2E) * __builtin_amdgcn_rcpf(L);
    const unsigned code = f2u(Ms * LOG2E + __builtin_amdgcn_logf(L)) >> 4;
    unsigned low = (((code >> (7 * (lane & 3))) & 0x7fu) << 1) | ((old & 1u) ^ 1u);
    unsigned wa = L_P + (2 * w + (c >> 3)) * 272 + (32 * g + (c & 7)) * 2, sh = (c & 8) * 2;
    asm volatile("" : "+v"(fw), "+v"(low), "+v"(wa), "+v"(sh));
    SB();
    PV(A, 20); PV(B, 21);
#pragma unroll
    for (int r = 0; r < 4; ++r) *(u16*)(lds + wa + 16 * r) = (u16)(hilo((O[r] + dppf<ROR8>(O[r])) * fw) >> sh);
#pragma unroll 1
    for (unsigned spins = 0; uniform64(gov) != tag && spins < SPIN_CAP; ++spins) gov = __hip_atomic_load(go, RLX_AGENT);
    bar();
    f32x4 D[4];
    project(W, lds, g, c, D);
    float y4[4];
#pragma unroll
    for (int k = 0; k < 4; ++k) y4[k] = (D[k][0] + D[k][1]) + (D[k][2] + D[k][3]);
    const auto s0 = __builtin_amdgcn_permlane32_swap(f2u(y4[0]), f2u(y4[2]), false, false), s1 = __builtin_amdgcn_permlane32_swap(f2u(y4[1]), f2u(y4[3]), false, false);
    const auto s2 = __builtin_amdgcn_permlane16_swap(f2u(u2f(s0[0]) + u2f(s0[1])), f2u(u2f(s1[0]) + u2f(s1[1])), false, false);
    const unsigned yw = (f2u(u2f(s2[0]) + u2f(s2[1])) & ~0xffu) | low;
    const u32x4 yv = {yw, f2u(dppf<0x55>(u2f(yw))), f2u(dppf<0xAA>(u2f(yw))), f2u(dppf<0xFF>(u2f(yw)))};
    if ((lane & 3) == 0) __builtin_amdgcn_raw_buffer_store_b128(yv, rws, lane * 4, WS_Y + src * 2048 + w * 256, AUX_SC1);
  }
}

extern "C" void kernel_launch(void* const* d_in, const int* in_sizes, int n_in, void* d_out, int out_size, void* d_ws, size_t ws_size, hipStream_t stream) {
  fused_decode<<<256, 512, 0, stream>>>((const u16*)d_in[0], (const u16*)d_in[1], (const u16*)d_in[2], (const u16*)d_in[3], (const u16*)d_in[4],
                                        (const u16*)d_in[6], (const u16*)d_in[7], (const u16*)d_in[8], (const u16*)d_in[9], (const u16*)d_in[10],
                                        (const u16*)d_in[11], (u16*)d_out, (char*)d_ws);
}
```
